```python
import torch

def _softmax(x, axis=-1):
    return torch.softmax(x, dim=axis)
def _gelu(x):
    return torch.nn.functional.gelu(x, approximate="tanh")
def _silu(x):
    return torch.nn.functional.silu(x)
def _sigmoid(x):
    return torch.sigmoid(x)
def _layernorm(x, axis=-1, eps=1e-5):
    m = x.mean(dim=axis, keepdim=True)
    v = x.var(dim=axis, keepdim=True, unbiased=False)
    return (x - m) / torch.sqrt(v + eps)
def _scatter_add(tgt, idx, src, axis=0):
    return tgt.clone().index_add_(axis, idx, src)
def _segment_sum(src, seg, n):
    out = torch.zeros((n,) + src.shape[1:], dtype=src.dtype)
    return out.index_add_(0, seg, src)
def _pad(x, pw):
    flat = tuple(v for pair in reversed(pw) for v in pair)
    return torch.nn.functional.pad(x, flat)

kernel_name = 'parallel_sum__ssm_conv_s4__dense_activation_relu__prompt'

def setup_inputs():

    _w = (lambda a: (lambda t: t.to(torch.bfloat16) if t.is_floating_point() else t)(torch.as_tensor(a)))
    def _A_setup():
        B, L, D, N = 1, 2048, 512, 128
        A_re = -(lambda _x: torch.exp(torch.as_tensor(_x)))((lambda *s: torch.randn(*s, dtype=torch.bfloat16) * 3.2)(D, N))
        A_im = torch.pi * (lambda *s: torch.randn(*s, dtype=torch.bfloat16) * 0.1)(D, N)
        B_re = (lambda *s: torch.randn(*s, dtype=torch.bfloat16) * 0.4)(D, N)
        B_im = (lambda *s: torch.randn(*s, dtype=torch.bfloat16) * 0.4)(D, N)
        C_re = (lambda *s: torch.randn(*s, dtype=torch.bfloat16) * 0.4)(D, N)
        C_im = (lambda *s: torch.randn(*s, dtype=torch.bfloat16) * 0.4)(D, N)
        log_dt = (lambda *s: torch.randn(*s, dtype=torch.bfloat16) * 0.1)(D) * 0.5 - 4.0
        Dskip = (lambda *s: torch.randn(*s, dtype=torch.bfloat16) * 0.2)(D)
        return {
            "u": _w((lambda *s: torch.randn(*s, dtype=torch.bfloat16) * 0.05)(B, L, D)),
            "A_re": _w(A_re),
            "A_im": _w(A_im),
            "B_re": _w(B_re),
            "B_im": _w(B_im),
            "C_re": _w(C_re),
            "C_im": _w(C_im),
            "log_dt": _w(log_dt),
            "Dskip": _w(Dskip),
        }
    _A_in = _A_setup()

    def _B_setup():
        B, S, D, Dff = 1, 2048, 512, 1376
        return {
            "x": _w((lambda *s: torch.randn(*s, dtype=torch.bfloat16) * 0.05)(B, S, D)),
            "w1": _w((lambda *s: torch.randn(*s, dtype=torch.bfloat16) * 0.05)(D, Dff)),
            "w2": _w((lambda *s: torch.randn(*s, dtype=torch.bfloat16) * 0.05)(Dff, D)),
        }
    _B_in = _B_setup()

    return {
        "A_u": _A_in["u"],
        "A_A_re": _A_in["A_re"],
        "A_A_im": _A_in["A_im"],
        "A_B_re": _A_in["B_re"],
        "A_B_im": _A_in["B_im"],
        "A_C_re": _A_in["C_re"],
        "A_C_im": _A_in["C_im"],
        "A_log_dt": _A_in["log_dt"],
        "A_Dskip": _A_in["Dskip"],
        "B_x": _B_in["x"],
        "B_w1": _B_in["w1"],
        "B_w2": _B_in["w2"],
    }

def reference(A_u, A_A_re, A_A_im, A_B_re, A_B_im, A_C_re, A_C_im, A_log_dt, A_Dskip, B_x, B_w1, B_w2):
    def _A_ref(u, A_re, A_im, B_re, B_im, C_re, C_im, log_dt, Dskip):
        Bsz, L, D = u.shape
        dt = torch.exp(log_dt)[:, None]
        A = A_re + 1j * A_im
        Bc = B_re + 1j * B_im
        Cc = C_re + 1j * C_im
        logdA = dt * A
        dB = dt * Bc
        l = (lambda x, dt: x.to(dt))(torch.arange(L), A_re.dtype)
        P = torch.exp(logdA[:, :, None] * l[None, None, :])
        K = torch.einsum("dn,dnl,dn->dl", Cc, P, dB).real
        L2 = 2 * L
        Kp = _pad(K, ((0, 0), (0, L)))
        up = (lambda x, *d: x.permute(*d))(u, 0, 2, 1)
        up = _pad(up, ((0, 0), (0, 0), (0, L)))
        Kf = (lambda x, axes=(-2,-1): torch.fft.rfft2(x.float(), dim=axes))(Kp[:, None, :], axes=(-2, -1))
        Uf = (lambda x, axes=(-2,-1): torch.fft.rfft2(x.float(), dim=axes))(up[:, :, None, :], axes=(-2, -1))
        Yf = Uf * Kf[None, :, :, :]
        y = (lambda x, s, axes=(-2,-1): torch.fft.irfft2(x, s=s, dim=axes).to(torch.bfloat16))(Yf, (1, L2), axes=(-2, -1))[:, :, 0, :L]
        y = (lambda x, *d: x.permute(*d))(y, 0, 2, 1) + u * Dskip[None, None, :]
        return y

    def _B_ref(x, w1, w2):
        h = torch.einsum("bsd,df->bsf", x, w1)
        zero = torch.zeros_like(h)
        h = torch.maximum(h, zero)
        return torch.einsum("bsf,fd->bsd", h, w2)

    x = A_u
    xn = _layernorm(x, axis=-1)
    return x + _A_ref(xn, A_A_re, A_A_im, A_B_re, A_B_im, A_C_re, A_C_im, A_log_dt, A_Dskip) + _B_ref(xn, B_w1, B_w2)
```

```cpp
#include <hip/hip_runtime.h>
#include <hip/hip_bf16.h>
#include <stdint.h>

typedef __attribute__((ext_vector_type(8))) short s16x8;
typedef __attribute__((ext_vector_type(8))) _Float16 f16x8;
typedef __attribute__((ext_vector_type(4))) float f32x4;
typedef __attribute__((ext_vector_type(16))) float f32x16;
typedef __attribute__((ext_vector_type(4))) unsigned u32x4;
typedef __attribute__((ext_vector_type(2))) unsigned u32x2;
typedef __attribute__((ext_vector_type(2))) __bf16 bf16x2_t;
typedef __attribute__((ext_vector_type(2))) _Float16 f16x2_t;

constexpr int SL = 2048;
constexpr int DM = 512;
constexpr int NS = 128;
constexpr int DFF = 1376;
constexpr int HS = 1408;
constexpr int KRL = 2176;

constexpr size_t WS_H = 0;
constexpr size_t WS_XNT = WS_H + (size_t)SL * HS * 2;
constexpr size_t WS_KR = WS_XNT + (size_t)DM * SL * 2;
constexpr size_t WS_YT = WS_KR + (size_t)DM * KRL * 2;
constexpr size_t WS_ST = WS_YT + (size_t)DM * SL * 2;
constexpr size_t WS_KS = WS_ST + (size_t)SL * 8;

__device__ __forceinline__ float bf2f(unsigned h) { return __uint_as_float(h << 16); }
__device__ __forceinline__ float bflo(unsigned u) { return __uint_as_float(u << 16); }
__device__ __forceinline__ float bfhi(unsigned u) { return __uint_as_float(u & 0xffff0000u); }
__device__ __forceinline__ unsigned pkbf(float lo, float hi) {
  bf16x2_t r = {(__bf16)lo, (__bf16)hi};
  return __builtin_bit_cast(unsigned, r);
}
__device__ __forceinline__ float rbf(float x) { return bflo(pkbf(x, 0.f)); }
__device__ __forceinline__ unsigned pkh(float lo, float hi) {
  f16x2_t r = {(_Float16)lo, (_Float16)hi};
  return __builtin_bit_cast(unsigned, r);
}

struct Params {
  const unsigned short* x; const unsigned short* Are; const unsigned short* Aim;
  const unsigned short* Bre; const unsigned short* Bim; const unsigned short* Cre; const unsigned short* Cim;
  const unsigned short* logdt; const unsigned short* Dsk; const unsigned short* w1; const unsigned short* w2;
  unsigned short* out; char* ws;
};

__device__ __forceinline__ void gemm1_role(const Params& p, int b, char* lds) {
  const int tid = threadIdx.x, lane = tid & 63;
  const int w = __builtin_amdgcn_readfirstlane(tid >> 6);
  const int c = lane & 15, g = lane >> 4;
  const int xcd = b & 7, idx = b >> 3;
  const int rt = xcd * 4 + (idx >> 3), cg = idx & 7;
  const int row0 = rt * 64;
  const int col0 = cg * 192 + w * 64;
  const bool gw = (w < 3) && (col0 < DFF);
  constexpr int P = 6;
  u32x2 br[P][8];
  int bcol = col0 + 4 * c;
  if (bcol >= DFF) bcol = 0;
  const unsigned short* wb = p.w1 + (size_t)(8 * g) * DFF + bcol;
  if (gw) {
#pragma unroll
    for (int s = 0; s < P; ++s)
#pragma unroll
      for (int j = 0; j < 8; ++j)
        br[s][j] = *(const u32x2*)(wb + (size_t)(32 * s + j) * DFF);
  }
  {
    const unsigned short* xr = p.x + (size_t)(row0 + 16 * w + c) * DM + 8 * g;
    u32x4 xv[16];
#pragma unroll
    for (int s = 0; s < 16; ++s) xv[s] = *(const u32x4*)(xr + 32 * s);
    float sum = 0.f;
#pragma unroll
    for (int s = 0; s < 16; ++s)
#pragma unroll
      for (int i = 0; i < 4; ++i) sum += bflo(xv[s][i]) + bfhi(xv[s][i]);
    sum += __shfl_xor(sum, 16);
    sum += __shfl_xor(sum, 32);
    const float mean = sum * (1.f / DM);
    float sq = 0.f;
#pragma unroll
    for (int s = 0; s < 16; ++s)
#pragma unroll
      for (int i = 0; i < 4; ++i) {
        float a = bflo(xv[s][i]) - mean, bb = bfhi(xv[s][i]) - mean;
        sq += a * a + bb * bb;
      }
    sq += __shfl_xor(sq, 16);
    sq += __shfl_xor(sq, 32);
    const float mb = rbf(mean);
    const float vb = rbf(sq * (1.f / DM));
    const float den = rbf(sqrtf(rbf(vb + 1e-5f)));
    const float rc = 1.f / den;
    if (cg == 0 && g == 0) {
      float2 st = make_float2(mb, den);
      *(float2*)(p.ws + WS_ST + (size_t)(row0 + 16 * w + c) * 8) = st;
    }
#pragma unroll
    for (int s = 0; s < 16; ++s) {
      u32x4 o;
#pragma unroll
      for (int i = 0; i < 4; ++i) {
        unsigned d = pkbf(bflo(xv[s][i]) - mb, bfhi(xv[s][i]) - mb);
        float a = bflo(d), bb = bfhi(d);
        float qa = a * rc, qb = bb * rc;
        qa = fmaf(fmaf(-qa, den, a), rc, qa);
        qb = fmaf(fmaf(-qb, den, bb), rc, qb);
        o[i] = pkbf(qa, qb);
      }
      *(u32x4*)(lds + ((w * 16 + s) * 64 + lane) * 16) = o;
    }
  }
  __syncthreads();
  if (gw) {
    f32x4 acc[4][4];
#pragma unroll
    for (int i = 0; i < 4; ++i)
#pragma unroll
      for (int t = 0; t < 4; ++t) acc[i][t] = (f32x4){0.f, 0.f, 0.f, 0.f};
#pragma unroll
    for (int s = 0; s < 16; ++s) {
      u32x4 bf[4];
#pragma unroll
      for (int q = 0; q < 4; ++q) {
        bf[0][q] = __builtin_amdgcn_perm(br[s % P][2 * q + 1][0], br[s % P][2 * q][0], 0x05040100u);
        bf[1][q] = __builtin_amdgcn_perm(br[s % P][2 * q + 1][0], br[s % P][2 * q][0], 0x07060302u);
        bf[2][q] = __builtin_amdgcn_perm(br[s % P][2 * q + 1][1], br[s % P][2 * q][1], 0x05040100u);
        bf[3][q] = __builtin_amdgcn_perm(br[s % P][2 * q + 1][1], br[s % P][2 * q][1], 0x07060302u);
      }
      if (s + P < 16) {
#pragma unroll
        for (int j = 0; j < 8; ++j)
          br[s % P][j] = *(const u32x2*)(wb + (size_t)(32 * (s + P) + j) * DFF);
      }
#pragma unroll
      for (int mt = 0; mt < 4; ++mt) {
        u32x4 af = *(const u32x4*)(lds + ((mt * 16 + s) * 64 + lane) * 16);
#pragma unroll
        for (int t = 0; t < 4; ++t)
          acc[mt][t] = __builtin_amdgcn_mfma_f32_16x16x32_bf16(__builtin_bit_cast(s16x8, af),
                                                               __builtin_bit_cast(s16x8, bf[t]), acc[mt][t], 0, 0, 0);
      }
    }
    if (col0 + 4 * c < DFF) {
      unsigned short* hp = (unsigned short*)(p.ws + WS_H) + (size_t)(row0 + 4 * g) * HS + col0 + 4 * c;
#pragma unroll
      for (int mt = 0; mt < 4; ++mt)
#pragma unroll
        for (int r = 0; r < 4; ++r) {
          u32x2 o;
          o[0] = pkbf(fmaxf(acc[mt][0][r], 0.f), fmaxf(acc[mt][1][r], 0.f));
          o[1] = pkbf(fmaxf(acc[mt][2][r], 0.f), fmaxf(acc[mt][3][r], 0.f));
          *(u32x2*)(hp + (size_t)(16 * mt + r) * HS) = o;
        }
    }
  } else if (w == 3) {
    const int G = g, q = c >> 2, pp = c & 3;
    const unsigned lbase = (unsigned)(size_t)lds;
    unsigned short* xt = (unsigned short*)(p.ws + WS_XNT);
#pragma unroll
    for (int s2 = 0; s2 < 2; ++s2)
#pragma unroll
      for (int u = 0; u < 2; ++u) {
        const int s = 2 * cg + s2;
        const int gp = 2 * u + (pp >> 1);
        unsigned a0 = lbase + ((G * 16 + s) * 64 + q + 16 * gp) * 16 + 8 * (pp & 1);
        u32x2 v0, v1, v2, v3;
        asm volatile(
            "ds_read_b64_tr_b16 %0, %4\n\t"
            "ds_read_b64_tr_b16 %1, %4 offset:64\n\t"
            "ds_read_b64_tr_b16 %2, %4 offset:128\n\t"
            "ds_read_b64_tr_b16 %3, %4 offset:192\n\t"
            "s_waitcnt lgkmcnt(0)"
            : "=&v"(v0), "=&v"(v1), "=&v"(v2), "=&v"(v3)
            : "v"(a0)
            : "memory");
        u32x4 o0, o1;
        o0[0] = pkh(bflo(v0[0]), bfhi(v0[0])); o0[1] = pkh(bflo(v0[1]), bfhi(v0[1]));
        o0[2] = pkh(bflo(v1[0]), bfhi(v1[0])); o0[3] = pkh(bflo(v1[1]), bfhi(v1[1]));
        o1[0] = pkh(bflo(v2[0]), bfhi(v2[0])); o1[1] = pkh(bflo(v2[1]), bfhi(v2[1]));
        o1[2] = pkh(bflo(v3[0]), bfhi(v3[0])); o1[3] = pkh(bflo(v3[1]), bfhi(v3[1]));
        const int ch = 64 * cg + 32 * s2 + 16 * u + c;
        unsigned short* dst = xt + (size_t)ch * SL + row0 + 16 * G;
        *(u32x4*)dst = o0;
        *(u32x4*)(dst + 8) = o1;
      }
  }
}

__device__ __forceinline__ void bsplit(float a, float b, unsigned& hi, unsigned& lo) {
  hi = pkbf(a, b);
  lo = pkbf(a - bflo(hi), b - bfhi(hi));
}

__device__ __forceinline__ void kgen_role(const Params& p, int b, char* lds) {
  const int tid = threadIdx.x, lane = tid & 63;
  const int w = __builtin_amdgcn_readfirstlane(tid >> 6);
  const int chl = w >> 1, hf = w & 1;
  const int d = 2 * b + chl;
  f32x4* prm = (f32x4*)lds + w * 64;
  float* tab = (float*)(lds + 4096) + w * 656;
  {
    const int n = 64 * hf + lane;
    const size_t o = (size_t)d * NS + n;
    const float dt = rbf(expf(bf2f(p.logdt[d])));
    const float zr = dt * bf2f(p.Are[o]), zi = dt * bf2f(p.Aim[o]);
    const float br = dt * bf2f(p.Bre[o]), bi = dt * bf2f(p.Bim[o]);
    const float cr = bf2f(p.Cre[o]), ci = bf2f(p.Cim[o]);
    const float wr = cr * br - ci * bi, wi = cr * bi + ci * br;
    f32x4 q;
    q[0] = zr * 1.4426950408889634f;
    q[1] = zi * 0.15915494309189535f;
    q[2] = 0.5f * log2f(wr * wr + wi * wi);
    q[3] = atan2f(wi, wr) * 0.15915494309189535f;
    prm[lane] = q;
  }
  __builtin_amdgcn_wave_barrier();
  asm volatile("s_waitcnt lgkmcnt(0)" ::: "memory");
  const int rc = lane & 31, h = lane >> 5;
  int qxi;
  if (rc < 8) qxi = rc;
  else if (rc < 12) qxi = 256 + 2 * (rc - 8);
  else if (rc < 16) qxi = 512 + 256 * ((rc - 12) >> 1) + 4 * ((rc - 12) & 1);
  else if (rc < 20) qxi = 1024 + 256 * (rc - 16);
  else if (rc == 20) qxi = 2048;
  else qxi = 0;
  const float qx = (float)qxi, qy = (float)(8 * rc);
  f32x16 acc;
#pragma unroll
  for (int i = 0; i < 16; ++i) acc[i] = 0.f;
#pragma unroll 2
  for (int s = 0; s < 8; ++s) {
    u32x4 ah, al, bh, bl;
#pragma unroll
    for (int i = 0; i < 4; ++i) {
      const f32x4 q = prm[8 * s + 4 * h + i];
      float e = __builtin_amdgcn_exp2f(fmaf(q[0], qx, q[2]));
      float ph = __builtin_amdgcn_fractf(fmaf(q[1], qx, q[3]));
      float xr = e * __builtin_amdgcn_cosf(ph), xi = e * __builtin_amdgcn_sinf(ph);
      unsigned hi, lo;
      bsplit(xr, -xi, hi, lo);
      ah[i] = hi; al[i] = lo;
      e = __builtin_amdgcn_exp2f(q[0] * qy);
      ph = __builtin_amdgcn_fractf(q[1] * qy);
      float yr = e * __builtin_amdgcn_cosf(ph), yi = e * __builtin_amdgcn_sinf(ph);
      bsplit(yr, yi, hi, lo);
      bh[i] = hi; bl[i] = lo;
    }
    acc = __builtin_amdgcn_mfma_f32_32x32x16_bf16(__builtin_bit_cast(s16x8, ah), __builtin_bit_cast(s16x8, bh), acc, 0, 0, 0);
    acc = __builtin_amdgcn_mfma_f32_32x32x16_bf16(__builtin_bit_cast(s16x8, ah), __builtin_bit_cast(s16x8, bl), acc, 0, 0, 0);
    acc = __builtin_amdgcn_mfma_f32_32x32x16_bf16(__builtin_bit_cast(s16x8, al), __builtin_bit_cast(s16x8, bh), acc, 0, 0, 0);
  }
#pragma unroll
  for (int reg = 0; reg < 12; ++reg) {
    const int row = (reg & 3) + 8 * (reg >> 2) + 4 * h;
    const int j = rc;
    int ti = -1;
    if (row < 8) ti = row + 8 * j;
    else if (row < 12) ti = 256 + (row - 8) + 4 * j;
    else if (row < 16) ti = 384 + 64 * ((row - 12) >> 1) + ((row - 12) & 1) + 2 * j;
    else if (row < 20) ti = 512 + 32 * (row - 16) + j;
    else if (row == 20 && j == 0) ti = 640;
    if (ti >= 0) tab[ti] = acc[reg];
  }
  __syncthreads();
  const float* t0 = (const float*)(lds + 4096) + (2 * chl) * 656;
  const float* t1 = t0 + 656;
  const int t2 = tid & 127;
  float mx = 0.f;
  for (int i = lane; i < 641; i += 64) mx = fmaxf(mx, fabsf(t0[i] + t1[i]));
#pragma unroll
  for (int o = 32; o > 0; o >>= 1) mx = fmaxf(mx, __shfl_xor(mx, o));
  int ex = 0;
  if (mx > 0.f && mx < 3.0e38f) {
    int e;
    frexpf(mx, &e);
    ex = 14 - e;
    ex = ex > 120 ? 120 : (ex < -120 ? -120 : ex);
  }
  const float sc = ldexpf(1.f, ex);
  if (t2 == 0) ((float*)(p.ws + WS_KS))[d] = ldexpf(1.f, -ex);
  unsigned short* kr = (unsigned short*)(p.ws + WS_KR) + (size_t)d * KRL;
  for (int ck = t2; ck < KRL / 8; ck += 128) {
    float v[8];
#pragma unroll
    for (int e = 0; e < 8; ++e) {
      const int lag = 2048 - (8 * ck + e);
      float val = 0.f;
      if (lag >= 0 && lag < 2048) {
        const int q = (int)rbf((float)lag);
        int ti;
        if (q < 256) ti = q;
        else if (q < 512) ti = 256 + ((q - 256) >> 1);
        else if (q < 1024) ti = 384 + ((q - 512) >> 2);
        else ti = 512 + ((q - 1024) >> 3);
        val = (t0[ti] + t1[ti]) * sc;
      }
      v[e] = val;
    }
    u32x4 o;
    o[0] = pkh(v[0], v[1]); o[1] = pkh(v[2], v[3]); o[2] = pkh(v[4], v[5]); o[3] = pkh(v[6], v[7]);
    *(u32x4*)(kr + 8 * ck) = o;
  }
}

__global__ void __launch_bounds__(256, 2) k_phase1(Params p) {
  __shared__ __attribute__((aligned(16))) char lds[65536];
  const int b = blockIdx.x;
  if (b < 256) gemm1_role(p, b, lds);
  else kgen_role(p, b - 256, lds);
}

__global__ void __launch_bounds__(256, 2) k_conv(Params p) {
  constexpr int CH_LDS = 4352 + 9216;
  __shared__ __attribute__((aligned(16))) char lds[2 * CH_LDS + 2 * 8192];
  const int tid = threadIdx.x, lane = tid & 63;
  const int w = __builtin_amdgcn_readfirstlane(tid >> 6);
  const int chl = w >> 1, hf = w & 1;
  const int d = 2 * blockIdx.x + chl;
  char* kl = lds + chl * CH_LDS;
  char* ul = kl + 4352;
  {
    const int t2 = tid & 127;
    const u32x4* ksrc = (const u32x4*)(p.ws + WS_KR + (size_t)d * KRL * 2);
    const u32x4* usrc = (const u32x4*)(p.ws + WS_XNT + (size_t)d * SL * 2);
    u32x4 kv0 = ksrc[t2], kv1 = ksrc[t2 + 128], kv2;
    if (t2 < 16) kv2 = ksrc[t2 + 256];
    u32x4 uv0 = usrc[t2], uv1 = usrc[t2 + 128];
    const u32x4 z = {0u, 0u, 0u, 0u};
    for (int i = t2; i < 288; i += 128) *(u32x4*)(ul + i * 16) = z;
    *(u32x4*)(kl + t2 * 16) = kv0;
    *(u32x4*)(kl + (t2 + 128) * 16) = kv1;
    if (t2 < 16) *(u32x4*)(kl + (t2 + 256) * 16) = kv2;
    *(u32x4*)(ul + (16 + (t2 >> 4)) * 288 + (t2 & 15) * 16) = uv0;
    *(u32x4*)(ul + (24 + (t2 >> 4)) * 288 + (t2 & 15) * 16) = uv1;
  }
  __syncthreads();
  const int a = lane & 15, g = lane >> 4;
  f32x4 acc[8];
#pragma unroll
  for (int r = 0; r < 8; ++r) acc[r] = (f32x4){0.f, 0.f, 0.f, 0.f};
  const char* ab = kl + 2 * (2144 - 8 * (a - g)) - 64 * 32 * hf;
  const char* bb = ul + 288 * (a + 16 - 8 * hf) + 16 * g;
#pragma unroll
  for (int i = 0; i < 32; ++i) {
    const int co = 12 - 4 * i;
    const int ro = (co >= 0) ? 0 : -((-co + 15) / 16);
    const int cc = co - 16 * ro;
    u32x4 w0 = *(const u32x4*)(ab - 64 * i - 16);
    u32x4 w1 = *(const u32x4*)(ab - 64 * i);
    u32x4 bv = *(const u32x4*)(bb + 288 * ro + 16 * cc);
    unsigned wd[8] = {w0[0], w0[1], w0[2], w0[3], w1[0], w1[1], w1[2], w1[3]};
    unsigned od[7];
#pragma unroll
    for (int k = 0; k < 7; ++k) od[k] = __builtin_amdgcn_alignbit(wd[k + 1], wd[k], 16);
    const f16x8 bfr = __builtin_bit_cast(f16x8, bv);
#pragma unroll
    for (int r = 0; r < 8; ++r) {
      u32x4 af;
      if ((r & 1) == 0) {
        const int s0 = (8 - r) >> 1;
        af = (u32x4){wd[s0], wd[s0 + 1], wd[s0 + 2], wd[s0 + 3]};
      } else {
        const int s0 = (7 - r) >> 1;
        af = (u32x4){od[s0], od[s0 + 1], od[s0 + 2], od[s0 + 3]};
      }
      acc[r] = __builtin_amdgcn_mfma_f32_16x16x32_f16(__builtin_bit_cast(f16x8, af), bfr, acc[r], 0, 0, 0);
    }
  }
  f32x4* red = (f32x4*)(lds + 2 * CH_LDS + chl * 8192);
  if (hf == 1) {
#pragma unroll
    for (int r = 0; r < 8; ++r) red[r * 64 + lane] = acc[r];
  }
  __syncthreads();
  if (hf == 0) {
    const float isc = ((const float*)(p.ws + WS_KS))[d];
#pragma unroll
    for (int r = 0; r < 8; ++r) acc[r] = (acc[r] + red[r * 64 + lane]) * isc;
    unsigned short* yt = (unsigned short*)(p.ws + WS_YT) + (size_t)d * SL + 128 * a + 32 * g;
#pragma unroll
    for (int reg = 0; reg < 4; ++reg) {
      u32x4 o;
      o[0] = pkbf(acc[0][reg], acc[1][reg]);
      o[1] = pkbf(acc[2][reg], acc[3][reg]);
      o[2] = pkbf(acc[4][reg], acc[5][reg]);
      o[3] = pkbf(acc[6][reg], acc[7][reg]);
      *(u32x4*)(yt + 8 * reg) = o;
    }
  }
}

__global__ void __launch_bounds__(256, 2) k_gemm2(Params p) {
  __shared__ __attribute__((aligned(16))) char lds[4 * 3 * 4096];
  const int tid = threadIdx.x, lane = tid & 63;
  const int w = __builtin_amdgcn_readfirstlane(tid >> 6);
  const int c = lane & 15, g = lane >> 4;
  const int b = blockIdx.x;
  const int xcd = b & 7, idx = b >> 3;
  const int rt = xcd * 4 + (idx >> 3), ct = idx & 7;
  const int row0 = rt * 64, col0 = ct * 64;
  const int s0 = 11 * w;
  const int ns = (w == 3) ? 10 : 11;
  const unsigned short* hb = (const unsigned short*)(p.ws + WS_H) + (size_t)(row0 + c) * HS + 32 * s0 + 8 * g;
  const unsigned short* wb = p.w2 + (size_t)(32 * s0 + 8 * g) * DM + col0 + 4 * c;
  constexpr int P = 4;
  u32x4 ar[P][4];
  u32x2 br[P][8];
#pragma unroll
  for (int s = 0; s < P; ++s) {
#pragma unroll
    for (int mt = 0; mt < 4; ++mt) ar[s][mt] = *(const u32x4*)(hb + (size_t)(16 * mt) * HS + 32 * s);
#pragma unroll
    for (int j = 0; j < 8; ++j) br[s][j] = *(const u32x2*)(wb + (size_t)(32 * s + j) * DM);
  }
  f32x4 acc[4][4];
#pragma unroll
  for (int i = 0; i < 4; ++i)
#pragma unroll
    for (int t = 0; t < 4; ++t) acc[i][t] = (f32x4){0.f, 0.f, 0.f, 0.f};
#pragma unroll
  for (int s = 0; s < 11; ++s) {
    if (s < ns) {
      u32x4 bf[4], af[4];
#pragma unroll
      for (int q = 0; q < 4; ++q) {
        bf[0][q] = __builtin_amdgcn_perm(br[s % P][2 * q + 1][0], br[s % P][2 * q][0], 0x05040100u);
        bf[1][q] = __builtin_amdgcn_perm(br[s % P][2 * q + 1][0], br[s % P][2 * q][0], 0x07060302u);
        bf[2][q] = __builtin_amdgcn_perm(br[s % P][2 * q + 1][1], br[s % P][2 * q][1], 0x05040100u);
        bf[3][q] = __builtin_amdgcn_perm(br[s % P][2 * q + 1][1], br[s % P][2 * q][1], 0x07060302u);
        af[q] = ar[s % P][q];
      }
      if (s + P < 11 && s + P < ns) {
#pragma unroll
        for (int mt = 0; mt < 4; ++mt) ar[s % P][mt] = *(const u32x4*)(hb + (size_t)(16 * mt) * HS + 32 * (s + P));
#pragma unroll
        for (int j = 0; j < 8; ++j) br[s % P][j] = *(const u32x2*)(wb + (size_t)(32 * (s + P) + j) * DM);
      }
#pragma unroll
      for (int mt = 0; mt < 4; ++mt)
#pragma unroll
        for (int t = 0; t < 4; ++t)
          acc[mt][t] = __builtin_amdgcn_mfma_f32_16x16x32_bf16(__builtin_bit_cast(s16x8, af[mt]),
                                                               __builtin_bit_cast(s16x8, bf[t]), acc[mt][t], 0, 0, 0);
    }
  }
  const int erow = row0 + 16 * w + 4 * g;
  u32x2 xv[4], yv[4];
  float2 st[4];
#pragma unroll
  for (int r = 0; r < 4; ++r) {
    xv[r] = *(const u32x2*)(p.x + (size_t)(erow + r) * DM + col0 + 4 * c);
    st[r] = *(const float2*)(p.ws + WS_ST + (size_t)(erow + r) * 8);
  }
#pragma unroll
  for (int t = 0; t < 4; ++t)
    yv[t] = *(const u32x2*)((const unsigned short*)(p.ws + WS_YT) + (size_t)(col0 + 4 * c + t) * SL + erow);
  const u32x2 dk = *(const u32x2*)(p.Dsk + col0 + 4 * c);
  f32x4* red = (f32x4*)lds;
#pragma unroll
  for (int mt = 0; mt < 4; ++mt) {
    if (mt != w) {
      const int slot = (w > mt) ? (w - 1) : w;
#pragma unroll
      for (int t = 0; t < 4; ++t) red[((mt * 3 + slot) * 4 + t) * 64 + lane] = acc[mt][t];
    }
  }
  __syncthreads();
  f32x4 f[4];
#pragma unroll
  for (int t = 0; t < 4; ++t) {
    f32x4 v = (w == 0) ? acc[0][t] : (w == 1) ? acc[1][t] : (w == 2) ? acc[2][t] : acc[3][t];
#pragma unroll
    for (int sl = 0; sl < 3; ++sl) v += red[((w * 3 + sl) * 4 + t) * 64 + lane];
    f[t] = v;
  }
  float dkf[4] = {bflo(dk[0]), bfhi(dk[0]), bflo(dk[1]), bfhi(dk[1])};
#pragma unroll
  for (int r = 0; r < 4; ++r) {
    float xf[4] = {bflo(xv[r][0]), bfhi(xv[r][0]), bflo(xv[r][1]), bfhi(xv[r][1])};
    float o[4];
    const float mb = st[r].x, den = st[r].y, rc = 1.f / den;
#pragma unroll
    for (int t = 0; t < 4; ++t) {
      const float a = rbf(xf[t] - mb);
      float q = a * rc;
      q = fmaf(fmaf(-q, den, a), rc, q);
      const float xn = rbf(q);
      const float y = (r & 1) ? bfhi(yv[t][r >> 1]) : bflo(yv[t][r >> 1]);
      const float ya = rbf(y + rbf(xn * dkf[t]));
      const float t1 = rbf(xf[t] + ya);
      o[t] = t1 + rbf(f[t][r]);
    }
    u32x2 ov;
    ov[0] = pkbf(o[0], o[1]);
    ov[1] = pkbf(o[2], o[3]);
    *(u32x2*)(p.out + (size_t)(erow + r) * DM + col0 + 4 * c) = ov;
  }
}

extern "C" void kernel_launch(void* const* d_in, const int* in_sizes, int n_in,
                              void* d_out, int out_size, void* d_ws, size_t ws_size,
                              hipStream_t stream) {
  Params p;
  p.x = (const unsigned short*)d_in[0];
  p.Are = (const unsigned short*)d_in[1];
  p.Aim = (const unsigned short*)d_in[2];
  p.Bre = (const unsigned short*)d_in[3];
  p.Bim = (const unsigned short*)d_in[4];
  p.Cre = (const unsigned short*)d_in[5];
  p.Cim = (const unsigned short*)d_in[6];
  p.logdt = (const unsigned short*)d_in[7];
  p.Dsk = (const unsigned short*)d_in[8];
  p.w1 = (const unsigned short*)d_in[10];
  p.w2 = (const unsigned short*)d_in[11];
  p.out = (unsigned short*)d_out;
  p.ws = (char*)d_ws;
  k_phase1<<<512, 256, 0, stream>>>(p);
  k_conv<<<256, 256, 0, stream>>>(p);
  k_gemm2<<<256, 256, 0, stream>>>(p);
}
```

```cpp
#include <hip/hip_runtime.h>
#include <hip/hip_bf16.h>
#include <stdint.h>

typedef __attribute__((ext_vector_type(8))) short s16x8;
typedef __attribute__((ext_vector_type(4))) short s16x4;
typedef __attribute__((ext_vector_type(8))) _Float16 f16x8;
typedef __attribute__((ext_vector_type(4))) float f32x4;
typedef __attribute__((ext_vector_type(2))) float f32x2;
typedef __attribute__((ext_vector_type(16))) float f32x16;
typedef __attribute__((ext_vector_type(4))) unsigned u32x4;
typedef __attribute__((ext_vector_type(2))) unsigned u32x2;
typedef __attribute__((ext_vector_type(2))) __bf16 bf16x2_t;
typedef __attribute__((ext_vector_type(2))) _Float16 f16x2_t;
#define LAS __attribute__((address_space(3)))

constexpr int SL = 2048;
constexpr int DM = 512;
constexpr int NS = 128;
constexpr int DFF = 1376;
constexpr int KRL = 2080;

constexpr size_t WS_H = 0;
constexpr size_t WS_XNT = WS_H + (size_t)128 * 43 * 1024;
constexpr size_t WS_KR = WS_XNT + (size_t)DM * SL * 2;
constexpr size_t WS_YT = WS_KR + (size_t)DM * KRL * 2;
constexpr size_t WS_ST = WS_YT + (size_t)DM * SL * 2;
constexpr size_t WS_KS = WS_ST + (size_t)SL * 8;
constexpr size_t WS_FL = WS_KS + 4096;
constexpr size_t WS_W2F = WS_FL + 32768;

#if defined(__HIP_DEVICE_COMPILE__)
#pragma clang attribute push(__attribute__((target("no-packed-fp32-ops"))), apply_to = function)
#endif
__device__ __forceinline__ float u2f(unsigned u) { return __builtin_bit_cast(float, u); }
__device__ __forceinline__ unsigned f2u(float f) { return __builtin_bit_cast(unsigned, f); }
__device__ __forceinline__ float shx(float v, int lane, int m) {
  return u2f((unsigned)__builtin_amdgcn_ds_bpermute((lane ^ m) << 2, (int)f2u(v)));
}
__device__ __forceinline__ void wg_sync() {
  __builtin_amdgcn_fence(__ATOMIC_RELEASE, "workgroup");
  __builtin_amdgcn_s_barrier();
  __builtin_amdgcn_fence(__ATOMIC_ACQUIRE, "workgroup");
}
__device__ __forceinline__ float exp_acc(float x) {
  const float L2E_HI = 1.44269502162933349609375f, L2E_LO = 1.925963033500011e-8f;
  const float hi = x * L2E_HI;
  const float lo = __builtin_fmaf(x, L2E_LO, __builtin_fmaf(x, L2E_HI, -hi));
  const float e = __builtin_amdgcn_exp2f(hi);
  return __builtin_fmaf(e, lo * 0.6931471805599453f, e);
}
#define TIDX ((int)__builtin_amdgcn_workitem_id_x())
#define BIDX ((int)__builtin_amdgcn_workgroup_id_x())
__device__ __forceinline__ float bf2f(unsigned h) { return u2f(h << 16); }
__device__ __forceinline__ float bflo(unsigned u) { return u2f(u << 16); }
__device__ __forceinline__ float bfhi(unsigned u) { return u2f(u & 0xffff0000u); }
__device__ __forceinline__ f32x2 bfup(unsigned u) { return (f32x2){bflo(u), bfhi(u)}; }
__device__ __forceinline__ unsigned pkbf(float lo, float hi) {
  bf16x2_t r = {(__bf16)lo, (__bf16)hi};
  return __builtin_bit_cast(unsigned, r);
}
__device__ __forceinline__ unsigned pkbf2(f32x2 v) { return pkbf(v[0], v[1]); }
__device__ __forceinline__ float rbf(float x) { return bflo(pkbf(x, 0.f)); }
__device__ __forceinline__ unsigned pkh(float lo, float hi) {
  f16x2_t r = {(_Float16)lo, (_Float16)hi};
  return __builtin_bit_cast(unsigned, r);
}

#define STAMP_DECL
#define STAMP_AT(kid, i) do {} while (0)
#define STAMP_FLUSH(kid) do {} while (0)
#define SB() __builtin_amdgcn_sched_barrier(0)
#define WAITV(n) asm volatile("s_waitcnt vmcnt(%0)" ::"n"(n) : "memory")
#define WAITL() asm volatile("s_waitcnt lgkmcnt(0)" ::: "memory")

struct Params {
  const unsigned short* x; const unsigned short* Are; const unsigned short* Aim;
  const unsigned short* Bre; const unsigned short* Bim; const unsigned short* Cre; const unsigned short* Cim;
  const unsigned short* logdt; const unsigned short* Dsk; const unsigned short* w1; const unsigned short* w2;
  unsigned short* out; char* ws;
};

__device__ __forceinline__ unsigned lds_arrive(LAS unsigned* cnt, int lane) {
  WAITL();
  unsigned old = 0;
  if (lane == 0) old = __hip_atomic_fetch_add(cnt, 1u, __ATOMIC_RELAXED, __HIP_MEMORY_SCOPE_WORKGROUP);
  return __builtin_amdgcn_readfirstlane(old);
}
__device__ __forceinline__ void lds_wait(LAS unsigned* cnt, unsigned target) {
  while (__builtin_amdgcn_readfirstlane(__hip_atomic_load(cnt, __ATOMIC_RELAXED, __HIP_MEMORY_SCOPE_WORKGROUP)) < target)
    __builtin_amdgcn_s_sleep(1);
  asm volatile("" ::: "memory");
}
__device__ __forceinline__ void glds16(const void* gsrc, unsigned lds_dst) {
  unsigned keep;
  asm volatile("s_mov_b32 %0, m0\n\ts_mov_b32 m0, %2\n\ts_nop 0\n\tglobal_load_lds_dwordx4 %1, off\n\ts_mov_b32 m0, %0"
               : "=&s"(keep) : "v"(gsrc), "s"(lds_dst) : "memory");
}

__device__ __forceinline__ void glds16s(const void* sbase, unsigned voff, unsigned lds_dst) {
  unsigned keep;
  asm volatile("s_mov_b32 %0, m0\n\ts_mov_b32 m0, %3\n\ts_nop 0\n\tglobal_load_lds_dwordx4 %1, %2\n\ts_mov_b32 m0, %0"
               : "=&s"(keep) : "v"(voff), "s"(sbase), "s"(lds_dst) : "memory");
}

typedef __attribute__((ext_vector_type(3))) unsigned u32x3;
typedef u32x3 __attribute__((aligned(4))) u32x3_a4;
constexpr int PB = 3;
constexpr int KT_LO = 21 * 272;
constexpr int KT_SZ = 2 * 21 * 272 + 64;
constexpr int L1_A = 0;
constexpr int L1_R = L1_A + 65536;
constexpr int L1_ST = L1_R + 4 * 12288;
constexpr int L1_CS = L1_ST + 1024;
constexpr int L1_C = L1_CS + 1536;
constexpr int L1_T = L1_C + 64;
constexpr int L1_SIZE = L1_T + 4 * KT_SZ;

__device__ __forceinline__ void bsplit(f32x2 v, unsigned& hi, unsigned& lo) {
  hi = pkbf2(v);
  asm("" : "+v"(hi));
  lo = pkbf(v[0] - bflo(hi), v[1] - bfhi(hi));
}
template <int CTRL> __device__ __forceinline__ float dpp_max(float v) {
  const int t = __builtin_amdgcn_update_dpp(0, (int)f2u(v), CTRL, 0xf, 0xf, true);
  return __builtin_fmaxf(v, u2f((unsigned)t));
}
__device__ __forceinline__ float wave_max_nonneg(float v) {
  v = dpp_max<0x140>(dpp_max<0x141>(dpp_max<0x4E>(dpp_max<0xB1>(v))));
  const float a = u2f((unsigned)__builtin_amdgcn_readlane((int)f2u(v), 0));
  const float b = u2f((unsigned)__builtin_amdgcn_readlane((int)f2u(v), 16));
  const float c = u2f((unsigned)__builtin_amdgcn_readlane((int)f2u(v), 32));
  const float d = u2f((unsigned)__builtin_amdgcn_readlane((int)f2u(v), 48));
  return __builtin_fmaxf(__builtin_fmaxf(a, b), __builtin_fmaxf(c, d));
}

__global__ void __launch_bounds__(512, 2) k_phase1(Params p) {
  __shared__ __attribute__((aligned(16))) char lds[L1_SIZE];
  STAMP_DECL
  const int tid = TIDX, lane = tid & 63;
  const int w = __builtin_amdgcn_readfirstlane(tid >> 6);
  const int b = BIDX;
  LAS unsigned* cnt = (LAS unsigned*)(LAS char*)(lds + L1_C);
  if (tid < 16) cnt[tid] = 0u;
  wg_sync();
  STAMP_AT(0, 0);
  const int xcd = b & 7, idx = b >> 3;
  const int rt = xcd * 4 + (idx >> 3), cg = idx & 7;
  const int row0 = rt * 64;
  const int c = lane & 15, g = lane >> 4;
  const unsigned gl16 = (unsigned)((g ^ c) * 16);
  const char* arow = lds + L1_A + c * 1024;
  if (w >= 4) {
    const int gi = w - 4, ch = gi & 1, kh = gi >> 1;
    const int col0 = (cg == 7) ? 1280 : cg * 192 + 96 * ch;
    const bool gw = (cg < 7) || (ch == 0);
    const int rot = 2 * (rt & 3);
    const unsigned lbase = (unsigned)(size_t)(LAS char*)lds;
    {
      const char* xr = (const char*)p.x + (size_t)(row0 + 16 * gi) * 1024;
#pragma unroll
      for (int i = 0; i < 16; ++i) glds16s(xr + i * 1024, (unsigned)((lane ^ i) * 16), lbase + L1_A + (16 * gi + i) * 1024);
    }
    if (gw) {
      unsigned ones = 0x3f803f80u;
      asm volatile("" : "+v"(ones));
      const char* bsrc = (const char*)p.w1 + (size_t)(256 * kh + 8 * g) * (DFF * 2) + (col0 + 6 * c) * 2;
      u32x3 bq[PB][8];
      auto issueB = [&](int s) {
        const char* q = bsrc + (size_t)((s + rot) & 7) * (32 * DFF * 2);
#pragma unroll
        for (int j = 0; j < 8; ++j) bq[s % PB][j] = *(const u32x3_a4*)(q + j * (DFF * 2));
      };
#pragma unroll
      for (int s = 0; s < PB; ++s) issueB(s);
      SB();
      WAITV(8 * PB);
      lds_arrive(cnt + 0, lane);
      STAMP_AT(0, 4);
      f32x4 acc[4][6];
      float cs[6] = {0.f, 0.f, 0.f, 0.f, 0.f, 0.f};
#pragma unroll
      for (int i = 0; i < 4; ++i)
#pragma unroll
        for (int t = 0; t < 6; ++t) acc[i][t] = (f32x4){0.f, 0.f, 0.f, 0.f};
      lds_wait(cnt + 0, 4);
      STAMP_AT(0, 1);
#pragma unroll
      for (int s = 0; s < 8; ++s) {
        s16x8 af[4];
        {
          const char* ap = arow + (gl16 ^ (unsigned)((8 * kh + ((s + rot) & 7)) * 64));
#pragma unroll
          for (int mt = 0; mt < 4; ++mt) af[mt] = *(const s16x8*)(ap + mt * 16384);
        }
        u32x4 F[6];
#pragma unroll
        for (int t = 0; t < 6; ++t)
#pragma unroll
          for (int q = 0; q < 4; ++q)
            F[t][q] = __builtin_amdgcn_perm(bq[s % PB][2 * q + 1][t >> 1], bq[s % PB][2 * q][t >> 1], (t & 1) ? 0x07060302u : 0x05040100u);
        SB();
        if (s + PB < 8) issueB(s + PB);
        SB();
#pragma unroll
        for (int t = 0; t < 6; ++t) {
#pragma unroll
          for (int mt = 0; mt < 4; ++mt)
            acc[mt][t] = __builtin_amdgcn_mfma_f32_16x16x32_bf16(__builtin_bit_cast(s16x8, F[t]), af[mt], acc[mt][t], 0, 0, 0);
#pragma unroll
          for (int e = 0; e < 4; ++e) {
            const unsigned be = F[t][e];
            cs[t] = __builtin_amdgcn_fdot2_f32_bf16(__builtin_bit_cast(bf16x2_t, be), __builtin_bit_cast(bf16x2_t, ones), cs[t], false);
          }
        }
        asm volatile("" : "+v"(cs[0]), "+v"(cs[1]), "+v"(cs[2]), "+v"(cs[3]), "+v"(cs[4]), "+v"(cs[5]));
        SB();
      }
      STAMP_AT(0, 2);
      f32x4* rx = (f32x4*)(lds + L1_R + gi * 12288);
      f32x4 mine[2][6];
      if (kh == 0) {
#pragma unroll
        for (int m = 0; m < 2; ++m)
#pragma unroll
          for (int t = 0; t < 6; ++t) { rx[(m * 6 + t) * 64 + lane] = acc[2 + m][t]; mine[m][t] = acc[m][t]; }
      } else {
#pragma unroll
        for (int m = 0; m < 2; ++m)
#pragma unroll
          for (int t = 0; t < 6; ++t) { rx[(m * 6 + t) * 64 + lane] = acc[m][t]; mine[m][t] = acc[2 + m][t]; }
      }
      float* csl = (float*)(lds + L1_CS);
#pragma unroll
      for (int t = 0; t < 6; ++t) {
        float v = cs[t];
        v += shx(v, lane, 16);
        v += shx(v, lane, 32);
        if (g == 0) csl[gi * 96 + 6 * c + t] = v;
      }
      lds_arrive(cnt + 5 + ch, lane);
      lds_wait(cnt + 5 + ch, 2);
      lds_wait(cnt + 4, 4);
      const f32x4* ro = (const f32x4*)(lds + L1_R + (gi ^ 2) * 12288);
      float cv[24];
#pragma unroll
      for (int k = 0; k < 6; ++k) {
        const f32x4 a = *(const f32x4*)(csl + ch * 96 + 24 * g + 4 * k);
        const f32x4 bb = *(const f32x4*)(csl + (ch + 2) * 96 + 24 * g + 4 * k);
#pragma unroll
        for (int e = 0; e < 4; ++e) cv[4 * k + e] = a[e] + bb[e];
      }
#pragma unroll
      for (int m = 0; m < 2; ++m) {
        const int mt = 2 * kh + m;
        const f32x4 st = *(const f32x4*)(lds + L1_ST + (16 * mt + c) * 16);
        float hv[24];
#pragma unroll
        for (int t = 0; t < 6; ++t) {
          const f32x4 o = ro[(m * 6 + t) * 64 + lane];
#pragma unroll
          for (int r = 0; r < 4; ++r)
            hv[6 * r + t] = __builtin_fmaxf((mine[m][t][r] + o[r] - st[0] * cv[6 * r + t]) * st[2], 0.f);
        }
#pragma unroll
        for (int ck = 0; ck < 3; ++ck) {
          const int F0 = col0 + 24 * g + 8 * ck;
          u32x4 o;
#pragma unroll
          for (int e = 0; e < 4; ++e) o[e] = pkbf(hv[8 * ck + 2 * e], hv[8 * ck + 2 * e + 1]);
          *(u32x4*)(p.ws + WS_H + ((size_t)(rt * 4 + mt) * 43 + (F0 >> 5)) * 1024 + (c + 16 * ((F0 >> 3) & 3)) * 16) = o;
        }
      }
      STAMP_AT(0, 3);
    } else {
      WAITV(0);
      lds_arrive(cnt + 0, lane);
    }
  } else {
    const int kw = w, chl = kw >> 1, nh = kw & 1;
    const int d = 2 * b + chl;
    char* treg = lds + L1_T + kw * KT_SZ;
    float a2, fr;
    f32x2 W;
    {
      const size_t o = (size_t)d * NS + 64 * nh + lane;
      const float lg = bf2f(p.logdt[d]);
      const float are = bf2f(p.Are[o]), aim = bf2f(p.Aim[o]);
      const float bre = bf2f(p.Bre[o]), bim = bf2f(p.Bim[o]);
      const float cr = bf2f(p.Cre[o]), ci = bf2f(p.Cim[o]);
      const float dt = rbf(exp_acc(lg));
      const float br = dt * bre, bi = dt * bim;
      a2 = (dt * are) * 1.4426950408889634f;
      fr = (dt * aim) * 0.15915494309189535f;
      W = (f32x2){cr * br - ci * bi, -(cr * bi + ci * br)};
    }
    STAMP_AT(0, 1);
    auto cexpq = [&](float q) -> f32x2 {
      const float e = __builtin_amdgcn_exp2f(a2 * q);
      const float ph = __builtin_amdgcn_fractf(fr * q);
      return (f32x2){__builtin_amdgcn_cosf(ph) * e, __builtin_amdgcn_sinf(ph) * e};
    };
    auto cmul = [](f32x2 a, f32x2 bb) -> f32x2 {
      return (f32x2){__builtin_fmaf(a[0], bb[0], -(a[1] * bb[1])), __builtin_fmaf(a[0], bb[1], a[1] * bb[0])};
    };
    auto put = [&](int row, f32x2 v) {
      unsigned hi, lo;
      bsplit(v, hi, lo);
      *(unsigned*)(treg + row * 272 + lane * 4) = hi;
      *(unsigned*)(treg + KT_LO + row * 272 + lane * 4) = lo;
    };
    const f32x2 E8 = cexpq(8.f);
    f32x2 ys[32];
    {
      const f32x2 E1 = cexpq(1.f), E256 = cexpq(256.f);
      ys[0] = (f32x2){1.f, 0.f}; ys[8] = cexpq(64.f); ys[16] = cexpq(128.f); ys[24] = cexpq(192.f);
      const f32x2 C1 = {E1[0], -E1[1]}, CL = {E256[0], -E256[1]};
      const f32x2 C2 = cmul(C1, C1), C4 = cmul(C2, C2);
      f32x2 xs[21];
      xs[0] = W;
      xs[8] = cmul(W, CL);
      xs[1] = cmul(xs[0], C1);  xs[12] = cmul(xs[8], CL);  xs[9] = cmul(xs[8], C2);
      xs[2] = cmul(xs[1], C1);  xs[14] = cmul(xs[12], CL); xs[10] = cmul(xs[9], C2); xs[13] = cmul(xs[12], C4);
      xs[3] = cmul(xs[2], C1);  xs[16] = cmul(xs[14], CL); xs[11] = cmul(xs[10], C2); xs[15] = cmul(xs[14], C4);
      xs[4] = cmul(xs[0], C4);  xs[17] = cmul(xs[16], CL);
      xs[5] = cmul(xs[1], C4);  xs[18] = cmul(xs[17], CL);
      xs[6] = cmul(xs[2], C4);  xs[19] = cmul(xs[18], CL);
      xs[7] = cmul(xs[3], C4);  xs[20] = cmul(xs[19], CL);
      SB();
#pragma unroll
      for (int i = 0; i < 21; ++i) put(i, xs[i]);
    }
    SB();
#pragma unroll
    for (int i = 1; i < 8; ++i)
#pragma unroll
      for (int k = 0; k < 4; ++k) ys[8 * k + i] = cmul(ys[8 * k + i - 1], E8);
    STAMP_AT(0, 5);
    const int fr16 = c, fg = g;
    const char* frd = treg + fr16 * 272 + 16 * fg;
    s16x8 ah[2][4], al[2][4];
#pragma unroll
    for (int rb = 0; rb < 2; ++rb)
#pragma unroll
      for (int s = 0; s < 4; ++s) {
        ah[rb][s] = *(const s16x8*)(frd + rb * 16 * 272 + 64 * s);
        al[rb][s] = *(const s16x8*)(frd + KT_LO + rb * 16 * 272 + 64 * s);
      }
    f32x4 acc[2][2];
#pragma unroll
    for (int cb = 0; cb < 2; ++cb) {
      WAITL();
      STAMP_AT(0, 6 + cb);
#pragma unroll
      for (int i = 0; i < 16; ++i) put(i, ys[16 * cb + i]);
#pragma unroll
      for (int rb = 0; rb < 2; ++rb) acc[rb][cb] = (f32x4){0.f, 0.f, 0.f, 0.f};
#pragma unroll
      for (int s = 0; s < 4; ++s) {
        const s16x8 bh = *(const s16x8*)(frd + 64 * s);
        const s16x8 bl = *(const s16x8*)(frd + KT_LO + 64 * s);
#pragma unroll
        for (int rb = 0; rb < 2; ++rb) {
          acc[rb][cb] = __builtin_amdgcn_mfma_f32_16x16x32_bf16(ah[rb][s], bh, acc[rb][cb], 0, 0, 0);
          acc[rb][cb] = __builtin_amdgcn_mfma_f32_16x16x32_bf16(ah[rb][s], bl, acc[rb][cb], 0, 0, 0);
          acc[rb][cb] = __builtin_amdgcn_mfma_f32_16x16x32_bf16(al[rb][s], bh, acc[rb][cb], 0, 0, 0);
        }
      }
      if (cb == 0) {
        lds_wait(cnt + 0, 4);
        const s16x8 onesf = {0x3f80, 0x3f80, 0x3f80, 0x3f80, 0x3f80, 0x3f80, 0x3f80, 0x3f80};
        f32x4 as0 = {0.f, 0.f, 0.f, 0.f}, aq0 = as0, as1 = as0, aq1 = as0;
        const char* ab2 = arow + kw * 16384;
#pragma unroll
        for (int s = 0; s < 16; s += 2) {
          const s16x8 x0 = *(const s16x8*)(ab2 + (gl16 ^ (unsigned)(s * 64)));
          const s16x8 x1 = *(const s16x8*)(ab2 + (gl16 ^ (unsigned)(s * 64 + 64)));
          as0 = __builtin_amdgcn_mfma_f32_16x16x32_bf16(onesf, x0, as0, 0, 0, 0);
          aq0 = __builtin_amdgcn_mfma_f32_16x16x32_bf16(x0, x0, aq0, 0, 0, 0);
          as1 = __builtin_amdgcn_mfma_f32_16x16x32_bf16(onesf, x1, as1, 0, 0, 0);
          aq1 = __builtin_amdgcn_mfma_f32_16x16x32_bf16(x1, x1, aq1, 0, 0, 0);
        }
        const int r3 = c & 3;
        const f32x4 aq = aq0 + aq1;
        const float q = (r3 == 0) ? aq[0] : (r3 == 1) ? aq[1] : (r3 == 2) ? aq[2] : aq[3];
        const float mean = (as0[0] + as1[0]) * (1.f / DM);
        const float var = __builtin_fmaxf(q * (1.f / DM) - mean * mean, 0.f);
        const float mb = rbf(mean);
        const float dn = rbf(__builtin_sqrtf(rbf(rbf(var) + 1e-5f)));
        if (g == (c >> 2)) {
          const int r = 16 * kw + c;
          *(f32x4*)(lds + L1_ST + r * 16) = (f32x4){mb, dn, 1.f / dn, 0.f};
          if (cg == 0) *(f32x2*)(p.ws + WS_ST + (size_t)(row0 + r) * 8) = (f32x2){mb, dn};
        }
        lds_arrive(cnt + 4, lane);
        STAMP_AT(0, 8);
      }
    }
    STAMP_AT(0, 2);
    WAITL();
    float* tab = (float*)treg;
#pragma unroll
    for (int cb = 0; cb < 2; ++cb) {
      const int j = 16 * cb + fr16;
#pragma unroll
      for (int reg = 0; reg < 4; ++reg) {
        int ti;
        if (fg < 2) ti = 4 * fg + reg + 8 * j;
        else if (fg == 2) ti = 256 + reg + 4 * j;
        else ti = 384 + 64 * (reg >> 1) + (reg & 1) + 2 * j;
        tab[ti] = acc[0][cb][reg];
        if (fg == 0) tab[512 + 32 * reg + j] = acc[1][cb][reg];
        if (fg == 1 && reg == 0 && j == 0) tab[640] = acc[1][cb][reg];
      }
    }
    lds_arrive(cnt + 1 + chl, lane);
    lds_wait(cnt + 1 + chl, 2);
    {
      const float* t0 = (const float*)(lds + L1_T + (2 * chl) * KT_SZ);
      float v[11];
      float mx = 0.f;
#pragma unroll
      for (int k = 0; k < 11; ++k) {
        const int i = (k < 10) ? lane + 64 * k : 640;
        v[k] = t0[i] + t0[i + KT_SZ / 4];
        mx = __builtin_fmaxf(mx, __builtin_fabsf(v[k]));
      }
      mx = wave_max_nonneg(mx);
      int eb = (int)(f2u(mx) >> 23);
      int ex = 140 - eb;
      ex = (eb == 0 || eb == 255) ? 0 : (ex > 120 ? 120 : ex);
      const float sc = u2f((unsigned)(127 + ex) << 23);
      if (lane == 0 && nh == 0) { ((float*)(p.ws + WS_KS))[d] = u2f((unsigned)(127 - ex) << 23); }
      _Float16* th = (_Float16*)(treg + 4096);
#pragma unroll
      for (int k = 0; k < 10; ++k) th[lane + 64 * k] = (_Float16)(v[k] * sc);
      if (lane == 0) { th[640] = (_Float16)(v[10] * sc); th[641] = (_Float16)0.f; }
      asm volatile("" ::: "memory");
      const unsigned short* tu = (const unsigned short*)th;
      unsigned short* kr = (unsigned short*)(p.ws + WS_KR) + (size_t)d * KRL;
      if (nh == 0) {
#pragma unroll
        for (int k = 0; k < 2; ++k) {
          const int ck = lane + 64 * k, M = 256 - ck;
          const unsigned a = tu[384 + M], bb = tu[383 + M];
          const unsigned aa = a | (a << 16), b2 = bb | (bb << 16);
          const unsigned mid = ((M & 1) ? bb : a) | (bb << 16);
          *(u32x4*)(kr + 8 * ck) = (u32x4){aa, aa, mid, b2};
        }
      } else {
        {
          const int ck = 128 + lane, M = 256 - ck;
          const unsigned a = tu[256 + 2 * M], bb = tu[255 + 2 * M], cc = tu[254 + 2 * M];
          *(u32x4*)(kr + 8 * ck) = (u32x4){a | (a << 16), a | (bb << 16), bb | (bb << 16), cc | (cc << 16)};
        }
#pragma unroll
        for (int k = 0; k < 2; ++k) {
          const int ck = 192 + lane + 64 * k;
          if (ck < KRL / 8) {
            unsigned hv[8];
#pragma unroll
            for (int e = 0; e < 8; ++e) {
              const int lag = 2048 - (8 * ck + e);
              int ti = (int)(pkbf((float)lag, 0.f) & 0xffffu) - 17024;
              ti = lag < 256 ? lag : ti;
              ti = (lag < 0) ? 641 : ti;
              hv[e] = tu[ti];
            }
            u32x4 o;
            o[0] = hv[0] | (hv[1] << 16); o[1] = hv[2] | (hv[3] << 16);
            o[2] = hv[4] | (hv[5] << 16); o[3] = hv[6] | (hv[7] << 16);
            *(u32x4*)(kr + 8 * ck) = o;
          }
        }
      }
      STAMP_AT(0, 3);
    }
    {
      lds_wait(cnt + 4, 4);
      const int q4 = c >> 2, pp = c & 3;
      f32x4 st[16];
#pragma unroll
      for (int r = 0; r < 16; ++r) st[r] = *(const f32x4*)(lds + L1_ST + (16 * g + r) * 16);
      const int ck = 8 * cg + 2 * kw + (pp >> 1);
      unsigned ov[8];
#pragma unroll
      for (int k = 0; k < 4; ++k) {
        const int r16 = 4 * k + q4;
        const char* a0 = lds + L1_A + (16 * g + r16) * 1024 + ((ck ^ r16) * 16) + 8 * (pp & 1);
        const u32x2 v = __builtin_bit_cast(u32x2, __builtin_amdgcn_ds_read_tr16_b64_v4i16((LAS s16x4*)a0));
#pragma unroll
        for (int e = 0; e < 2; ++e) {
          const unsigned ve = v[e];
          const f32x4 s0 = st[4 * k + 2 * e], s1 = st[4 * k + 2 * e + 1];
          const float t0 = rbf(bflo(ve) - s0[0]), t1 = rbf(bfhi(ve) - s1[0]);
          float q0 = t0 * s0[2], q1 = t1 * s1[2];
          q0 = __builtin_fmaf(__builtin_fmaf(-q0, s0[1], t0), s0[2], q0);
          q1 = __builtin_fmaf(__builtin_fmaf(-q1, s1[1], t1), s1[2], q1);
          const unsigned xb = pkbf(q0, q1);
          ov[2 * k + e] = pkh(bflo(xb), bfhi(xb));
        }
      }
      unsigned short* dst = (unsigned short*)(p.ws + WS_XNT) + (size_t)(64 * cg + 16 * kw + c) * SL + row0 + 16 * g;
      *(u32x4*)dst = (u32x4){ov[0], ov[1], ov[2], ov[3]};
      *(u32x4*)(dst + 8) = (u32x4){ov[4], ov[5], ov[6], ov[7]};
    }
    {
      char* ydst = p.ws + WS_YT + (size_t)b * 8192 + kw * 2048 + lane * 16;
      const u32x4 sent = {0xffffffffu, 0xffffffffu, 0xffffffffu, 0xffffffffu};
      *(u32x4*)ydst = sent;
      *(u32x4*)(ydst + 1024) = sent;
    }
    {
      const int blk = 4 * b + kw;
      if (blk < 344) {
        const int ct = blk / 43, ks = blk % 43;
        const unsigned short* wsrc = p.w2 + (size_t)(32 * ks + 8 * g) * DM + 64 * ct + 4 * c;
        u32x2 r[8];
#pragma unroll
        for (int j = 0; j < 8; ++j) r[j] = *(const u32x2*)(wsrc + (size_t)j * DM);
        u32x4 f4[4];
#pragma unroll
        for (int q2 = 0; q2 < 4; ++q2) {
          f4[0][q2] = __builtin_amdgcn_perm(r[2 * q2 + 1][0], r[2 * q2][0], 0x05040100u);
          f4[1][q2] = __builtin_amdgcn_perm(r[2 * q2 + 1][0], r[2 * q2][0], 0x07060302u);
          f4[2][q2] = __builtin_amdgcn_perm(r[2 * q2 + 1][1], r[2 * q2][1], 0x05040100u);
          f4[3][q2] = __builtin_amdgcn_perm(r[2 * q2 + 1][1], r[2 * q2][1], 0x07060302u);
        }
        char* dst2 = p.ws + WS_W2F + (size_t)blk * 4096 + lane * 16;
#pragma unroll
        for (int t = 0; t < 4; ++t) *(u32x4*)(dst2 + t * 1024) = f4[t];
      }
    }
    STAMP_AT(0, 9);
  }
  STAMP_FLUSH(0);
}

#define GAS __attribute__((address_space(1)))
constexpr int KCS = KRL * 2;
constexpr int CV_CH = 8 * KCS + 4608;
constexpr int L2_CR = 2 * CV_CH;
constexpr int L2_GR = L2_CR + 16384;
constexpr int L2_Y = L2_GR + 49152;
constexpr int L2_T1 = L2_Y + 64 * 136;
constexpr int L2_C = L2_T1 + 8192;
constexpr int L2_SIZE = L2_C + 64;

__global__ void __launch_bounds__(512, 2) k_phase2(Params p) {
  __shared__ __attribute__((aligned(16))) char lds[L2_SIZE];
  STAMP_DECL
  const int tid = TIDX, lane = tid & 63;
  const int w = __builtin_amdgcn_readfirstlane(tid >> 6);
  const int b = BIDX;
  LAS unsigned* cnt = (LAS unsigned*)(LAS char*)(lds + L2_C);
  if (tid < 16) cnt[tid] = 0u;
  wg_sync();
  STAMP_AT(1, 0);
  const int xcd = b & 7, idx = b >> 3;
  const int rt = xcd * 4 + (idx >> 3), ct = idx & 7;
  const int row0 = rt * 64, col0 = ct * 64;
  const int c = lane & 15, g = lane >> 4;
  GAS unsigned* flags = (GAS unsigned*)(p.ws + WS_FL);
  if (w < 4) {
    const int chl = w >> 1, hf = w & 1;
    const int d = 2 * b + chl;
    char* kl = lds + chl * CV_CH;
    char* ul = kl + 8 * KCS;
    const int erow = row0 + 16 * w + 4 * g;
    u32x2 xv[4];
    f32x4 st01, st23;
    u32x2 dk;
    float isc;
    {
      const int t2 = tid & 127;
      const u32x4* ksrc = (const u32x4*)(p.ws + WS_KR + (size_t)d * KCS);
      const u32x4* usrc = (const u32x4*)(p.ws + WS_XNT + (size_t)d * SL * 2);
      u32x4 kc[3], kp[3];
#pragma unroll
      for (int k = 0; k < 3; ++k) {
        int ci = t2 + 128 * k;
        ci = ci < 260 ? ci : 259;
        kc[k] = ksrc[ci];
        kp[k] = ksrc[ci > 0 ? ci - 1 : 0];
      }
      u32x4 uv0 = usrc[t2], uv1 = usrc[t2 + 128];
      SB();
#pragma unroll
      for (int r = 0; r < 4; ++r) xv[r] = *(const u32x2*)(p.x + (size_t)(erow + r) * DM + col0 + 4 * c);
      st01 = *(const f32x4*)(p.ws + WS_ST + (size_t)erow * 8);
      st23 = *(const f32x4*)(p.ws + WS_ST + (size_t)erow * 8 + 16);
      dk = *(const u32x2*)(p.Dsk + col0 + 4 * c);
      isc = ((const float*)(p.ws + WS_KS))[d];
      SB();
      const u32x4 z = {0u, 0u, 0u, 0u};
      if (t2 < 32) *(u32x4*)(ul + t2 * 16) = z;
#pragma unroll
      for (int k = 0; k < 3; ++k) {
        const int ci = t2 + 128 * k;
        if (ci < 260) {
          unsigned wd[8] = {kp[k][0], kp[k][1], kp[k][2], kp[k][3], kc[k][0], kc[k][1], kc[k][2], kc[k][3]};
          unsigned od[7];
#pragma unroll
          for (int j = 0; j < 7; ++j) od[j] = __builtin_amdgcn_alignbit(wd[j + 1], wd[j], 16);
          char* dst = kl + ci * 16;
#pragma unroll
          for (int sg = 0; sg < 8; ++sg) {
            u32x4 o;
            if ((sg & 1) == 0) { const int s0 = (8 - sg) >> 1; o = (u32x4){wd[s0], wd[s0 + 1], wd[s0 + 2], wd[s0 + 3]}; }
            else { const int s0 = (7 - sg) >> 1; o = (u32x4){od[s0], od[s0 + 1], od[s0 + 2], od[s0 + 3]}; }
            *(u32x4*)(dst + sg * KCS) = o;
          }
        }
      }
      *(u32x4*)(ul + 512 + t2 * 16) = uv0;
      *(u32x4*)(ul + 512 + (t2 + 128) * 16) = uv1;
    }
    lds_arrive(cnt + chl, lane);
    lds_wait(cnt + chl, 2);
    STAMP_AT(1, 1);
    const int a = c;
    f32x4 acc[8];
#pragma unroll
    for (int r = 0; r < 8; ++r) acc[r] = (f32x4){0.f, 0.f, 0.f, 0.f};
    const char* ab = kl + (a & 7) * KCS + 2 * (2032 + 8 * g - 8 * (a >> 3)) - 64 * 4 * hf;
    const char* bb = ul + 2 * (240 + 16 * a + 8 * g) - 64 * 4 * hf;
    f16x8 bf[2][8], af[2][8];
#pragma unroll
    for (int t = 0; t < 8; ++t) { bf[0][t] = *(const f16x8*)(bb + 512 * t); af[0][t] = *(const f16x8*)(ab - 512 * t); }
#pragma unroll
    for (int i = 0; i < 4; ++i) {
      if (i + 1 < 4) {
#pragma unroll
        for (int t = 0; t < 8; ++t) {
          bf[(i + 1) & 1][t] = *(const f16x8*)(bb - 64 * (i + 1) + 512 * t);
          af[(i + 1) & 1][t] = *(const f16x8*)(ab - 64 * (i + 1) - 512 * t);
        }
      }
      SB();
#pragma unroll
      for (int v = 0; v < 8; ++v)
#pragma unroll
        for (int t = v; t < 8; ++t)
          acc[t] = __builtin_amdgcn_mfma_f32_16x16x32_f16(af[i & 1][v], bf[i & 1][t - v], acc[t], 0, 0, 0);
      SB();
    }
    STAMP_AT(1, 2);
    f32x4* red = (f32x4*)(lds + L2_CR + chl * 8192);
    f32x4 mine[4];
    if (hf) {
#pragma unroll
      for (int r = 0; r < 4; ++r) { red[(4 + r) * 64 + lane] = acc[r]; mine[r] = acc[4 + r]; }
    } else {
#pragma unroll
      for (int r = 0; r < 4; ++r) { red[r * 64 + lane] = acc[4 + r]; mine[r] = acc[r]; }
    }
    lds_arrive(cnt + 2 + chl, lane);
    lds_wait(cnt + 2 + chl, 2);
    {
      unsigned pk[4][2];
#pragma unroll
      for (int r = 0; r < 4; ++r) {
        const f32x4 v = (mine[r] + red[(4 * (1 - hf) + r) * 64 + lane]) * isc;
        pk[r][0] = pkbf(v[0], v[1]);
        pk[r][1] = pkbf(v[2], v[3]);
      }
      const auto rsrc = __builtin_amdgcn_make_buffer_rsrc((void*)(p.ws + WS_YT), 0, DM * SL * 2, 0x00020000);
#pragma unroll
      for (int r = 0; r < 2; ++r) {
        auto s0 = __builtin_amdgcn_permlane16_swap(pk[r][0], pk[r + 2][0], false, false);
        auto s1 = __builtin_amdgcn_permlane16_swap(pk[r][1], pk[r + 2][1], false, false);
        const unsigned a0 = s0[0], b0 = s0[1], a1 = s1[0], b1 = s1[1];
        const u32x4 o = {a0, a1, b0, b1};
        const int tile = r + 2 * (g & 1);
        const int off = (d * SL + 1024 * hf + 256 * tile + 16 * a + 8 * (g >> 1)) * 2;
        __builtin_amdgcn_raw_buffer_store_b128(o, rsrc, off, 0, 16);
      }
    }
    STAMP_AT(1, 3);
    {
      const int ch = 16 * w + (lane >> 2), k0 = (lane & 3) * 2;
      const auto rsrc = __builtin_amdgcn_make_buffer_rsrc((void*)(p.ws + WS_YT), 0, DM * SL * 2, 0x00020000);
      const int off = ((col0 + ch) * SL + row0) * 2 + k0 * 16;
      float xf[4][4], xd[4][4];
      {
        const float dkf[4] = {bflo(dk[0]), bfhi(dk[0]), bflo(dk[1]), bfhi(dk[1])};
#pragma unroll
        for (int r = 0; r < 4; ++r) {
          xf[r][0] = bflo(xv[r][0]); xf[r][1] = bfhi(xv[r][0]); xf[r][2] = bflo(xv[r][1]); xf[r][3] = bfhi(xv[r][1]);
          const float mb = (r == 0) ? st01[0] : (r == 1) ? st01[2] : (r == 2) ? st23[0] : st23[2];
          const float den = (r == 0) ? st01[1] : (r == 1) ? st01[3] : (r == 2) ? st23[1] : st23[3];
          const float rc = 1.f / den;
#pragma unroll
          for (int t = 0; t < 4; ++t) {
            const float a0 = rbf(xf[r][t] - mb);
            float q = a0 * rc;
            q = __builtin_fmaf(__builtin_fmaf(-q, den, a0), rc, q);
            xd[r][t] = rbf(rbf(q) * dkf[t]);
            asm volatile("" : "+v"(xd[r][t]), "+v"(xf[r][t]));
          }
        }
      }
      u32x4 v0, v1;
      unsigned spins = 0;
      lds_wait(cnt + 7, 4);
      while (true) {
        v0 = __builtin_amdgcn_raw_buffer_load_b128(rsrc, off, 0, 16);
        v1 = __builtin_amdgcn_raw_buffer_load_b128(rsrc, off + 16, 0, 16);
        bool bad = false;
#pragma unroll
        for (int e = 0; e < 4; ++e) {
          const unsigned a0 = v0[e], a1 = v1[e];
          bad |= ((a0 & 0xffffu) == 0xffffu) | ((a0 >> 16) == 0xffffu) | ((a1 & 0xffffu) == 0xffffu) | ((a1 >> 16) == 0xffffu);
        }
        if (__builtin_amdgcn_ballot_w64(bad) == 0ull) break;
        if (++spins > (1u << 18)) break;
        __builtin_amdgcn_s_sleep(4);
      }
      STAMP_AT(1, 4);
      char* yd = lds + L2_Y + ch * 136 + k0 * 16;
      *(u32x2*)yd = (u32x2){v0[0], v0[1]}; *(u32x2*)(yd + 8) = (u32x2){v0[2], v0[3]};
      *(u32x2*)(yd + 16) = (u32x2){v1[0], v1[1]}; *(u32x2*)(yd + 24) = (u32x2){v1[2], v1[3]};
      lds_arrive(cnt + 4, lane);
      lds_wait(cnt + 4, 4);
      u32x2 yv[4];
#pragma unroll
      for (int t = 0; t < 4; ++t) yv[t] = *(const u32x2*)(lds + L2_Y + (4 * c + t) * 136 + (16 * w + 4 * g) * 2);
#pragma unroll
      for (int r = 0; r < 4; ++r) {
        float o[4];
#pragma unroll
        for (int t = 0; t < 4; ++t) {
          const unsigned yw = yv[t][r >> 1];
          const float y = (r & 1) ? bfhi(yw) : bflo(yw);
          o[t] = xf[r][t] + rbf(y + xd[r][t]);
        }
        *(u32x2*)(lds + L2_T1 + ((w * 4 + r) * 64 + lane) * 8) = (u32x2){pkbf(o[0], o[1]), pkbf(o[2], o[3])};
      }
    }
    lds_arrive(cnt + 8 + w, lane);
    STAMP_AT(1, 5);
  } else {
    const int gw = w - 4;
    const int s0 = 11 * gw;
    const int ns = (gw == 3) ? 10 : 11;
    const char* hb = p.ws + WS_H + ((size_t)(rt * 4) * 43 + s0) * 1024 + lane * 16;
    const char* wb = p.ws + WS_W2F + ((size_t)ct * 43 + s0) * 4096 + lane * 16;
    constexpr int P = 4;
    u32x4 ar[P][4];
    u32x4 br[P][4];
    const int rot = 0;

    auto issue = [&](int s) {
      int ks = s + rot;
      ks = ks >= ns ? ks - ns : ks;
#pragma unroll
      for (int mt = 0; mt < 4; ++mt) ar[s % P][mt] = *(const u32x4*)(hb + (size_t)mt * 43 * 1024 + ks * 1024);
#pragma unroll
      for (int j = 0; j < 4; ++j) br[s % P][j] = *(const u32x4*)(wb + ks * 4096 + j * 1024);
    };
#pragma unroll
    for (int s = 0; s < P; ++s) issue(s);
    SB();
    f32x4 acc[4][4];
#pragma unroll
    for (int i = 0; i < 4; ++i)
#pragma unroll
      for (int t = 0; t < 4; ++t) acc[i][t] = (f32x4){0.f, 0.f, 0.f, 0.f};
#pragma unroll
    for (int s = 0; s < 11; ++s) {
      if (s < ns) {
#pragma unroll
        for (int mt = 0; mt < 4; ++mt)
#pragma unroll
          for (int t = 0; t < 4; ++t)
            acc[mt][t] = __builtin_amdgcn_mfma_f32_16x16x32_bf16(__builtin_bit_cast(s16x8, ar[s % P][mt]),
                                                                 __builtin_bit_cast(s16x8, br[s % P][t]), acc[mt][t], 0, 0, 0);
        SB();
        if (s + P < 11 && s + P < ns) issue(s + P);
        if (s == 6 && lane == 0) __hip_atomic_fetch_add(cnt + 7, 1u, __ATOMIC_RELAXED, __HIP_MEMORY_SCOPE_WORKGROUP);
        SB();
      }
    }

    const int erow = row0 + 16 * gw + 4 * g;
    STAMP_AT(1, 1);
    f32x4* red = (f32x4*)(lds + L2_GR);
    f32x4 f[4];
#pragma unroll
    for (int mt = 0; mt < 4; ++mt) {
      if (mt != gw) {
        const int slot = (gw > mt) ? (gw - 1) : gw;
#pragma unroll
        for (int t = 0; t < 4; ++t) red[((mt * 3 + slot) * 4 + t) * 64 + lane] = acc[mt][t];
      } else {
#pragma unroll
        for (int t = 0; t < 4; ++t) f[t] = acc[mt][t];
      }
    }
    lds_arrive(cnt + 6, lane);
    lds_wait(cnt + 6, 4);
    STAMP_AT(1, 2);
#pragma unroll
    for (int t = 0; t < 4; ++t) {
#pragma unroll
      for (int sl = 0; sl < 3; ++sl) f[t] += red[((gw * 3 + sl) * 4 + t) * 64 + lane];
#pragma unroll
      for (int r = 0; r < 4; ++r) f[t][r] = rbf(f[t][r]);
      asm volatile("" : "+v"(f[t]));
    }
    lds_wait(cnt + 8 + gw, 1);
    STAMP_AT(1, 3);
#pragma unroll
    for (int r = 0; r < 4; ++r) {
      const u32x2 tv = *(const u32x2*)(lds + L2_T1 + ((gw * 4 + r) * 64 + lane) * 8);
      const unsigned t0 = tv[0], t1 = tv[1];
      u32x2 ov;
      ov[0] = pkbf(bflo(t0) + f[0][r], bfhi(t0) + f[1][r]);
      ov[1] = pkbf(bflo(t1) + f[2][r], bfhi(t1) + f[3][r]);
      *(u32x2*)(p.out + (size_t)(erow + r) * DM + col0 + 4 * c) = ov;
    }
    STAMP_AT(1, 4);
  }
  STAMP_FLUSH(1);
}

#if defined(__HIP_DEVICE_COMPILE__)
#pragma clang attribute pop
#endif

extern "C" void kernel_launch(void* const* d_in, const int* in_sizes, int n_in,
                              void* d_out, int out_size, void* d_ws, size_t ws_size,
                              hipStream_t stream) {
  Params p;
  p.x = (const unsigned short*)d_in[0];
  p.Are = (const unsigned short*)d_in[1];
  p.Aim = (const unsigned short*)d_in[2];
  p.Bre = (const unsigned short*)d_in[3];
  p.Bim = (const unsigned short*)d_in[4];
  p.Cre = (const unsigned short*)d_in[5];
  p.Cim = (const unsigned short*)d_in[6];
  p.logdt = (const unsigned short*)d_in[7];
  p.Dsk = (const unsigned short*)d_in[8];
  p.w1 = (const unsigned short*)d_in[10];
  p.w2 = (const unsigned short*)d_in[11];
  p.out = (unsigned short*)d_out;
  p.ws = (char*)d_ws;
  k_phase1<<<256, 512, 0, stream>>>(p);
  k_phase2<<<256, 512, 0, stream>>>(p);
}
```

```cpp
#include <hip/hip_runtime.h>
#include <hip/hip_bf16.h>
#include <stdint.h>

typedef __attribute__((ext_vector_type(8))) short s16x8;
typedef __attribute__((ext_vector_type(4))) short s16x4;
typedef __attribute__((ext_vector_type(8))) _Float16 f16x8;
typedef __attribute__((ext_vector_type(4))) float f32x4;
typedef __attribute__((ext_vector_type(2))) float f32x2;
typedef __attribute__((ext_vector_type(16))) float f32x16;
typedef __attribute__((ext_vector_type(4))) unsigned u32x4;
typedef __attribute__((ext_vector_type(2))) unsigned u32x2;
typedef __attribute__((ext_vector_type(2))) __bf16 bf16x2_t;
typedef __attribute__((ext_vector_type(2))) _Float16 f16x2_t;
#define LAS __attribute__((address_space(3)))

constexpr int SL = 2048;
constexpr int DM = 512;
constexpr int NS = 128;
constexpr int DFF = 1376;
constexpr int KRL = 2080;

constexpr size_t WS_H = 0;
constexpr size_t WS_XNT = WS_H + (size_t)128 * 43 * 1024;
constexpr size_t WS_KR = WS_XNT + (size_t)DM * SL * 2;
constexpr size_t WS_YT = WS_KR + (size_t)DM * KRL * 2;
constexpr size_t WS_ST = WS_YT + (size_t)DM * SL * 2;
constexpr size_t WS_KS = WS_ST + (size_t)SL * 8;
constexpr size_t WS_FL = WS_KS + 4096;
constexpr size_t WS_W2F = WS_FL + 32768;

#if defined(__HIP_DEVICE_COMPILE__)
#pragma clang attribute push(__attribute__((target("no-packed-fp32-ops"))), apply_to = function)
#endif
__device__ __forceinline__ float u2f(unsigned u) { return __builtin_bit_cast(float, u); }
__device__ __forceinline__ unsigned f2u(float f) { return __builtin_bit_cast(unsigned, f); }
__device__ __forceinline__ float shx(float v, int lane, int m) {
  return u2f((unsigned)__builtin_amdgcn_ds_bpermute((lane ^ m) << 2, (int)f2u(v)));
}
__device__ __forceinline__ void wg_sync() {
  __builtin_amdgcn_fence(__ATOMIC_RELEASE, "workgroup");
  __builtin_amdgcn_s_barrier();
  __builtin_amdgcn_fence(__ATOMIC_ACQUIRE, "workgroup");
}
__device__ __forceinline__ float exp_acc(float x) {
  const float L2E_HI = 1.44269502162933349609375f, L2E_LO = 1.925963033500011e-8f;
  const float hi = x * L2E_HI;
  const float lo = __builtin_fmaf(x, L2E_LO, __builtin_fmaf(x, L2E_HI, -hi));
  const float e = __builtin_amdgcn_exp2f(hi);
  return __builtin_fmaf(e, lo * 0.6931471805599453f, e);
}
#define TIDX ((int)__builtin_amdgcn_workitem_id_x())
#define BIDX ((int)__builtin_amdgcn_workgroup_id_x())
__device__ __forceinline__ float bf2f(unsigned h) { return u2f(h << 16); }
__device__ __forceinline__ float bflo(unsigned u) { return u2f(u << 16); }
__device__ __forceinline__ float bfhi(unsigned u) { return u2f(u & 0xffff0000u); }
__device__ __forceinline__ f32x2 bfup(unsigned u) { return (f32x2){bflo(u), bfhi(u)}; }
__device__ __forceinline__ unsigned pkbf(float lo, float hi) {
  bf16x2_t r = {(__bf16)lo, (__bf16)hi};
  return __builtin_bit_cast(unsigned, r);
}
__device__ __forceinline__ unsigned pkbf2(f32x2 v) { return pkbf(v[0], v[1]); }
__device__ __forceinline__ float rbf(float x) { return bflo(pkbf(x, 0.f)); }
__device__ __forceinline__ unsigned pkh(float lo, float hi) {
  f16x2_t r = {(_Float16)lo, (_Float16)hi};
  return __builtin_bit_cast(unsigned, r);
}

#define STAMP_DECL
#define STAMP_AT(kid, i) do {} while (0)
#define STAMP_FLUSH(kid) do {} while (0)
#define SB() __builtin_amdgcn_sched_barrier(0)
#define WAITV(n) asm volatile("s_waitcnt vmcnt(%0)" ::"n"(n) : "memory")
#define WAITL() asm volatile("s_waitcnt lgkmcnt(0)" ::: "memory")

struct Params {
  const unsigned short* x; const unsigned short* Are; const unsigned short* Aim;
  const unsigned short* Bre; const unsigned short* Bim; const unsigned short* Cre; const unsigned short* Cim;
  const unsigned short* logdt; const unsigned short* Dsk; const unsigned short* w1; const unsigned short* w2;
  unsigned short* out; char* ws;
};

__device__ __forceinline__ unsigned lds_arrive(LAS unsigned* cnt, int lane) {
  WAITL();
  unsigned old = 0;
  if (lane == 0) old = __hip_atomic_fetch_add(cnt, 1u, __ATOMIC_RELAXED, __HIP_MEMORY_SCOPE_WORKGROUP);
  return __builtin_amdgcn_readfirstlane(old);
}
__device__ __forceinline__ void lds_wait(LAS unsigned* cnt, unsigned target) {
  while (__builtin_amdgcn_readfirstlane(__hip_atomic_load(cnt, __ATOMIC_RELAXED, __HIP_MEMORY_SCOPE_WORKGROUP)) < target)
    __builtin_amdgcn_s_sleep(1);
  asm volatile("" ::: "memory");
}
__device__ __forceinline__ void lds_wait2(LAS unsigned* c0, unsigned t0, LAS unsigned* c1, unsigned t1) {
  while (true) {
    const unsigned a = __hip_atomic_load(c0, __ATOMIC_RELAXED, __HIP_MEMORY_SCOPE_WORKGROUP);
    const unsigned b = __hip_atomic_load(c1, __ATOMIC_RELAXED, __HIP_MEMORY_SCOPE_WORKGROUP);
    if (__builtin_amdgcn_readfirstlane(a) >= t0 && __builtin_amdgcn_readfirstlane(b) >= t1) break;
    __builtin_amdgcn_s_sleep(1);
  }
  asm volatile("" ::: "memory");
}
__device__ __forceinline__ void lds_signal(LAS unsigned* cnt, int lane) {
  WAITL();
  if (lane == 0) __hip_atomic_fetch_add(cnt, 1u, __ATOMIC_RELAXED, __HIP_MEMORY_SCOPE_WORKGROUP);
}
__device__ __forceinline__ void glds16(const void* gsrc, unsigned lds_dst) {
  unsigned keep;
  asm volatile("s_mov_b32 %0, m0\n\ts_mov_b32 m0, %2\n\ts_nop 0\n\tglobal_load_lds_dwordx4 %1, off\n\ts_mov_b32 m0, %0"
               : "=&s"(keep) : "v"(gsrc), "s"(lds_dst) : "memory");
}

__device__ __forceinline__ void glds16s(const void* sbase, unsigned voff, unsigned lds_dst) {
  unsigned keep;
  asm volatile("s_mov_b32 %0, m0\n\ts_mov_b32 m0, %3\n\ts_nop 0\n\tglobal_load_lds_dwordx4 %1, %2\n\ts_mov_b32 m0, %0"
               : "=&s"(keep) : "v"(voff), "s"(sbase), "s"(lds_dst) : "memory");
}

template <int MODE> __device__ __forceinline__ void st16(void* ptr, u32x4 v) {
  if (MODE == 0) *(u32x4*)ptr = v;
  else if (MODE == 1) __builtin_nontemporal_store(v, (u32x4*)ptr);
  else if (MODE == 2) asm volatile("global_store_dwordx4 %0, %1, off sc1\n\ts_nop 1" :: "v"(ptr), "v"(v) : "memory");
  else asm volatile("global_store_dwordx4 %0, %1, off sc0 sc1\n\ts_nop 1" :: "v"(ptr), "v"(v) : "memory");
}
typedef __attribute__((ext_vector_type(2))) short s16x2;
template <int V> struct IC { static constexpr int v = V; };
typedef __attribute__((ext_vector_type(3))) unsigned u32x3;
typedef u32x3 __attribute__((aligned(4))) u32x3_a4;
constexpr int PB = 3;
constexpr int KT_P = 528;
constexpr int KT_CH = 54 * KT_P;
constexpr int L1_A = 0;
constexpr int L1_R = L1_A + 65536;
constexpr int L1_ST = L1_R + 2 * KT_CH;
constexpr int L1_CS = L1_ST + 1024;
constexpr int L1_C = L1_CS + 1536;
constexpr int L1_TH = L1_C + 64;
constexpr int L1_SIZE = L1_TH + 2688 + 16;

__device__ __forceinline__ void bsplit(f32x2 v, unsigned& hi, unsigned& lo) {
  hi = pkbf2(v);
  asm("" : "+v"(hi));
  lo = pkbf(v[0] - bflo(hi), v[1] - bfhi(hi));
}
template <int CTRL> __device__ __forceinline__ float dpp_max(float v) {
  const int t = __builtin_amdgcn_update_dpp(0, (int)f2u(v), CTRL, 0xf, 0xf, true);
  return __builtin_fmaxf(v, u2f((unsigned)t));
}
__device__ __forceinline__ float wave_max_nonneg(float v) {
  v = dpp_max<0x140>(dpp_max<0x141>(dpp_max<0x4E>(dpp_max<0xB1>(v))));
  const float a = u2f((unsigned)__builtin_amdgcn_readlane((int)f2u(v), 0));
  const float b = u2f((unsigned)__builtin_amdgcn_readlane((int)f2u(v), 16));
  const float c = u2f((unsigned)__builtin_amdgcn_readlane((int)f2u(v), 32));
  const float d = u2f((unsigned)__builtin_amdgcn_readlane((int)f2u(v), 48));
  return __builtin_fmaxf(__builtin_fmaxf(a, b), __builtin_fmaxf(c, d));
}

__global__ void __launch_bounds__(512, 2) k_phase1(Params p) {
  __shared__ __attribute__((aligned(16))) char lds[L1_SIZE];
  STAMP_DECL
  const int tid = TIDX, lane = tid & 63;
  const int w = __builtin_amdgcn_readfirstlane(tid >> 6);
  const int b = BIDX;
  LAS unsigned* cnt = (LAS unsigned*)(LAS char*)(lds + L1_C);
  if (tid < 16) cnt[tid] = 0u;
  wg_sync();
  STAMP_AT(0, 0);
  const int xcd = b & 7, idx = b >> 3;
  const int rt = (xcd >> 1) * 8 + (idx >> 2), cg = (xcd & 1) * 4 + (idx & 3);
  const int row0 = rt * 64;
  const int c = lane & 15, g = lane >> 4;
  const unsigned gl16 = (unsigned)((g ^ c) * 16);
  const char* arow = lds + L1_A + c * 1024;
  if (w >= 4) {
    const int gi = w - 4, ch = gi & 1, kh = gi >> 1;
    const int col0 = (cg == 7) ? 1280 : cg * 192 + 96 * ch;
    const bool gw = (cg < 7) || (ch == 0);
    const int rot = rt & 7;
    const unsigned lbase = (unsigned)(size_t)(LAS char*)lds;
    if (gw) {
      const char* bsrc = (const char*)p.w1 + (size_t)(256 * kh) * (DFF * 2) + col0 * 2;
      const unsigned bvo = (unsigned)(8 * g * (DFF * 2) + 12 * c);
      u32x3 bq[PB][8];
      auto issueB = [&](int s) {
        const char* q = bsrc + (size_t)((s + rot) & 7) * (32 * DFF * 2);
#pragma unroll
        for (int j = 0; j < 8; ++j) bq[s % PB][j] = *(const u32x3_a4*)(q + j * (DFF * 2) + bvo);
      };
      lds_wait(cnt + 3, 4);
#pragma unroll
      for (int s = 0; s < PB; ++s) issueB(s);
      SB();
      STAMP_AT(0, 4);
      f32x4 acc[4][6];
      f32x4 acs[6];
      const s16x8 onesf = {0x3f80, 0x3f80, 0x3f80, 0x3f80, 0x3f80, 0x3f80, 0x3f80, 0x3f80};
#pragma unroll
      for (int t = 0; t < 6; ++t) acs[t] = (f32x4){0.f, 0.f, 0.f, 0.f};
#pragma unroll
      for (int i = 0; i < 4; ++i)
#pragma unroll
        for (int t = 0; t < 6; ++t) acc[i][t] = (f32x4){0.f, 0.f, 0.f, 0.f};
      lds_wait(cnt + 0, 4);
      STAMP_AT(0, 1);
      auto mkF = [&](int s, int t) -> s16x8 {
        u32x4 F;
#pragma unroll
        for (int q = 0; q < 4; ++q)
          F[q] = __builtin_amdgcn_perm(bq[s % PB][2 * q + 1][t >> 1], bq[s % PB][2 * q][t >> 1], (t & 1) ? 0x07060302u : 0x05040100u);
        return __builtin_bit_cast(s16x8, F);
      };
      s16x8 Fc = mkF(0, 0);
#pragma unroll
      for (int s = 0; s < 8; ++s) {
        s16x8 af[4];
        {
          const char* ap = arow + (gl16 ^ (unsigned)((8 * kh + ((s + rot) & 7)) * 64));
#pragma unroll
          for (int mt = 0; mt < 4; ++mt) af[mt] = *(const s16x8*)(ap + mt * 16384);
        }
        const char* q = bsrc + (size_t)((s + PB - 1 + rot) & 7) * (32 * DFF * 2);
#pragma unroll
        for (int t = 0; t < 6; ++t) {
          s16x8 Fn = Fc;
          if (t + 1 < 6) Fn = mkF(s, t + 1);
          else if (s + 1 < 8) Fn = mkF(s + 1, 0);
          if (s >= 1 && s + PB - 1 < 8 && t < 4) {
            bq[(s + PB - 1) % PB][2 * t] = *(const u32x3_a4*)(q + (2 * t) * (DFF * 2) + bvo);
            bq[(s + PB - 1) % PB][2 * t + 1] = *(const u32x3_a4*)(q + (2 * t + 1) * (DFF * 2) + bvo);
          }
          SB();
#pragma unroll
          for (int mt = 0; mt < 4; ++mt)
            acc[mt][t] = __builtin_amdgcn_mfma_f32_16x16x32_bf16(Fc, af[mt], acc[mt][t], 0, 0, 0);
          acs[t] = __builtin_amdgcn_mfma_f32_16x16x32_bf16(Fc, onesf, acs[t], 0, 0, 0);
          SB();
          Fc = Fn;
        }
      }
      STAMP_AT(0, 2);
      int tz = TIDX;
      asm volatile("" : "+v"(tz));
      {
      const int lane = tz & 63, c = tz & 15, g = (tz >> 4) & 3;
      lds_wait2(cnt + 8, 2, cnt + 9, 2);
      f32x4* rx = (f32x4*)(lds + L1_R + gi * 12288);
      const f32x4* ro = (const f32x4*)(lds + L1_R + (gi ^ 2) * 12288);
      f32x4* csl = (f32x4*)(lds + L1_CS);
      auto fin = [&](auto KH) {
        constexpr int kk = decltype(KH)::v;
#pragma unroll
        for (int m = 0; m < 2; ++m)
#pragma unroll
          for (int t = 0; t < 6; ++t) rx[(m * 6 + t) * 64 + lane] = acc[2 * (1 - kk) + m][t];
        if (c == 0) {
#pragma unroll
          for (int t = 0; t < 6; ++t) csl[(gi * 6 + t) * 4 + g] = acs[t];
        }
        lds_signal(cnt + 5 + ch, lane);
        lds_wait2(cnt + 5 + ch, 2, cnt + 4, 4);
#pragma unroll
        for (int t = 0; t < 6; ++t) acs[t] += csl[((gi ^ 2) * 6 + t) * 4 + g];
#pragma unroll
        for (int m = 0; m < 2; ++m) {
          const int mt = 2 * kk + m;
          const f32x4 st = *(const f32x4*)(lds + L1_ST + (16 * mt + c) * 16);
          float hv[24];
#pragma unroll
          for (int t = 0; t < 6; ++t) {
            const f32x4 o = ro[(m * 6 + t) * 64 + lane];
#pragma unroll
            for (int r = 0; r < 4; ++r)
              hv[6 * r + t] = __builtin_fmaf(-st[0], acs[t][r], acc[2 * kk + m][t][r] + o[r]) * st[2];
          }
#pragma unroll
          for (int ck = 0; ck < 3; ++ck) {
            const int F0 = col0 + 24 * g + 8 * ck;
            u32x4 o;
#pragma unroll
            for (int e = 0; e < 4; ++e) {
              const s16x2 pv = __builtin_bit_cast(s16x2, pkbf(hv[8 * ck + 2 * e], hv[8 * ck + 2 * e + 1]));
              o[e] = __builtin_bit_cast(unsigned, __builtin_elementwise_max(pv, (s16x2){0, 0}));
            }
            st16<0>(p.ws + WS_H + ((size_t)(rt * 4 + mt) * 43 + (F0 >> 5)) * 1024 + (c + 16 * ((F0 >> 3) & 3)) * 16, o);
          }
        }
      };
      if (kh == 0) fin(IC<0>{}); else fin(IC<1>{});
      }
      STAMP_AT(0, 3);
    }
  } else {
    const int kw = w, chl = kw >> 1, nh = kw & 1;
    const int d = 2 * b + chl;
    char* tch = lds + L1_R + chl * KT_CH;
    unsigned short* th = (unsigned short*)(lds + L1_TH + chl * 1344);
    float a2, fr, wsc_inv;
    f32x2 W;
    bool grow;
    u32x4 xq[16];
    u32x2 w2r[8];
    const int blk = 4 * b + kw;
    const bool dow2 = blk < 344;
    {
      const size_t o = (size_t)d * NS + 64 * nh + lane, o2 = o ^ 64;
      const float lg = bf2f(p.logdt[d]);
      const float are = bf2f(p.Are[o]), aim = bf2f(p.Aim[o]);
      const float bre = bf2f(p.Bre[o]), bim = bf2f(p.Bim[o]);
      const float cr = bf2f(p.Cre[o]), ci = bf2f(p.Cim[o]);
      const float are2 = bf2f(p.Are[o2]);
      const float bre2 = bf2f(p.Bre[o2]), bim2 = bf2f(p.Bim[o2]);
      const float cr2 = bf2f(p.Cre[o2]), ci2 = bf2f(p.Cim[o2]);
      SB();
      {
        const char* xr = (const char*)p.x + (size_t)(row0 + 16 * kw) * 1024 + lane * 16;
#pragma unroll
        for (int i = 0; i < 16; ++i) xq[i] = *(const u32x4*)(xr + i * 1024);
      }
      if (dow2) {
        const int ct = blk / 43, ks = blk % 43;
        const unsigned short* wsrc = p.w2 + (size_t)(32 * ks + 8 * g) * DM + 64 * ct + 4 * c;
#pragma unroll
        for (int jj = 0; jj < 8; ++jj) w2r[jj] = *(const u32x2*)(wsrc + (size_t)jj * DM);
      }
      SB();
      if (lane == 0) __hip_atomic_fetch_add(cnt + 3, 1u, __ATOMIC_RELAXED, __HIP_MEMORY_SCOPE_WORKGROUP);
      SB();
      const float dt = rbf(exp_acc(lg));
      const float br = dt * bre, bi = dt * bim;
      a2 = (dt * are) * 1.4426950408889634f;
      fr = (dt * aim) * 0.15915494309189535f;
      W = (f32x2){cr * br - ci * bi, -(cr * bi + ci * br)};
      const float m1 = (__builtin_fabsf(cr) + __builtin_fabsf(ci)) * (__builtin_fabsf(bre) + __builtin_fabsf(bim));
      const float m2 = (__builtin_fabsf(cr2) + __builtin_fabsf(ci2)) * (__builtin_fabsf(bre2) + __builtin_fabsf(bim2));
      const float mw = wave_max_nonneg(__builtin_fmaxf(m1, m2)) * dt;
      const int eb = (int)(f2u(mw) >> 23);
      int ex = 136 - eb;
      ex = ex > 120 ? 120 : (ex < -120 ? -120 : ex);
      const float wsc = u2f((unsigned)(127 + ex) << 23);
      wsc_inv = u2f((unsigned)(127 - ex) << 23);
      W = (f32x2){W[0] * wsc, W[1] * wsc};
      const float gmax = __builtin_fmaxf(dt * are, dt * are2);
      grow = __builtin_amdgcn_ballot_w64(!(gmax * 2048.f < 2.f) || eb >= 250) != 0ull;
    }
    STAMP_AT(0, 1);
    auto cexpq = [&](float q) -> f32x2 {
      const float e = __builtin_amdgcn_exp2f(a2 * q);
      const float ph = __builtin_amdgcn_fractf(fr * q);
      return (f32x2){__builtin_amdgcn_cosf(ph) * e, __builtin_amdgcn_sinf(ph) * e};
    };
    auto cmul = [](f32x2 a, f32x2 bb) -> f32x2 {
      return (f32x2){__builtin_fmaf(a[0], bb[0], -(a[1] * bb[1])), __builtin_fmaf(a[0], bb[1], a[1] * bb[0])};
    };
    char* tw = tch + (64 * nh + lane) * 4;
    auto put = [&](int row, f32x2 v) {
      float v0 = v[0], v1 = v[1];
      asm("" : "+v"(v0), "+v"(v1));
      *(unsigned*)(tw + row * KT_P) = pkh(v0, v1);
    };
    auto xstore = [&]() {
#pragma unroll
      for (int i = 0; i < 16; ++i) *(u32x4*)(lds + L1_A + (16 * kw + i) * 1024 + ((lane ^ i) * 16)) = xq[i];
      lds_signal(cnt + 0, lane);
      if (dow2) {
        u32x4 f4[4];
#pragma unroll
        for (int q2 = 0; q2 < 4; ++q2) {
          f4[0][q2] = __builtin_amdgcn_perm(w2r[2 * q2 + 1][0], w2r[2 * q2][0], 0x05040100u);
          f4[1][q2] = __builtin_amdgcn_perm(w2r[2 * q2 + 1][0], w2r[2 * q2][0], 0x07060302u);
          f4[2][q2] = __builtin_amdgcn_perm(w2r[2 * q2 + 1][1], w2r[2 * q2][1], 0x05040100u);
          f4[3][q2] = __builtin_amdgcn_perm(w2r[2 * q2 + 1][1], w2r[2 * q2][1], 0x07060302u);
        }
        char* dst2 = p.ws + WS_W2F + (size_t)blk * 4096 + lane * 16;
#pragma unroll
        for (int t = 0; t < 4; ++t) st16<2>(dst2 + t * 1024, f4[t]);
      }
    };
    if (!grow) {
      const f32x2 E1 = cexpq(1.f), E256 = cexpq(256.f), E8 = cexpq(8.f);
      f32x2 y0 = (f32x2){1.f, 0.f}, y1 = cexpq(64.f), y2 = cexpq(128.f), y3 = cexpq(192.f);
      const f32x2 C1 = {E1[0], -E1[1]}, CL = {E256[0], -E256[1]};
      const f32x2 C2 = cmul(C1, C1), C4 = cmul(C2, C2);
      f32x2 xs[21];
      xs[0] = W;
      xs[8] = cmul(W, CL);
      xs[1] = cmul(xs[0], C1);  xs[12] = cmul(xs[8], CL);  xs[9] = cmul(xs[8], C2);
      xs[2] = cmul(xs[1], C1);  xs[14] = cmul(xs[12], CL); xs[10] = cmul(xs[9], C2); xs[13] = cmul(xs[12], C4);
      xs[3] = cmul(xs[2], C1);  xs[16] = cmul(xs[14], CL); xs[11] = cmul(xs[10], C2); xs[15] = cmul(xs[14], C4);
      xs[4] = cmul(xs[0], C4);  xs[17] = cmul(xs[16], CL);
      xs[5] = cmul(xs[1], C4);  xs[18] = cmul(xs[17], CL);
      xs[6] = cmul(xs[2], C4);  xs[19] = cmul(xs[18], CL);
      xs[7] = cmul(xs[3], C4);  xs[20] = cmul(xs[19], CL);
#pragma unroll
      for (int i = 0; i < 21; ++i) put(i, xs[i]);
      SB();
      xstore();
      SB();
      put(21, y0); put(29, y1); put(37, y2); put(45, y3);
#pragma unroll
      for (int i = 1; i < 8; ++i) {
        y0 = cmul(y0, E8); y1 = cmul(y1, E8); y2 = cmul(y2, E8); y3 = cmul(y3, E8);
        put(21 + i, y0); put(29 + i, y1); put(37 + i, y2); put(45 + i, y3);
      }
    } else {
      *(f32x4*)(tch + (64 * nh + lane) * 16) = (f32x4){a2, fr, W[0], -W[1]};
      xstore();
    }
    lds_signal(cnt + 1 + chl, lane);
    STAMP_AT(0, 4);
    lds_wait(cnt + 1 + chl, 2);
    f32x4 acc[2];
    acc[0] = (f32x4){0.f, 0.f, 0.f, 0.f}; acc[1] = acc[0];
    const int j = 16 * nh + c;
    if (!grow) {
      const char* fa = tch + c * KT_P + 16 * g;
      const char* fb = tch + (21 + j) * KT_P + 16 * g;
#pragma unroll
      for (int s = 0; s < 8; ++s) {
        const f16x8 bfr = *(const f16x8*)(fb + 64 * s);
        acc[0] = __builtin_amdgcn_mfma_f32_16x16x32_f16(*(const f16x8*)(fa + 64 * s), bfr, acc[0], 0, 0, 0);
        acc[1] = __builtin_amdgcn_mfma_f32_16x16x32_f16(*(const f16x8*)(fa + 16 * KT_P + 64 * s), bfr, acc[1], 0, 0, 0);
      }
    } else {
      for (int e = 0; e < 8; ++e) {
        const int row = 16 * (e >> 2) + 4 * g + (e & 3);
        const int qx = row < 8 ? row : row < 12 ? 256 + 2 * (row - 8) : row < 16 ? 512 + 256 * ((row - 12) >> 1) + 4 * (row & 1)
                                                                   : row < 21 ? 1024 + 256 * (row - 16) : 0;
        const float q = (float)(qx + 8 * j);
        float sacc = 0.f;
        for (int n = 0; n < 128; ++n) {
          const f32x4 pr = *(const f32x4*)(tch + n * 16);
          const float ph = __builtin_amdgcn_fractf(pr[1] * q);
          sacc += __builtin_amdgcn_exp2f(pr[0] * q) * (pr[2] * __builtin_amdgcn_cosf(ph) - pr[3] * __builtin_amdgcn_sinf(ph));
        }
        if (e < 4) acc[0][e & 3] = sacc; else acc[1][e & 3] = sacc;
      }
    }
    STAMP_AT(0, 5);
    {
      const bool v1 = (g == 0), v2 = (g == 1 && j == 0);
      float mx = __builtin_fmaxf(__builtin_fmaxf(__builtin_fabsf(acc[0][0]), __builtin_fabsf(acc[0][1])),
                                 __builtin_fmaxf(__builtin_fabsf(acc[0][2]), __builtin_fabsf(acc[0][3])));
      const float m1 = __builtin_fmaxf(__builtin_fmaxf(__builtin_fabsf(acc[1][1]), __builtin_fabsf(acc[1][2])), __builtin_fabsf(acc[1][3]));
      mx = __builtin_fmaxf(mx, (v1 || v2) ? __builtin_fabsf(acc[1][0]) : 0.f);
      mx = __builtin_fmaxf(mx, v1 ? m1 : 0.f);
      mx = wave_max_nonneg(mx);
      float* mxl = (float*)(lds + L1_TH + 2688) + kw;
      if (lane == 0) *mxl = mx;
      lds_signal(cnt + 8 + chl, lane);
      lds_wait(cnt + 8 + chl, 2);
      mx = __builtin_fmaxf(mx, ((const float*)(lds + L1_TH + 2688))[kw ^ 1]);
      const int eb = (int)(f2u(mx) >> 23);
      int ex = 140 - eb;
      ex = (eb == 0 || eb == 255) ? 0 : (ex > 120 ? 120 : ex);
      const float sc = u2f((unsigned)(127 + ex) << 23);
      if (lane == 0 && nh == 0) ((float*)(p.ws + WS_KS))[d] = u2f((unsigned)(127 - ex) << 23) * wsc_inv;
      const int b0 = (g < 2 ? 4 * g + 8 * j : g == 2 ? 256 + 4 * j : 384 + 2 * j);
      *(unsigned*)(th + b0) = pkh(acc[0][0] * sc, acc[0][1] * sc);
      *(unsigned*)(th + b0 + (g == 3 ? 64 : 2)) = pkh(acc[0][2] * sc, acc[0][3] * sc);
      const unsigned p01 = pkh(acc[1][0] * sc, acc[1][1] * sc), p23 = pkh(acc[1][2] * sc, acc[1][3] * sc);
      if (v1) {
        th[512 + j] = (unsigned short)p01; th[544 + j] = (unsigned short)(p01 >> 16);
        th[576 + j] = (unsigned short)p23; th[608 + j] = (unsigned short)(p23 >> 16);
      }
      if (v2) th[640] = (unsigned short)p01;
      lds_signal(cnt + 10 + chl, lane);
    }
    STAMP_AT(0, 6);
    {
      lds_wait(cnt + 0, 4);
      const s16x8 onesf = {0x3f80, 0x3f80, 0x3f80, 0x3f80, 0x3f80, 0x3f80, 0x3f80, 0x3f80};
      f32x4 as0 = {0.f, 0.f, 0.f, 0.f}, aq0 = as0, as1 = as0, aq1 = as0;
      const char* ab2 = arow + kw * 16384;
#pragma unroll
      for (int s = 0; s < 16; s += 2) {
        const s16x8 x0 = *(const s16x8*)(ab2 + (gl16 ^ (unsigned)(s * 64)));
        const s16x8 x1 = *(const s16x8*)(ab2 + (gl16 ^ (unsigned)(s * 64 + 64)));
        as0 = __builtin_amdgcn_mfma_f32_16x16x32_bf16(onesf, x0, as0, 0, 0, 0);
        aq0 = __builtin_amdgcn_mfma_f32_16x16x32_bf16(x0, x0, aq0, 0, 0, 0);
        as1 = __builtin_amdgcn_mfma_f32_16x16x32_bf16(onesf, x1, as1, 0, 0, 0);
        aq1 = __builtin_amdgcn_mfma_f32_16x16x32_bf16(x1, x1, aq1, 0, 0, 0);
      }
      const int r3 = c & 3;
      const f32x4 aq = aq0 + aq1;
      const float q = (r3 == 0) ? aq[0] : (r3 == 1) ? aq[1] : (r3 == 2) ? aq[2] : aq[3];
      const float mean = (as0[0] + as1[0]) * (1.f / DM);
      const float var = __builtin_fmaxf(q * (1.f / DM) - mean * mean, 0.f);
      const float mb = rbf(mean);
      const float dn = rbf(__builtin_sqrtf(rbf(rbf(var) + 1e-5f)));
      if (g == (c >> 2)) {
        const int r = 16 * kw + c;
        *(f32x4*)(lds + L1_ST + r * 16) = (f32x4){mb, dn, 1.f / dn, 0.f};
        if (cg == 0) *(f32x2*)(p.ws + WS_ST + (size_t)(row0 + r) * 8) = (f32x2){mb, dn};
      }
      lds_signal(cnt + 4, lane);
    }
    STAMP_AT(0, 7);
    {
      lds_wait(cnt + 10 + chl, 2);
      const unsigned short* tu = th;
      unsigned short* kr = (unsigned short*)(p.ws + WS_KR) + (size_t)d * KRL;
      if (nh == 0) {
#pragma unroll
        for (int k = 0; k < 2; ++k) {
          const int ck = lane + 64 * k, M = 256 - ck;
          const unsigned a = tu[384 + M], bb = tu[383 + M];
          const unsigned aa = a | (a << 16), b2 = bb | (bb << 16);
          const unsigned mid = ((M & 1) ? bb : a) | (bb << 16);
          st16<2>(kr + 8 * ck, (u32x4){aa, aa, mid, b2});
        }
        if (lane < 4) st16<2>(kr + 8 * (256 + lane), (u32x4){lane == 0 ? (unsigned)tu[0] : 0u, 0u, 0u, 0u});
      } else {
        {
          const int ck = 128 + lane, M = 256 - ck;
          const unsigned a = tu[256 + 2 * M], bb = tu[255 + 2 * M], cc = tu[254 + 2 * M];
          st16<2>(kr + 8 * ck, (u32x4){a | (a << 16), a | (bb << 16), bb | (bb << 16), cc | (cc << 16)});
        }
        {
          const int ck = 192 + lane, M = 256 - ck;
          const bool s1 = lane < 32;
          const int base = s1 ? 128 + 4 * M : 8 * M;
          const int o1[8] = {0, 0, 1, 2, 2, 2, 3, 4};
          unsigned hv[8];
#pragma unroll
          for (int e = 0; e < 8; ++e) hv[e] = tu[base - (s1 ? o1[e] : e)];
          st16<2>(kr + 8 * ck, (u32x4){hv[0] | (hv[1] << 16), hv[2] | (hv[3] << 16), hv[4] | (hv[5] << 16), hv[6] | (hv[7] << 16)});
        }
      }
    }
    STAMP_AT(0, 3);
    {
      lds_wait(cnt + 4, 4);
      const int q4 = c >> 2, pp = c & 3;
      f32x4 st[16];
#pragma unroll
      for (int r = 0; r < 16; ++r) st[r] = *(const f32x4*)(lds + L1_ST + (16 * g + r) * 16);
      const int ck = 8 * cg + 2 * kw + (pp >> 1);
      unsigned ov[8];
#pragma unroll
      for (int k = 0; k < 4; ++k) {
        const int r16 = 4 * k + q4;
        const char* a0 = lds + L1_A + (16 * g + r16) * 1024 + ((ck ^ r16) * 16) + 8 * (pp & 1);
        const u32x2 v = __builtin_bit_cast(u32x2, __builtin_amdgcn_ds_read_tr16_b64_v4i16((LAS s16x4*)a0));
#pragma unroll
        for (int e = 0; e < 2; ++e) {
          const unsigned ve = v[e];
          const f32x4 s0 = st[4 * k + 2 * e], s1 = st[4 * k + 2 * e + 1];
          const float t0 = rbf(bflo(ve) - s0[0]), t1 = rbf(bfhi(ve) - s1[0]);
          float q0 = t0 * s0[2], q1 = t1 * s1[2];
          q0 = __builtin_fmaf(__builtin_fmaf(-q0, s0[1], t0), s0[2], q0);
          q1 = __builtin_fmaf(__builtin_fmaf(-q1, s1[1], t1), s1[2], q1);
          const unsigned xb = pkbf(q0, q1);
          ov[2 * k + e] = pkh(bflo(xb), bfhi(xb));
        }
      }
      unsigned short* dst = (unsigned short*)(p.ws + WS_XNT) + (size_t)(64 * cg + 16 * kw + c) * SL + row0 + 16 * g;
      st16<2>(dst, (u32x4){ov[0], ov[1], ov[2], ov[3]});
      st16<2>(dst + 8, (u32x4){ov[4], ov[5], ov[6], ov[7]});
    }
    {
      char* ydst = p.ws + WS_YT + (size_t)b * 8192 + kw * 2048 + lane * 16;
      const u32x4 sent = {0xffffffffu, 0xffffffffu, 0xffffffffu, 0xffffffffu};
      st16<2>(ydst, sent);
      st16<2>(ydst + 1024, sent);
    }
    STAMP_AT(0, 9);
  }
  STAMP_FLUSH(0);
}

#define GAS __attribute__((address_space(1)))
constexpr int KCS = KRL * 2;
constexpr int CV_CH = 8 * KCS + 4608;
constexpr int L2_CR = 2 * CV_CH;
constexpr int L2_GR = L2_CR + 16384;
constexpr int L2_Y = L2_GR + 49152;
constexpr int L2_T1 = L2_Y + 64 * 136;
constexpr int L2_C = L2_T1 + 8192;
constexpr int L2_SIZE = L2_C + 64;

__global__ void __launch_bounds__(512, 2) k_phase2(Params p) {
  __shared__ __attribute__((aligned(16))) char lds[L2_SIZE];
  STAMP_DECL
  const int tid = TIDX, lane = tid & 63;
  const int w = __builtin_amdgcn_readfirstlane(tid >> 6);
  const int b = BIDX;
  LAS unsigned* cnt = (LAS unsigned*)(LAS char*)(lds + L2_C);
  if (tid < 16) cnt[tid] = 0u;
  wg_sync();
  STAMP_AT(1, 0);
  const int xcd = b & 7, idx = b >> 3;
  const int rt = xcd * 4 + (idx >> 3), ct = idx & 7;
  const int row0 = rt * 64, col0 = ct * 64;
  const int c = lane & 15, g = lane >> 4;
  GAS unsigned* flags = (GAS unsigned*)(p.ws + WS_FL);
  if (w < 4) {
    const int chl = w >> 1, hf = w & 1;
    const int d = 2 * b + chl;
    char* kl = lds + chl * CV_CH;
    char* ul = kl + 8 * KCS;
    const int erow = row0 + 16 * w + 4 * g;
    u32x2 xv[4];
    f32x4 st01, st23;
    u32x2 dk;
    float isc;
    {
      const int t2 = tid & 127;
      const u32x4* ksrc = (const u32x4*)(p.ws + WS_KR + (size_t)d * KCS);
      const u32x4* usrc = (const u32x4*)(p.ws + WS_XNT + (size_t)d * SL * 2);
      u32x4 kc[3], kp[3];
#pragma unroll
      for (int k = 0; k < 3; ++k) {
        int ci = t2 + 128 * k;
        ci = ci < 260 ? ci : 259;
        kc[k] = ksrc[ci];
        kp[k] = ksrc[ci > 0 ? ci - 1 : 0];
      }
      u32x4 uv0 = usrc[t2], uv1 = usrc[t2 + 128];
      SB();
#pragma unroll
      for (int r = 0; r < 4; ++r) xv[r] = *(const u32x2*)(p.x + (size_t)(erow + r) * DM + col0 + 4 * c);
      st01 = *(const f32x4*)(p.ws + WS_ST + (size_t)erow * 8);
      st23 = *(const f32x4*)(p.ws + WS_ST + (size_t)erow * 8 + 16);
      dk = *(const u32x2*)(p.Dsk + col0 + 4 * c);
      isc = ((const float*)(p.ws + WS_KS))[d];
      SB();
      const u32x4 z = {0u, 0u, 0u, 0u};
      if (t2 < 32) *(u32x4*)(ul + t2 * 16) = z;
#pragma unroll
      for (int k = 0; k < 3; ++k) {
        const int ci = t2 + 128 * k;
        if (ci < 260) {
          unsigned wd[8] = {kp[k][0], kp[k][1], kp[k][2], kp[k][3], kc[k][0], kc[k][1], kc[k][2], kc[k][3]};
          unsigned od[7];
#pragma unroll
          for (int j = 0; j < 7; ++j) od[j] = __builtin_amdgcn_alignbit(wd[j + 1], wd[j], 16);
          char* dst = kl + ci * 16;
#pragma unroll
          for (int sg = 0; sg < 8; ++sg) {
            u32x4 o;
            if ((sg & 1) == 0) { const int s0 = (8 - sg) >> 1; o = (u32x4){wd[s0], wd[s0 + 1], wd[s0 + 2], wd[s0 + 3]}; }
            else { const int s0 = (7 - sg) >> 1; o = (u32x4){od[s0], od[s0 + 1], od[s0 + 2], od[s0 + 3]}; }
            *(u32x4*)(dst + sg * KCS) = o;
          }
        }
      }
      *(u32x4*)(ul + 512 + t2 * 16) = uv0;
      *(u32x4*)(ul + 512 + (t2 + 128) * 16) = uv1;
    }
    lds_signal(cnt + chl, lane);
    lds_wait(cnt + chl, 2);
    STAMP_AT(1, 1);
    const int a = c;
    f32x4 acc[8];
#pragma unroll
    for (int r = 0; r < 8; ++r) acc[r] = (f32x4){0.f, 0.f, 0.f, 0.f};
    const char* ab = kl + (a & 7) * KCS + 2 * (2032 + 8 * g - 8 * (a >> 3)) - 64 * 4 * hf;
    const char* bb = ul + 2 * (240 + 16 * a + 8 * g) - 64 * 4 * hf;
    f16x8 bf[2][8], af[2][8];
#pragma unroll
    for (int t = 0; t < 8; ++t) { bf[0][t] = *(const f16x8*)(bb + 512 * t); af[0][t] = *(const f16x8*)(ab - 512 * t); }
#pragma unroll
    for (int i = 0; i < 4; ++i) {
      if (i + 1 < 4) {
#pragma unroll
        for (int t = 0; t < 8; ++t) {
          bf[(i + 1) & 1][t] = *(const f16x8*)(bb - 64 * (i + 1) + 512 * t);
          af[(i + 1) & 1][t] = *(const f16x8*)(ab - 64 * (i + 1) - 512 * t);
        }
      }
      SB();
#pragma unroll
      for (int v = 0; v < 8; ++v)
#pragma unroll
        for (int t = v; t < 8; ++t)
          acc[t] = __builtin_amdgcn_mfma_f32_16x16x32_f16(af[i & 1][v], bf[i & 1][t - v], acc[t], 0, 0, 0);
      SB();
    }
    STAMP_AT(1, 2);
    f32x4* red = (f32x4*)(lds + L2_CR + chl * 8192);
    f32x4 mine[4];
    if (hf) {
#pragma unroll
      for (int r = 0; r < 4; ++r) { red[(4 + r) * 64 + lane] = acc[r]; mine[r] = acc[4 + r]; }
    } else {
#pragma unroll
      for (int r = 0; r < 4; ++r) { red[r * 64 + lane] = acc[4 + r]; mine[r] = acc[r]; }
    }
    lds_signal(cnt + 2 + chl, lane);
    lds_wait(cnt + 2 + chl, 2);
    {
      unsigned pk[4][2];
#pragma unroll
      for (int r = 0; r < 4; ++r) {
        const f32x4 v = (mine[r] + red[(4 * (1 - hf) + r) * 64 + lane]) * isc;
        pk[r][0] = pkbf(v[0], v[1]);
        pk[r][1] = pkbf(v[2], v[3]);
      }
      const auto rsrc = __builtin_amdgcn_make_buffer_rsrc((void*)(p.ws + WS_YT), 0, DM * SL * 2, 0x00020000);
#pragma unroll
      for (int r = 0; r < 2; ++r) {
        auto s0 = __builtin_amdgcn_permlane16_swap(pk[r][0], pk[r + 2][0], false, false);
        auto s1 = __builtin_amdgcn_permlane16_swap(pk[r][1], pk[r + 2][1], false, false);
        const unsigned a0 = s0[0], b0 = s0[1], a1 = s1[0], b1 = s1[1];
        const u32x4 o = {a0, a1, b0, b1};
        const int tile = r + 2 * (g & 1);
        const int off = (d * SL + 1024 * hf + 256 * tile + 16 * a + 8 * (g >> 1)) * 2;
        __builtin_amdgcn_raw_buffer_store_b128(o, rsrc, off, 0, 16);
      }
    }
    STAMP_AT(1, 3);
    {
      const int ch = 16 * w + (lane >> 2), k0 = (lane & 3) * 2;
      const auto rsrc = __builtin_amdgcn_make_buffer_rsrc((void*)(p.ws + WS_YT), 0, DM * SL * 2, 0x00020000);
      const int off = ((col0 + ch) * SL + row0) * 2 + k0 * 16;
      float xf[4][4], xd[4][4];
      {
        const float dkf[4] = {bflo(dk[0]), bfhi(dk[0]), bflo(dk[1]), bfhi(dk[1])};
#pragma unroll
        for (int r = 0; r < 4; ++r) {
          xf[r][0] = bflo(xv[r][0]); xf[r][1] = bfhi(xv[r][0]); xf[r][2] = bflo(xv[r][1]); xf[r][3] = bfhi(xv[r][1]);
          const float mb = (r == 0) ? st01[0] : (r == 1) ? st01[2] : (r == 2) ? st23[0] : st23[2];
          const float den = (r == 0) ? st01[1] : (r == 1) ? st01[3] : (r == 2) ? st23[1] : st23[3];
          const float rc = 1.f / den;
#pragma unroll
          for (int t = 0; t < 4; ++t) {
            const float a0 = rbf(xf[r][t] - mb);
            float q = a0 * rc;
            q = __builtin_fmaf(__builtin_fmaf(-q, den, a0), rc, q);
            xd[r][t] = rbf(rbf(q) * dkf[t]);
            asm volatile("" : "+v"(xd[r][t]), "+v"(xf[r][t]));
          }
        }
      }
      u32x4 v0, v1;
      unsigned spins = 0;
      lds_wait(cnt + 7, 4);
      while (true) {
        v0 = __builtin_amdgcn_raw_buffer_load_b128(rsrc, off, 0, 16);
        v1 = __builtin_amdgcn_raw_buffer_load_b128(rsrc, off + 16, 0, 16);
        bool bad = false;
#pragma unroll
        for (int e = 0; e < 4; ++e) {
          const unsigned a0 = v0[e], a1 = v1[e];
          bad |= ((a0 & 0xffffu) == 0xffffu) | ((a0 >> 16) == 0xffffu) | ((a1 & 0xffffu) == 0xffffu) | ((a1 >> 16) == 0xffffu);
        }
        if (__builtin_amdgcn_ballot_w64(bad) == 0ull) break;
        if (++spins > (1u << 18)) break;
        __builtin_amdgcn_s_sleep(4);
      }
      STAMP_AT(1, 4);
      char* yd = lds + L2_Y + ch * 136 + k0 * 16;
      *(u32x2*)yd = (u32x2){v0[0], v0[1]}; *(u32x2*)(yd + 8) = (u32x2){v0[2], v0[3]};
      *(u32x2*)(yd + 16) = (u32x2){v1[0], v1[1]}; *(u32x2*)(yd + 24) = (u32x2){v1[2], v1[3]};
      lds_signal(cnt + 4, lane);
      lds_wait(cnt + 4, 4);
      u32x2 yv[4];
#pragma unroll
      for (int t = 0; t < 4; ++t) yv[t] = *(const u32x2*)(lds + L2_Y + (4 * c + t) * 136 + (16 * w + 4 * g) * 2);
#pragma unroll
      for (int r = 0; r < 4; ++r) {
        float o[4];
#pragma unroll
        for (int t = 0; t < 4; ++t) {
          const unsigned yw = yv[t][r >> 1];
          const float y = (r & 1) ? bfhi(yw) : bflo(yw);
          o[t] = xf[r][t] + rbf(y + xd[r][t]);
        }
        *(u32x2*)(lds + L2_T1 + ((w * 4 + r) * 64 + lane) * 8) = (u32x2){pkbf(o[0], o[1]), pkbf(o[2], o[3])};
      }
    }
    lds_signal(cnt + 8 + w, lane);
    STAMP_AT(1, 5);
  } else {
    const int gw = w - 4;
    const int s0 = 11 * gw;
    const int ns = (gw == 3) ? 10 : 11;
    const char* hb = p.ws + WS_H + ((size_t)(rt * 4) * 43 + s0) * 1024 + lane * 16;
    const char* wb = p.ws + WS_W2F + ((size_t)ct * 43 + s0) * 4096 + lane * 16;
    constexpr int P = 4;
    u32x4 ar[P][4];
    u32x4 br[P][4];
    const int rot = 0;

    auto issue = [&](int s) {
      int ks = s + rot;
      ks = ks >= ns ? ks - ns : ks;
#pragma unroll
      for (int mt = 0; mt < 4; ++mt) ar[s % P][mt] = *(const u32x4*)(hb + (size_t)mt * 43 * 1024 + ks * 1024);
#pragma unroll
      for (int j = 0; j < 4; ++j) br[s % P][j] = *(const u32x4*)(wb + ks * 4096 + j * 1024);
    };
#pragma unroll
    for (int s = 0; s < P; ++s) issue(s);
    SB();
    f32x4 acc[4][4];
#pragma unroll
    for (int i = 0; i < 4; ++i)
#pragma unroll
      for (int t = 0; t < 4; ++t) acc[i][t] = (f32x4){0.f, 0.f, 0.f, 0.f};
#pragma unroll
    for (int s = 0; s < 11; ++s) {
      if (s < ns) {
#pragma unroll
        for (int mt = 0; mt < 4; ++mt)
#pragma unroll
          for (int t = 0; t < 4; ++t)
            acc[mt][t] = __builtin_amdgcn_mfma_f32_16x16x32_bf16(__builtin_bit_cast(s16x8, ar[s % P][mt]),
                                                                 __builtin_bit_cast(s16x8, br[s % P][t]), acc[mt][t], 0, 0, 0);
        SB();
        if (s + P < 11 && s + P < ns) issue(s + P);
        if (s == 6 && lane == 0) __hip_atomic_fetch_add(cnt + 7, 1u, __ATOMIC_RELAXED, __HIP_MEMORY_SCOPE_WORKGROUP);
        SB();
      }
    }

    const int erow = row0 + 16 * gw + 4 * g;
    STAMP_AT(1, 1);
    f32x4* red = (f32x4*)(lds + L2_GR);
    f32x4 f[4];
#pragma unroll
    for (int mt = 0; mt < 4; ++mt) {
      if (mt != gw) {
        const int slot = (gw > mt) ? (gw - 1) : gw;
#pragma unroll
        for (int t = 0; t < 4; ++t) red[((mt * 3 + slot) * 4 + t) * 64 + lane] = acc[mt][t];
      } else {
#pragma unroll
        for (int t = 0; t < 4; ++t) f[t] = acc[mt][t];
      }
    }
    lds_signal(cnt + 6, lane);
    lds_wait(cnt + 6, 4);
    STAMP_AT(1, 2);
#pragma unroll
    for (int t = 0; t < 4; ++t) {
#pragma unroll
      for (int sl = 0; sl < 3; ++sl) f[t] += red[((gw * 3 + sl) * 4 + t) * 64 + lane];
#pragma unroll
      for (int r = 0; r < 4; ++r) f[t][r] = rbf(f[t][r]);
      asm volatile("" : "+v"(f[t]));
    }
    lds_wait(cnt + 8 + gw, 1);
    STAMP_AT(1, 3);
#pragma unroll
    for (int r = 0; r < 4; ++r) {
      const u32x2 tv = *(const u32x2*)(lds + L2_T1 + ((gw * 4 + r) * 64 + lane) * 8);
      const unsigned t0 = tv[0], t1 = tv[1];
      u32x2 ov;
      ov[0] = pkbf(bflo(t0) + f[0][r], bfhi(t0) + f[1][r]);
      ov[1] = pkbf(bflo(t1) + f[2][r], bfhi(t1) + f[3][r]);
      *(u32x2*)(p.out + (size_t)(erow + r) * DM + col0 + 4 * c) = ov;
    }
    STAMP_AT(1, 4);
  }
  STAMP_FLUSH(1);
}

#if defined(__HIP_DEVICE_COMPILE__)
#pragma clang attribute pop
#endif

extern "C" void kernel_launch(void* const* d_in, const int* in_sizes, int n_in,
                              void* d_out, int out_size, void* d_ws, size_t ws_size,
                              hipStream_t stream) {
  Params p;
  p.x = (const unsigned short*)d_in[0];
  p.Are = (const unsigned short*)d_in[1];
  p.Aim = (const unsigned short*)d_in[2];
  p.Bre = (const unsigned short*)d_in[3];
  p.Bim = (const unsigned short*)d_in[4];
  p.Cre = (const unsigned short*)d_in[5];
  p.Cim = (const unsigned short*)d_in[6];
  p.logdt = (const unsigned short*)d_in[7];
  p.Dsk = (const unsigned short*)d_in[8];
  p.w1 = (const unsigned short*)d_in[10];
  p.w2 = (const unsigned short*)d_in[11];
  p.out = (unsigned short*)d_out;
  p.ws = (char*)d_ws;
  k_phase1<<<256, 512, 0, stream>>>(p);
  k_phase2<<<256, 512, 0, stream>>>(p);
}
```

```cpp
#include <hip/hip_runtime.h>
#include <hip/hip_bf16.h>
#include <stdint.h>

typedef __attribute__((ext_vector_type(8))) short s16x8;
typedef __attribute__((ext_vector_type(4))) short s16x4;
typedef __attribute__((ext_vector_type(8))) _Float16 f16x8;
typedef __attribute__((ext_vector_type(4))) float f32x4;
typedef __attribute__((ext_vector_type(2))) float f32x2;
typedef __attribute__((ext_vector_type(16))) float f32x16;
typedef __attribute__((ext_vector_type(4))) unsigned u32x4;
typedef __attribute__((ext_vector_type(2))) unsigned u32x2;
typedef __attribute__((ext_vector_type(2))) __bf16 bf16x2_t;
typedef __attribute__((ext_vector_type(2))) _Float16 f16x2_t;
#define LAS __attribute__((address_space(3)))

constexpr int SL = 2048;
constexpr int DM = 512;
constexpr int NS = 128;
constexpr int DFF = 1376;
constexpr int KRL = 2080;

constexpr size_t WS_H = 0;
constexpr size_t WS_XNT = WS_H + (size_t)128 * 43 * 1024;
constexpr size_t WS_KR = WS_XNT + (size_t)DM * SL * 2;
constexpr size_t WS_YT = WS_KR + (size_t)DM * KRL * 2;
constexpr size_t WS_ST = WS_YT + (size_t)DM * SL * 2;
constexpr size_t WS_KS = WS_ST + (size_t)SL * 8;
constexpr size_t WS_FL = WS_KS + 4096;
constexpr size_t WS_W2F = WS_FL + 32768;

#if defined(__HIP_DEVICE_COMPILE__)
#pragma clang attribute push(__attribute__((target("no-packed-fp32-ops"))), apply_to = function)
#endif
__device__ __forceinline__ float u2f(unsigned u) { return __builtin_bit_cast(float, u); }
__device__ __forceinline__ unsigned f2u(float f) { return __builtin_bit_cast(unsigned, f); }
__device__ __forceinline__ float shx(float v, int lane, int m) {
  return u2f((unsigned)__builtin_amdgcn_ds_bpermute((lane ^ m) << 2, (int)f2u(v)));
}
__device__ __forceinline__ void wg_sync() {
  __builtin_amdgcn_fence(__ATOMIC_RELEASE, "workgroup");
  __builtin_amdgcn_s_barrier();
  __builtin_amdgcn_fence(__ATOMIC_ACQUIRE, "workgroup");
}
__device__ __forceinline__ float exp_acc(float x) {
  const float L2E_HI = 1.44269502162933349609375f, L2E_LO = 1.925963033500011e-8f;
  const float hi = x * L2E_HI;
  const float lo = __builtin_fmaf(x, L2E_LO, __builtin_fmaf(x, L2E_HI, -hi));
  const float e = __builtin_amdgcn_exp2f(hi);
  return __builtin_fmaf(e, lo * 0.6931471805599453f, e);
}
#define TIDX ((int)__builtin_amdgcn_workitem_id_x())
#define BIDX ((int)__builtin_amdgcn_workgroup_id_x())
__device__ __forceinline__ float bf2f(unsigned h) { return u2f(h << 16); }
__device__ __forceinline__ float bflo(unsigned u) { return u2f(u << 16); }
__device__ __forceinline__ float bfhi(unsigned u) { return u2f(u & 0xffff0000u); }
__device__ __forceinline__ f32x2 bfup(unsigned u) { return (f32x2){bflo(u), bfhi(u)}; }
__device__ __forceinline__ unsigned pkbf(float lo, float hi) {
  bf16x2_t r = {(__bf16)lo, (__bf16)hi};
  return __builtin_bit_cast(unsigned, r);
}
__device__ __forceinline__ unsigned pkbf2(f32x2 v) { return pkbf(v[0], v[1]); }
__device__ __forceinline__ float rbf(float x) { return bflo(pkbf(x, 0.f)); }
__device__ __forceinline__ unsigned pkh(float lo, float hi) {
  f16x2_t r = {(_Float16)lo, (_Float16)hi};
  return __builtin_bit_cast(unsigned, r);
}

#define STAMP_DECL
#define STAMP_AT(kid, i) do {} while (0)
#define STAMP_FLUSH(kid) do {} while (0)
#define SB() __builtin_amdgcn_sched_barrier(0)
#define WAITV(n) asm volatile("s_waitcnt vmcnt(%0)" ::"n"(n) : "memory")
#define WAITL() asm volatile("s_waitcnt lgkmcnt(0)" ::: "memory")

struct Params {
  const unsigned short* x; const unsigned short* Are; const unsigned short* Aim;
  const unsigned short* Bre; const unsigned short* Bim; const unsigned short* Cre; const unsigned short* Cim;
  const unsigned short* logdt; const unsigned short* Dsk; const unsigned short* w1; const unsigned short* w2;
  unsigned short* out; char* ws;
};

__device__ __forceinline__ unsigned lds_arrive(LAS unsigned* cnt, int lane) {
  WAITL();
  unsigned old = 0;
  if (lane == 0) old = __hip_atomic_fetch_add(cnt, 1u, __ATOMIC_RELAXED, __HIP_MEMORY_SCOPE_WORKGROUP);
  return __builtin_amdgcn_readfirstlane(old);
}
__device__ __forceinline__ void lds_wait(LAS unsigned* cnt, unsigned target) {
  while (__builtin_amdgcn_readfirstlane(__hip_atomic_load(cnt, __ATOMIC_RELAXED, __HIP_MEMORY_SCOPE_WORKGROUP)) < target)
    __builtin_amdgcn_s_sleep(1);
  asm volatile("" ::: "memory");
}
__device__ __forceinline__ void lds_wait2(LAS unsigned* c0, unsigned t0, LAS unsigned* c1, unsigned t1) {
  while (true) {
    const unsigned a = __hip_atomic_load(c0, __ATOMIC_RELAXED, __HIP_MEMORY_SCOPE_WORKGROUP);
    const unsigned b = __hip_atomic_load(c1, __ATOMIC_RELAXED, __HIP_MEMORY_SCOPE_WORKGROUP);
    if (__builtin_amdgcn_readfirstlane(a) >= t0 && __builtin_amdgcn_readfirstlane(b) >= t1) break;
    __builtin_amdgcn_s_sleep(1);
  }
  asm volatile("" ::: "memory");
}
__device__ __forceinline__ void lds_signal(LAS unsigned* cnt, int lane) {
  WAITL();
  if (lane == 0) __hip_atomic_fetch_add(cnt, 1u, __ATOMIC_RELAXED, __HIP_MEMORY_SCOPE_WORKGROUP);
}
__device__ __forceinline__ void glds16(const void* gsrc, unsigned lds_dst) {
  unsigned keep;
  asm volatile("s_mov_b32 %0, m0\n\ts_mov_b32 m0, %2\n\ts_nop 0\n\tglobal_load_lds_dwordx4 %1, off\n\ts_mov_b32 m0, %0"
               : "=&s"(keep) : "v"(gsrc), "s"(lds_dst) : "memory");
}

__device__ __forceinline__ void glds16s(const void* sbase, unsigned voff, unsigned lds_dst) {
  unsigned keep;
  asm volatile("s_mov_b32 %0, m0\n\ts_mov_b32 m0, %3\n\ts_nop 0\n\tglobal_load_lds_dwordx4 %1, %2\n\ts_mov_b32 m0, %0"
               : "=&s"(keep) : "v"(voff), "s"(sbase), "s"(lds_dst) : "memory");
}

template <int MODE> __device__ __forceinline__ void st16(void* ptr, u32x4 v) {
  if (MODE == 0) *(u32x4*)ptr = v;
  else if (MODE == 1) __builtin_nontemporal_store(v, (u32x4*)ptr);
  else if (MODE == 2) asm volatile("global_store_dwordx4 %0, %1, off sc1\n\ts_nop 1" :: "v"(ptr), "v"(v) : "memory");
  else asm volatile("global_store_dwordx4 %0, %1, off sc0 sc1\n\ts_nop 1" :: "v"(ptr), "v"(v) : "memory");
}
typedef __attribute__((ext_vector_type(2))) short s16x2;
template <int V> struct IC { static constexpr int v = V; };
typedef __attribute__((ext_vector_type(3))) unsigned u32x3;
typedef u32x3 __attribute__((aligned(4))) u32x3_a4;
constexpr int PB = 3;
constexpr int KT_P = 528;
constexpr int KT_CH = 54 * KT_P;
constexpr int L1_A = 0;
constexpr int L1_R = L1_A + 65536;
constexpr int L1_ST = L1_R + 2 * KT_CH;
constexpr int L1_CS = L1_ST + 1024;
constexpr int L1_C = L1_CS + 1536;
constexpr int L1_TH = L1_C + 64;
constexpr int L1_SIZE = L1_TH + 2688 + 16;

__device__ __forceinline__ void bsplit(f32x2 v, unsigned& hi, unsigned& lo) {
  hi = pkbf2(v);
  asm("" : "+v"(hi));
  lo = pkbf(v[0] - bflo(hi), v[1] - bfhi(hi));
}
template <int CTRL> __device__ __forceinline__ float dpp_max(float v) {
  const int t = __builtin_amdgcn_update_dpp(0, (int)f2u(v), CTRL, 0xf, 0xf, true);
  return __builtin_fmaxf(v, u2f((unsigned)t));
}
__device__ __forceinline__ float wave_max_nonneg(float v) {
  v = dpp_max<0x140>(dpp_max<0x141>(dpp_max<0x4E>(dpp_max<0xB1>(v))));
  const float a = u2f((unsigned)__builtin_amdgcn_readlane((int)f2u(v), 0));
  const float b = u2f((unsigned)__builtin_amdgcn_readlane((int)f2u(v), 16));
  const float c = u2f((unsigned)__builtin_amdgcn_readlane((int)f2u(v), 32));
  const float d = u2f((unsigned)__builtin_amdgcn_readlane((int)f2u(v), 48));
  return __builtin_fmaxf(__builtin_fmaxf(a, b), __builtin_fmaxf(c, d));
}

__global__ void __launch_bounds__(512, 2) k_phase1(Params p) {
  __shared__ __attribute__((aligned(16))) char lds[L1_SIZE];
  STAMP_DECL
  const int tid = TIDX, lane = tid & 63;
  const int w = __builtin_amdgcn_readfirstlane(tid >> 6);
  const int b = BIDX;
  LAS unsigned* cnt = (LAS unsigned*)(LAS char*)(lds + L1_C);
  if (tid < 16) cnt[tid] = 0u;
  wg_sync();
  STAMP_AT(0, 0);
  const int xcd = b & 7, idx = b >> 3;
  const int rt = (xcd >> 1) * 8 + (idx >> 2), cg = (xcd & 1) * 4 + (idx & 3);
  const int row0 = rt * 64;
  const int c = lane & 15, g = lane >> 4;
  const unsigned gl16 = (unsigned)((g ^ c) * 16);
  const char* arow = lds + L1_A + c * 1024;
  if (w >= 4) {
    const int gi = w - 4, ch = gi & 1, kh = gi >> 1;
    const int col0 = (cg == 7) ? 1280 : cg * 192 + 96 * ch;
    const bool gw = (cg < 7) || (ch == 0);
    const int rot = rt & 7;
    const unsigned lbase = (unsigned)(size_t)(LAS char*)lds;
    if (gw) {
      const char* bsrc = (const char*)p.w1 + (size_t)(256 * kh) * (DFF * 2) + col0 * 2;
      const unsigned bvo = (unsigned)(8 * g * (DFF * 2) + 12 * c);
      u32x3 bq[PB][8];
      auto issueB = [&](int s) {
        const char* q = bsrc + (size_t)((s + rot) & 7) * (32 * DFF * 2);
#pragma unroll
        for (int j = 0; j < 8; ++j) bq[s % PB][j] = *(const u32x3_a4*)(q + j * (DFF * 2) + bvo);
      };
      lds_wait(cnt + 3, 4);
#pragma unroll
      for (int s = 0; s < PB; ++s) issueB(s);
      SB();
      STAMP_AT(0, 4);
      f32x4 acc[4][6];
      f32x4 acs[6];
      const s16x8 onesf = {0x3f80, 0x3f80, 0x3f80, 0x3f80, 0x3f80, 0x3f80, 0x3f80, 0x3f80};
#pragma unroll
      for (int t = 0; t < 6; ++t) acs[t] = (f32x4){0.f, 0.f, 0.f, 0.f};
#pragma unroll
      for (int i = 0; i < 4; ++i)
#pragma unroll
        for (int t = 0; t < 6; ++t) acc[i][t] = (f32x4){0.f, 0.f, 0.f, 0.f};
      lds_wait(cnt + 0, 4);
      STAMP_AT(0, 1);
      auto mkF = [&](int s, int t) -> s16x8 {
        u32x4 F;
#pragma unroll
        for (int q = 0; q < 4; ++q)
          F[q] = __builtin_amdgcn_perm(bq[s % PB][2 * q + 1][t >> 1], bq[s % PB][2 * q][t >> 1], (t & 1) ? 0x07060302u : 0x05040100u);
        return __builtin_bit_cast(s16x8, F);
      };
      s16x8 Fc = mkF(0, 0);
#pragma unroll
      for (int s = 0; s < 8; ++s) {
        s16x8 af[4];
        {
          const char* ap = arow + (gl16 ^ (unsigned)((8 * kh + ((s + rot) & 7)) * 64));
#pragma unroll
          for (int mt = 0; mt < 4; ++mt) af[mt] = *(const s16x8*)(ap + mt * 16384);
        }
        const char* q = bsrc + (size_t)((s + PB - 1 + rot) & 7) * (32 * DFF * 2);
#pragma unroll
        for (int t = 0; t < 6; ++t) {
          s16x8 Fn = Fc;
          if (t + 1 < 6) Fn = mkF(s, t + 1);
          else if (s + 1 < 8) Fn = mkF(s + 1, 0);
          if (s >= 1 && s + PB - 1 < 8 && t < 4) {
            bq[(s + PB - 1) % PB][2 * t] = *(const u32x3_a4*)(q + (2 * t) * (DFF * 2) + bvo);
            bq[(s + PB - 1) % PB][2 * t + 1] = *(const u32x3_a4*)(q + (2 * t + 1) * (DFF * 2) + bvo);
          }
          SB();
#pragma unroll
          for (int mt = 0; mt < 4; ++mt)
            acc[mt][t] = __builtin_amdgcn_mfma_f32_16x16x32_bf16(Fc, af[mt], acc[mt][t], 0, 0, 0);
          acs[t] = __builtin_amdgcn_mfma_f32_16x16x32_bf16(Fc, onesf, acs[t], 0, 0, 0);
          SB();
          Fc = Fn;
        }
      }
      STAMP_AT(0, 2);
      int tz = TIDX;
      asm volatile("" : "+v"(tz));
      {
      const int lane = tz & 63, c = tz & 15, g = (tz >> 4) & 3;
      lds_wait2(cnt + 8, 2, cnt + 9, 2);
      f32x4* rx = (f32x4*)(lds + L1_R + gi * 12288);
      const f32x4* ro = (const f32x4*)(lds + L1_R + (gi ^ 2) * 12288);
      f32x4* csl = (f32x4*)(lds + L1_CS);
      auto fin = [&](auto KH) {
        constexpr int kk = decltype(KH)::v;
#pragma unroll
        for (int m = 0; m < 2; ++m)
#pragma unroll
          for (int t = 0; t < 6; ++t) rx[(m * 6 + t) * 64 + lane] = acc[2 * (1 - kk) + m][t];
        if (c == 0) {
#pragma unroll
          for (int t = 0; t < 6; ++t) csl[(gi * 6 + t) * 4 + g] = acs[t];
        }
        lds_signal(cnt + 5 + ch, lane);
        lds_wait2(cnt + 5 + ch, 2, cnt + 4, 4);
        f32x4 cso[6], oo[2][6], stv[2];
#pragma unroll
        for (int t = 0; t < 6; ++t) cso[t] = csl[((gi ^ 2) * 6 + t) * 4 + g];
#pragma unroll
        for (int m = 0; m < 2; ++m) {
          stv[m] = *(const f32x4*)(lds + L1_ST + (16 * (2 * kk + m) + c) * 16);
#pragma unroll
          for (int t = 0; t < 6; ++t) oo[m][t] = ro[(m * 6 + t) * 64 + lane];
        }
        SB();
#pragma unroll
        for (int t = 0; t < 6; ++t) acs[t] += cso[t];
#pragma unroll
        for (int m = 0; m < 2; ++m) {
          const int mt = 2 * kk + m;
          const f32x4 st = stv[m];
          float hv[24];
#pragma unroll
          for (int t = 0; t < 6; ++t) {
#pragma unroll
            for (int r = 0; r < 4; ++r)
              hv[6 * r + t] = __builtin_fmaf(-st[0], acs[t][r], acc[2 * kk + m][t][r] + oo[m][t][r]) * st[2];
          }
#pragma unroll
          for (int ck = 0; ck < 3; ++ck) {
            const int F0 = col0 + 24 * g + 8 * ck;
            u32x4 o;
#pragma unroll
            for (int e = 0; e < 4; ++e) {
              const s16x2 pv = __builtin_bit_cast(s16x2, pkbf(hv[8 * ck + 2 * e], hv[8 * ck + 2 * e + 1]));
              o[e] = __builtin_bit_cast(unsigned, __builtin_elementwise_max(pv, (s16x2){0, 0}));
            }
            st16<0>(p.ws + WS_H + ((size_t)(rt * 4 + mt) * 43 + (F0 >> 5)) * 1024 + (c + 16 * ((F0 >> 3) & 3)) * 16, o);
          }
        }
      };
      if (kh == 0) fin(IC<0>{}); else fin(IC<1>{});
      }
      STAMP_AT(0, 3);
    }
  } else {
    const int kw = w, chl = kw >> 1, nh = kw & 1;
    const int d = 2 * b + chl;
    char* tch = lds + L1_R + chl * KT_CH;
    unsigned short* th = (unsigned short*)(lds + L1_TH + chl * 1344);
    float a2, fr, wsc_inv;
    f32x2 W;
    bool grow;
    u32x4 xq[16];
    u32x2 w2r[8];
    const int blk = 4 * b + kw;
    const bool dow2 = blk < 344;
    {
      {
        const char* xr = (const char*)p.x + (size_t)(row0 + 16 * kw) * 1024 + lane * 16;
#pragma unroll
        for (int i = 0; i < 16; ++i) xq[i] = *(const u32x4*)(xr + i * 1024);
      }
      SB();
      const size_t o = (size_t)d * NS + 64 * nh + lane, o2 = o ^ 64;
      const float lg = bf2f(p.logdt[d]);
      const float are = bf2f(p.Are[o]), aim = bf2f(p.Aim[o]);
      const float bre = bf2f(p.Bre[o]), bim = bf2f(p.Bim[o]);
      const float cr = bf2f(p.Cre[o]), ci = bf2f(p.Cim[o]);
      const float are2 = bf2f(p.Are[o2]);
      const float bre2 = bf2f(p.Bre[o2]), bim2 = bf2f(p.Bim[o2]);
      const float cr2 = bf2f(p.Cre[o2]), ci2 = bf2f(p.Cim[o2]);
      if (dow2) {
        const int ct = blk / 43, ks = blk % 43;
        const unsigned short* wsrc = p.w2 + (size_t)(32 * ks + 8 * g) * DM + 64 * ct + 4 * c;
#pragma unroll
        for (int jj = 0; jj < 8; ++jj) w2r[jj] = *(const u32x2*)(wsrc + (size_t)jj * DM);
      }
      SB();
      if (lane == 0) __hip_atomic_fetch_add(cnt + 3, 1u, __ATOMIC_RELAXED, __HIP_MEMORY_SCOPE_WORKGROUP);
      SB();
      const float dt = rbf(exp_acc(lg));
      const float br = dt * bre, bi = dt * bim;
      a2 = (dt * are) * 1.4426950408889634f;
      fr = (dt * aim) * 0.15915494309189535f;
      W = (f32x2){cr * br - ci * bi, -(cr * bi + ci * br)};
      const float m1 = (__builtin_fabsf(cr) + __builtin_fabsf(ci)) * (__builtin_fabsf(bre) + __builtin_fabsf(bim));
      const float m2 = (__builtin_fabsf(cr2) + __builtin_fabsf(ci2)) * (__builtin_fabsf(bre2) + __builtin_fabsf(bim2));
      const float mw = wave_max_nonneg(__builtin_fmaxf(m1, m2)) * dt;
      const int eb = (int)(f2u(mw) >> 23);
      int ex = 136 - eb;
      ex = ex > 120 ? 120 : (ex < -120 ? -120 : ex);
      const float wsc = u2f((unsigned)(127 + ex) << 23);
      wsc_inv = u2f((unsigned)(127 - ex) << 23);
      W = (f32x2){W[0] * wsc, W[1] * wsc};
      const float gmax = __builtin_fmaxf(dt * are, dt * are2);
      grow = __builtin_amdgcn_ballot_w64(!(gmax * 2048.f < 2.f) || eb >= 250) != 0ull;
    }
    STAMP_AT(0, 1);
    auto cexpq = [&](float q) -> f32x2 {
      const float e = __builtin_amdgcn_exp2f(a2 * q);
      const float ph = __builtin_amdgcn_fractf(fr * q);
      return (f32x2){__builtin_amdgcn_cosf(ph) * e, __builtin_amdgcn_sinf(ph) * e};
    };
    auto cmul = [](f32x2 a, f32x2 bb) -> f32x2 {
      return (f32x2){__builtin_fmaf(a[0], bb[0], -(a[1] * bb[1])), __builtin_fmaf(a[0], bb[1], a[1] * bb[0])};
    };
    char* tw = tch + (64 * nh + lane) * 4;
    auto put = [&](int row, f32x2 v) {
      float v0 = v[0], v1 = v[1];
      asm("" : "+v"(v0), "+v"(v1));
      *(unsigned*)(tw + row * KT_P) = pkh(v0, v1);
    };
    auto xstore = [&]() {
#pragma unroll
      for (int i = 0; i < 16; ++i) *(u32x4*)(lds + L1_A + (16 * kw + i) * 1024 + ((lane ^ i) * 16)) = xq[i];
      lds_signal(cnt + 0, lane);
      if (dow2) {
        u32x4 f4[4];
#pragma unroll
        for (int q2 = 0; q2 < 4; ++q2) {
          f4[0][q2] = __builtin_amdgcn_perm(w2r[2 * q2 + 1][0], w2r[2 * q2][0], 0x05040100u);
          f4[1][q2] = __builtin_amdgcn_perm(w2r[2 * q2 + 1][0], w2r[2 * q2][0], 0x07060302u);
          f4[2][q2] = __builtin_amdgcn_perm(w2r[2 * q2 + 1][1], w2r[2 * q2][1], 0x05040100u);
          f4[3][q2] = __builtin_amdgcn_perm(w2r[2 * q2 + 1][1], w2r[2 * q2][1], 0x07060302u);
        }
        char* dst2 = p.ws + WS_W2F + (size_t)blk * 4096 + lane * 16;
#pragma unroll
        for (int t = 0; t < 4; ++t) st16<2>(dst2 + t * 1024, f4[t]);
      }
    };
    xstore();
    SB();
    if (!grow) {
      const f32x2 E1 = cexpq(1.f), E256 = cexpq(256.f), E8 = cexpq(8.f);
      f32x2 y0 = (f32x2){1.f, 0.f}, y1 = cexpq(64.f), y2 = cexpq(128.f), y3 = cexpq(192.f);
      const f32x2 C1 = {E1[0], -E1[1]}, CL = {E256[0], -E256[1]};
      const f32x2 C2 = cmul(C1, C1), C4 = cmul(C2, C2);
      f32x2 xs[21];
      xs[0] = W;
      xs[8] = cmul(W, CL);
      xs[1] = cmul(xs[0], C1);  xs[12] = cmul(xs[8], CL);  xs[9] = cmul(xs[8], C2);
      xs[2] = cmul(xs[1], C1);  xs[14] = cmul(xs[12], CL); xs[10] = cmul(xs[9], C2); xs[13] = cmul(xs[12], C4);
      xs[3] = cmul(xs[2], C1);  xs[16] = cmul(xs[14], CL); xs[11] = cmul(xs[10], C2); xs[15] = cmul(xs[14], C4);
      xs[4] = cmul(xs[0], C4);  xs[17] = cmul(xs[16], CL);
      xs[5] = cmul(xs[1], C4);  xs[18] = cmul(xs[17], CL);
      xs[6] = cmul(xs[2], C4);  xs[19] = cmul(xs[18], CL);
      xs[7] = cmul(xs[3], C4);  xs[20] = cmul(xs[19], CL);
#pragma unroll
      for (int i = 0; i < 21; ++i) put(i, xs[i]);
      put(21, y0); put(29, y1); put(37, y2); put(45, y3);
#pragma unroll
      for (int i = 1; i < 8; ++i) {
        y0 = cmul(y0, E8); y1 = cmul(y1, E8); y2 = cmul(y2, E8); y3 = cmul(y3, E8);
        put(21 + i, y0); put(29 + i, y1); put(37 + i, y2); put(45 + i, y3);
      }
    } else {
      *(f32x4*)(tch + (64 * nh + lane) * 16) = (f32x4){a2, fr, W[0], -W[1]};
    }
    lds_signal(cnt + 1 + chl, lane);
    STAMP_AT(0, 4);
    {
      lds_wait(cnt + 0, 4);
      const s16x8 onesf = {0x3f80, 0x3f80, 0x3f80, 0x3f80, 0x3f80, 0x3f80, 0x3f80, 0x3f80};
      f32x4 as0 = {0.f, 0.f, 0.f, 0.f}, aq0 = as0, as1 = as0, aq1 = as0;
      const char* ab2 = arow + kw * 16384;
      s16x8 xf[16];
#pragma unroll
      for (int s = 0; s < 16; ++s) xf[s] = *(const s16x8*)(ab2 + (gl16 ^ (unsigned)(s * 64)));
      SB();
#pragma unroll
      for (int s = 0; s < 16; s += 2) {
        as0 = __builtin_amdgcn_mfma_f32_16x16x32_bf16(onesf, xf[s], as0, 0, 0, 0);
        aq0 = __builtin_amdgcn_mfma_f32_16x16x32_bf16(xf[s], xf[s], aq0, 0, 0, 0);
        as1 = __builtin_amdgcn_mfma_f32_16x16x32_bf16(onesf, xf[s + 1], as1, 0, 0, 0);
        aq1 = __builtin_amdgcn_mfma_f32_16x16x32_bf16(xf[s + 1], xf[s + 1], aq1, 0, 0, 0);
      }
      const int r3 = c & 3;
      const f32x4 aq = aq0 + aq1;
      const float q = (r3 == 0) ? aq[0] : (r3 == 1) ? aq[1] : (r3 == 2) ? aq[2] : aq[3];
      const float mean = (as0[0] + as1[0]) * (1.f / DM);
      const float var = __builtin_fmaxf(q * (1.f / DM) - mean * mean, 0.f);
      const float mb = rbf(mean);
      const float dn = rbf(__builtin_sqrtf(rbf(rbf(var) + 1e-5f)));
      if (g == (c >> 2)) {
        const int r = 16 * kw + c;
        *(f32x4*)(lds + L1_ST + r * 16) = (f32x4){mb, dn, 1.f / dn, 0.f};
        if (cg == 0) *(f32x2*)(p.ws + WS_ST + (size_t)(row0 + r) * 8) = (f32x2){mb, dn};
      }
      lds_signal(cnt + 4, lane);
    }
    STAMP_AT(0, 8);
    lds_wait(cnt + 1 + chl, 2);
    f32x4 acc[2];
    acc[0] = (f32x4){0.f, 0.f, 0.f, 0.f}; acc[1] = acc[0];
    const int j = 16 * nh + c;
    if (!grow) {
      const char* fa = tch + c * KT_P + 16 * g;
      const char* fb = tch + (21 + j) * KT_P + 16 * g;
      f16x8 fA0[8], fA1[8], fB[8];
#pragma unroll
      for (int s = 0; s < 8; ++s) {
        fB[s] = *(const f16x8*)(fb + 64 * s);
        fA0[s] = *(const f16x8*)(fa + 64 * s);
        fA1[s] = *(const f16x8*)(fa + 16 * KT_P + 64 * s);
      }
      SB();
#pragma unroll
      for (int s = 0; s < 8; ++s) {
        acc[0] = __builtin_amdgcn_mfma_f32_16x16x32_f16(fA0[s], fB[s], acc[0], 0, 0, 0);
        acc[1] = __builtin_amdgcn_mfma_f32_16x16x32_f16(fA1[s], fB[s], acc[1], 0, 0, 0);
      }
    } else {
      for (int e = 0; e < 8; ++e) {
        const int row = 16 * (e >> 2) + 4 * g + (e & 3);
        const int qx = row < 8 ? row : row < 12 ? 256 + 2 * (row - 8) : row < 16 ? 512 + 256 * ((row - 12) >> 1) + 4 * (row & 1)
                                                                   : row < 21 ? 1024 + 256 * (row - 16) : 0;
        const float q = (float)(qx + 8 * j);
        float sacc = 0.f;
        for (int n = 0; n < 128; ++n) {
          const f32x4 pr = *(const f32x4*)(tch + n * 16);
          const float ph = __builtin_amdgcn_fractf(pr[1] * q);
          sacc += __builtin_amdgcn_exp2f(pr[0] * q) * (pr[2] * __builtin_amdgcn_cosf(ph) - pr[3] * __builtin_amdgcn_sinf(ph));
        }
        if (e < 4) acc[0][e & 3] = sacc; else acc[1][e & 3] = sacc;
      }
    }
    STAMP_AT(0, 5);
    {
      const bool v1 = (g == 0), v2 = (g == 1 && j == 0);
      float mx = __builtin_fmaxf(__builtin_fmaxf(__builtin_fabsf(acc[0][0]), __builtin_fabsf(acc[0][1])),
                                 __builtin_fmaxf(__builtin_fabsf(acc[0][2]), __builtin_fabsf(acc[0][3])));
      const float m1 = __builtin_fmaxf(__builtin_fmaxf(__builtin_fabsf(acc[1][1]), __builtin_fabsf(acc[1][2])), __builtin_fabsf(acc[1][3]));
      mx = __builtin_fmaxf(mx, (v1 || v2) ? __builtin_fabsf(acc[1][0]) : 0.f);
      mx = __builtin_fmaxf(mx, v1 ? m1 : 0.f);
      mx = wave_max_nonneg(mx);
      float* mxl = (float*)(lds + L1_TH + 2688) + kw;
      if (lane == 0) *mxl = mx;
      lds_signal(cnt + 8 + chl, lane);
    {
      lds_wait(cnt + 4, 4);
      const int q4 = c >> 2, pp = c & 3;
      f32x4 st[16];
#pragma unroll
      for (int r = 0; r < 16; ++r) st[r] = *(const f32x4*)(lds + L1_ST + (16 * g + r) * 16);
      const int ck = 8 * cg + 2 * kw + (pp >> 1);
      unsigned ov[8];
      u32x2 vv[4];
#pragma unroll
      for (int k = 0; k < 4; ++k) {
        const int r16 = 4 * k + q4;
        const char* a0 = lds + L1_A + (16 * g + r16) * 1024 + ((ck ^ r16) * 16) + 8 * (pp & 1);
        vv[k] = __builtin_bit_cast(u32x2, __builtin_amdgcn_ds_read_tr16_b64_v4i16((LAS s16x4*)a0));
      }
      SB();
#pragma unroll
      for (int k = 0; k < 4; ++k) {
        const u32x2 v = vv[k];
#pragma unroll
        for (int e = 0; e < 2; ++e) {
          const unsigned ve = v[e];
          const f32x4 s0 = st[4 * k + 2 * e], s1 = st[4 * k + 2 * e + 1];
          const float t0 = rbf(bflo(ve) - s0[0]), t1 = rbf(bfhi(ve) - s1[0]);
          float q0 = t0 * s0[2], q1 = t1 * s1[2];
          q0 = __builtin_fmaf(__builtin_fmaf(-q0, s0[1], t0), s0[2], q0);
          q1 = __builtin_fmaf(__builtin_fmaf(-q1, s1[1], t1), s1[2], q1);
          const unsigned xb = pkbf(q0, q1);
          ov[2 * k + e] = pkh(bflo(xb), bfhi(xb));
        }
      }
      unsigned short* dst = (unsigned short*)(p.ws + WS_XNT) + (size_t)(64 * cg + 16 * kw + c) * SL + row0 + 16 * g;
      st16<2>(dst, (u32x4){ov[0], ov[1], ov[2], ov[3]});
      st16<2>(dst + 8, (u32x4){ov[4], ov[5], ov[6], ov[7]});
    }
    {
      char* ydst = p.ws + WS_YT + (size_t)b * 8192 + kw * 2048 + lane * 16;
      const u32x4 sent = {0xffffffffu, 0xffffffffu, 0xffffffffu, 0xffffffffu};
      st16<2>(ydst, sent);
      st16<2>(ydst + 1024, sent);
    }
      lds_wait(cnt + 8 + chl, 2);
      mx = __builtin_fmaxf(mx, ((const float*)(lds + L1_TH + 2688))[kw ^ 1]);
      const int eb = (int)(f2u(mx) >> 23);
      int ex = 140 - eb;
      ex = (eb == 0 || eb == 255) ? 0 : (ex > 120 ? 120 : ex);
      const float sc = u2f((unsigned)(127 + ex) << 23);
      if (lane == 0 && nh == 0) ((float*)(p.ws + WS_KS))[d] = u2f((unsigned)(127 - ex) << 23) * wsc_inv;
      const int b0 = (g < 2 ? 4 * g + 8 * j : g == 2 ? 256 + 4 * j : 384 + 2 * j);
      *(unsigned*)(th + b0) = pkh(acc[0][0] * sc, acc[0][1] * sc);
      *(unsigned*)(th + b0 + (g == 3 ? 64 : 2)) = pkh(acc[0][2] * sc, acc[0][3] * sc);
      const unsigned p01 = pkh(acc[1][0] * sc, acc[1][1] * sc), p23 = pkh(acc[1][2] * sc, acc[1][3] * sc);
      if (v1) {
        th[512 + j] = (unsigned short)p01; th[544 + j] = (unsigned short)(p01 >> 16);
        th[576 + j] = (unsigned short)p23; th[608 + j] = (unsigned short)(p23 >> 16);
      }
      if (v2) th[640] = (unsigned short)p01;
      lds_signal(cnt + 10 + chl, lane);
    }
    STAMP_AT(0, 6);
    STAMP_AT(0, 7);
    {
      lds_wait(cnt + 10 + chl, 2);
      const unsigned short* tu = th;
      unsigned short* kr = (unsigned short*)(p.ws + WS_KR) + (size_t)d * KRL;
      if (nh == 0) {
#pragma unroll
        for (int k = 0; k < 2; ++k) {
          const int ck = lane + 64 * k, M = 256 - ck;
          const unsigned a = tu[384 + M], bb = tu[383 + M];
          const unsigned aa = a | (a << 16), b2 = bb | (bb << 16);
          const unsigned mid = ((M & 1) ? bb : a) | (bb << 16);
          st16<2>(kr + 8 * ck, (u32x4){aa, aa, mid, b2});
        }
        if (lane < 4) st16<2>(kr + 8 * (256 + lane), (u32x4){lane == 0 ? (unsigned)tu[0] : 0u, 0u, 0u, 0u});
      } else {
        {
          const int ck = 128 + lane, M = 256 - ck;
          const unsigned a = tu[256 + 2 * M], bb = tu[255 + 2 * M], cc = tu[254 + 2 * M];
          st16<2>(kr + 8 * ck, (u32x4){a | (a << 16), a | (bb << 16), bb | (bb << 16), cc | (cc << 16)});
        }
        {
          const int ck = 192 + lane, M = 256 - ck;
          const bool s1 = lane < 32;
          const int base = s1 ? 128 + 4 * M : 8 * M;
          const int o1[8] = {0, 0, 1, 2, 2, 2, 3, 4};
          unsigned hv[8];
#pragma unroll
          for (int e = 0; e < 8; ++e) hv[e] = tu[base - (s1 ? o1[e] : e)];
          st16<2>(kr + 8 * ck, (u32x4){hv[0] | (hv[1] << 16), hv[2] | (hv[3] << 16), hv[4] | (hv[5] << 16), hv[6] | (hv[7] << 16)});
        }
      }
    }
    STAMP_AT(0, 3);
    STAMP_AT(0, 9);
  }
  STAMP_FLUSH(0);
}

#define GAS __attribute__((address_space(1)))
constexpr int KCS = KRL * 2;
constexpr int CV_CH = 8 * KCS + 4608;
constexpr int L2_CR = 2 * CV_CH;
constexpr int L2_GR = L2_CR + 16384;
constexpr int L2_Y = L2_GR + 49152;
constexpr int L2_T1 = L2_Y + 64 * 136;
constexpr int L2_C = L2_T1 + 8192;
constexpr int L2_SIZE = L2_C + 64;

__global__ void __launch_bounds__(512, 2) k_phase2(Params p) {
  __shared__ __attribute__((aligned(16))) char lds[L2_SIZE];
  STAMP_DECL
  const int tid = TIDX, lane = tid & 63;
  const int w = __builtin_amdgcn_readfirstlane(tid >> 6);
  const int b = BIDX;
  LAS unsigned* cnt = (LAS unsigned*)(LAS char*)(lds + L2_C);
  if (tid < 16) cnt[tid] = 0u;
  wg_sync();
  STAMP_AT(1, 0);
  const int xcd = b & 7, idx = b >> 3;
  const int rt = xcd * 4 + (idx >> 3), ct = idx & 7;
  const int row0 = rt * 64, col0 = ct * 64;
  const int c = lane & 15, g = lane >> 4;
  GAS unsigned* flags = (GAS unsigned*)(p.ws + WS_FL);
  if (w < 4) {
    const int chl = w >> 1, hf = w & 1;
    const int d = 2 * b + chl;
    char* kl = lds + chl * CV_CH;
    char* ul = kl + 8 * KCS;
    const int erow = row0 + 16 * w + 4 * g;
    u32x2 xv[4];
    f32x4 st01, st23;
    u32x2 dk;
    float isc;
    {
      const int t2 = tid & 127;
      const u32x4* ksrc = (const u32x4*)(p.ws + WS_KR + (size_t)d * KCS);
      const u32x4* usrc = (const u32x4*)(p.ws + WS_XNT + (size_t)d * SL * 2);
      u32x4 kc[3], kp[3];
#pragma unroll
      for (int k = 0; k < 3; ++k) {
        int ci = t2 + 128 * k;
        ci = ci < 260 ? ci : 259;
        kc[k] = ksrc[ci];
        kp[k] = ksrc[ci > 0 ? ci - 1 : 0];
      }
      u32x4 uv0 = usrc[t2], uv1 = usrc[t2 + 128];
      SB();
#pragma unroll
      for (int r = 0; r < 4; ++r) xv[r] = *(const u32x2*)(p.x + (size_t)(erow + r) * DM + col0 + 4 * c);
      st01 = *(const f32x4*)(p.ws + WS_ST + (size_t)erow * 8);
      st23 = *(const f32x4*)(p.ws + WS_ST + (size_t)erow * 8 + 16);
      dk = *(const u32x2*)(p.Dsk + col0 + 4 * c);
      isc = ((const float*)(p.ws + WS_KS))[d];
      SB();
      const u32x4 z = {0u, 0u, 0u, 0u};
      if (t2 < 32) *(u32x4*)(ul + t2 * 16) = z;
#pragma unroll
      for (int k = 0; k < 3; ++k) {
        const int ci = t2 + 128 * k;
        if (ci < 260) {
          unsigned wd[8] = {kp[k][0], kp[k][1], kp[k][2], kp[k][3], kc[k][0], kc[k][1], kc[k][2], kc[k][3]};
          unsigned od[7];
#pragma unroll
          for (int j = 0; j < 7; ++j) od[j] = __builtin_amdgcn_alignbit(wd[j + 1], wd[j], 16);
          char* dst = kl + ci * 16;
#pragma unroll
          for (int sg = 0; sg < 8; ++sg) {
            u32x4 o;
            if ((sg & 1) == 0) { const int s0 = (8 - sg) >> 1; o = (u32x4){wd[s0], wd[s0 + 1], wd[s0 + 2], wd[s0 + 3]}; }
            else { const int s0 = (7 - sg) >> 1; o = (u32x4){od[s0], od[s0 + 1], od[s0 + 2], od[s0 + 3]}; }
            *(u32x4*)(dst + sg * KCS) = o;
          }
        }
      }
      *(u32x4*)(ul + 512 + t2 * 16) = uv0;
      *(u32x4*)(ul + 512 + (t2 + 128) * 16) = uv1;
    }
    lds_signal(cnt + chl, lane);
    lds_wait(cnt + chl, 2);
    STAMP_AT(1, 1);
    const int a = c;
    f32x4 acc[8];
#pragma unroll
    for (int r = 0; r < 8; ++r) acc[r] = (f32x4){0.f, 0.f, 0.f, 0.f};
    const char* ab = kl + (a & 7) * KCS + 2 * (2032 + 8 * g - 8 * (a >> 3)) - 64 * 4 * hf;
    const char* bb = ul + 2 * (240 + 16 * a + 8 * g) - 64 * 4 * hf;
    f16x8 bf[2][8], af[2][8];
#pragma unroll
    for (int t = 0; t < 8; ++t) { bf[0][t] = *(const f16x8*)(bb + 512 * t); af[0][t] = *(const f16x8*)(ab - 512 * t); }
#pragma unroll
    for (int i = 0; i < 4; ++i) {
      if (i + 1 < 4) {
#pragma unroll
        for (int t = 0; t < 8; ++t) {
          bf[(i + 1) & 1][t] = *(const f16x8*)(bb - 64 * (i + 1) + 512 * t);
          af[(i + 1) & 1][t] = *(const f16x8*)(ab - 64 * (i + 1) - 512 * t);
        }
      }
      SB();
#pragma unroll
      for (int v = 0; v < 8; ++v)
#pragma unroll
        for (int t = v; t < 8; ++t)
          acc[t] = __builtin_amdgcn_mfma_f32_16x16x32_f16(af[i & 1][v], bf[i & 1][t - v], acc[t], 0, 0, 0);
      SB();
    }
    STAMP_AT(1, 2);
    f32x4* red = (f32x4*)(lds + L2_CR + chl * 8192);
    f32x4 mine[4];
    if (hf) {
#pragma unroll
      for (int r = 0; r < 4; ++r) { red[(4 + r) * 64 + lane] = acc[r]; mine[r] = acc[4 + r]; }
    } else {
#pragma unroll
      for (int r = 0; r < 4; ++r) { red[r * 64 + lane] = acc[4 + r]; mine[r] = acc[r]; }
    }
    lds_signal(cnt + 2 + chl, lane);
    lds_wait(cnt + 2 + chl, 2);
    {
      unsigned pk[4][2];
#pragma unroll
      for (int r = 0; r < 4; ++r) {
        const f32x4 v = (mine[r] + red[(4 * (1 - hf) + r) * 64 + lane]) * isc;
        pk[r][0] = pkbf(v[0], v[1]);
        pk[r][1] = pkbf(v[2], v[3]);
      }
      const auto rsrc = __builtin_amdgcn_make_buffer_rsrc((void*)(p.ws + WS_YT), 0, DM * SL * 2, 0x00020000);
#pragma unroll
      for (int r = 0; r < 2; ++r) {
        auto s0 = __builtin_amdgcn_permlane16_swap(pk[r][0], pk[r + 2][0], false, false);
        auto s1 = __builtin_amdgcn_permlane16_swap(pk[r][1], pk[r + 2][1], false, false);
        const unsigned a0 = s0[0], b0 = s0[1], a1 = s1[0], b1 = s1[1];
        const u32x4 o = {a0, a1, b0, b1};
        const int tile = r + 2 * (g & 1);
        const int off = (d * SL + 1024 * hf + 256 * tile + 16 * a + 8 * (g >> 1)) * 2;
        __builtin_amdgcn_raw_buffer_store_b128(o, rsrc, off, 0, 16);
      }
    }
    STAMP_AT(1, 3);
    {
      const int ch = 16 * w + (lane >> 2), k0 = (lane & 3) * 2;
      const auto rsrc = __builtin_amdgcn_make_buffer_rsrc((void*)(p.ws + WS_YT), 0, DM * SL * 2, 0x00020000);
      const int off = ((col0 + ch) * SL + row0) * 2 + k0 * 16;
      float xf[4][4], xd[4][4];
      {
        const float dkf[4] = {bflo(dk[0]), bfhi(dk[0]), bflo(dk[1]), bfhi(dk[1])};
#pragma unroll
        for (int r = 0; r < 4; ++r) {
          xf[r][0] = bflo(xv[r][0]); xf[r][1] = bfhi(xv[r][0]); xf[r][2] = bflo(xv[r][1]); xf[r][3] = bfhi(xv[r][1]);
          const float mb = (r == 0) ? st01[0] : (r == 1) ? st01[2] : (r == 2) ? st23[0] : st23[2];
          const float den = (r == 0) ? st01[1] : (r == 1) ? st01[3] : (r == 2) ? st23[1] : st23[3];
          const float rc = 1.f / den;
#pragma unroll
          for (int t = 0; t < 4; ++t) {
            const float a0 = rbf(xf[r][t] - mb);
            float q = a0 * rc;
            q = __builtin_fmaf(__builtin_fmaf(-q, den, a0), rc, q);
            xd[r][t] = rbf(rbf(q) * dkf[t]);
            asm volatile("" : "+v"(xd[r][t]), "+v"(xf[r][t]));
          }
        }
      }
      u32x4 v0, v1;
      unsigned spins = 0;
      lds_wait(cnt + 7, 4);
      while (true) {
        v0 = __builtin_amdgcn_raw_buffer_load_b128(rsrc, off, 0, 16);
        v1 = __builtin_amdgcn_raw_buffer_load_b128(rsrc, off + 16, 0, 16);
        bool bad = false;
#pragma unroll
        for (int e = 0; e < 4; ++e) {
          const unsigned a0 = v0[e], a1 = v1[e];
          bad |= ((a0 & 0xffffu) == 0xffffu) | ((a0 >> 16) == 0xffffu) | ((a1 & 0xffffu) == 0xffffu) | ((a1 >> 16) == 0xffffu);
        }
        if (__builtin_amdgcn_ballot_w64(bad) == 0ull) break;
        if (++spins > (1u << 18)) break;
        __builtin_amdgcn_s_sleep(4);
      }
      STAMP_AT(1, 4);
      char* yd = lds + L2_Y + ch * 136 + k0 * 16;
      *(u32x2*)yd = (u32x2){v0[0], v0[1]}; *(u32x2*)(yd + 8) = (u32x2){v0[2], v0[3]};
      *(u32x2*)(yd + 16) = (u32x2){v1[0], v1[1]}; *(u32x2*)(yd + 24) = (u32x2){v1[2], v1[3]};
      lds_signal(cnt + 4, lane);
      lds_wait(cnt + 4, 4);
      u32x2 yv[4];
#pragma unroll
      for (int t = 0; t < 4; ++t) yv[t] = *(const u32x2*)(lds + L2_Y + (4 * c + t) * 136 + (16 * w + 4 * g) * 2);
#pragma unroll
      for (int r = 0; r < 4; ++r) {
        float o[4];
#pragma unroll
        for (int t = 0; t < 4; ++t) {
          const unsigned yw = yv[t][r >> 1];
          const float y = (r & 1) ? bfhi(yw) : bflo(yw);
          o[t] = xf[r][t] + rbf(y + xd[r][t]);
        }
        *(u32x2*)(lds + L2_T1 + ((w * 4 + r) * 64 + lane) * 8) = (u32x2){pkbf(o[0], o[1]), pkbf(o[2], o[3])};
      }
    }
    lds_signal(cnt + 8 + w, lane);
    STAMP_AT(1, 5);
  } else {
    const int gw = w - 4;
    const int s0 = 11 * gw;
    const int ns = (gw == 3) ? 10 : 11;
    const char* hb = p.ws + WS_H + ((size_t)(rt * 4) * 43 + s0) * 1024 + lane * 16;
    const char* wb = p.ws + WS_W2F + ((size_t)ct * 43 + s0) * 4096 + lane * 16;
    constexpr int P = 4;
    u32x4 ar[P][4];
    u32x4 br[P][4];
    const int rot = 0;

    auto issue = [&](int s) {
      int ks = s + rot;
      ks = ks >= ns ? ks - ns : ks;
#pragma unroll
      for (int mt = 0; mt < 4; ++mt) ar[s % P][mt] = *(const u32x4*)(hb + (size_t)mt * 43 * 1024 + ks * 1024);
#pragma unroll
      for (int j = 0; j < 4; ++j) br[s % P][j] = *(const u32x4*)(wb + ks * 4096 + j * 1024);
    };
#pragma unroll
    for (int s = 0; s < P; ++s) issue(s);
    SB();
    f32x4 acc[4][4];
#pragma unroll
    for (int i = 0; i < 4; ++i)
#pragma unroll
      for (int t = 0; t < 4; ++t) acc[i][t] = (f32x4){0.f, 0.f, 0.f, 0.f};
#pragma unroll
    for (int s = 0; s < 11; ++s) {
      if (s < ns) {
#pragma unroll
        for (int mt = 0; mt < 4; ++mt)
#pragma unroll
          for (int t = 0; t < 4; ++t)
            acc[mt][t] = __builtin_amdgcn_mfma_f32_16x16x32_bf16(__builtin_bit_cast(s16x8, ar[s % P][mt]),
                                                                 __builtin_bit_cast(s16x8, br[s % P][t]), acc[mt][t], 0, 0, 0);
        SB();
        if (s + P < 11 && s + P < ns) issue(s + P);
        if (s == 6 && lane == 0) __hip_atomic_fetch_add(cnt + 7, 1u, __ATOMIC_RELAXED, __HIP_MEMORY_SCOPE_WORKGROUP);
        SB();
      }
    }

    const int erow = row0 + 16 * gw + 4 * g;
    STAMP_AT(1, 1);
    f32x4* red = (f32x4*)(lds + L2_GR);
    f32x4 f[4];
#pragma unroll
    for (int mt = 0; mt < 4; ++mt) {
      if (mt != gw) {
        const int slot = (gw > mt) ? (gw - 1) : gw;
#pragma unroll
        for (int t = 0; t < 4; ++t) red[((mt * 3 + slot) * 4 + t) * 64 + lane] = acc[mt][t];
      } else {
#pragma unroll
        for (int t = 0; t < 4; ++t) f[t] = acc[mt][t];
      }
    }
    lds_signal(cnt + 6, lane);
    lds_wait(cnt + 6, 4);
    STAMP_AT(1, 2);
    f32x4 pr[4][3];
#pragma unroll
    for (int t = 0; t < 4; ++t)
#pragma unroll
      for (int sl = 0; sl < 3; ++sl) pr[t][sl] = red[((gw * 3 + sl) * 4 + t) * 64 + lane];
    SB();
#pragma unroll
    for (int t = 0; t < 4; ++t) {
      f[t] += (pr[t][0] + pr[t][1]) + pr[t][2];
#pragma unroll
      for (int r = 0; r < 4; ++r) f[t][r] = rbf(f[t][r]);
      asm volatile("" : "+v"(f[t]));
    }
    lds_wait(cnt + 8 + gw, 1);
    STAMP_AT(1, 3);
#pragma unroll
    for (int r = 0; r < 4; ++r) {
      const u32x2 tv = *(const u32x2*)(lds + L2_T1 + ((gw * 4 + r) * 64 + lane) * 8);
      const unsigned t0 = tv[0], t1 = tv[1];
      u32x2 ov;
      ov[0] = pkbf(bflo(t0) + f[0][r], bfhi(t0) + f[1][r]);
      ov[1] = pkbf(bflo(t1) + f[2][r], bfhi(t1) + f[3][r]);
      asm volatile("global_store_dwordx2 %0, %1, off sc1" :: "v"(p.out + (size_t)(erow + r) * DM + col0 + 4 * c), "v"(ov) : "memory");
    }
    STAMP_AT(1, 4);
  }
  STAMP_FLUSH(1);
}

#if defined(__HIP_DEVICE_COMPILE__)
#pragma clang attribute pop
#endif

extern "C" void kernel_launch(void* const* d_in, const int* in_sizes, int n_in,
                              void* d_out, int out_size, void* d_ws, size_t ws_size,
                              hipStream_t stream) {
  Params p;
  p.x = (const unsigned short*)d_in[0];
  p.Are = (const unsigned short*)d_in[1];
  p.Aim = (const unsigned short*)d_in[2];
  p.Bre = (const unsigned short*)d_in[3];
  p.Bim = (const unsigned short*)d_in[4];
  p.Cre = (const unsigned short*)d_in[5];
  p.Cim = (const unsigned short*)d_in[6];
  p.logdt = (const unsigned short*)d_in[7];
  p.Dsk = (const unsigned short*)d_in[8];
  p.w1 = (const unsigned short*)d_in[10];
  p.w2 = (const unsigned short*)d_in[11];
  p.out = (unsigned short*)d_out;
  p.ws = (char*)d_ws;
  k_phase1<<<256, 512, 0, stream>>>(p);
  k_phase2<<<256, 512, 0, stream>>>(p);
}
```

```cpp
#include <hip/hip_runtime.h>
#include <hip/hip_bf16.h>
#include <stdint.h>

typedef __attribute__((ext_vector_type(8))) short s16x8;
typedef __attribute__((ext_vector_type(4))) short s16x4;
typedef __attribute__((ext_vector_type(8))) _Float16 f16x8;
typedef __attribute__((ext_vector_type(4))) float f32x4;
typedef __attribute__((ext_vector_type(2))) float f32x2;
typedef __attribute__((ext_vector_type(16))) float f32x16;
typedef __attribute__((ext_vector_type(4))) unsigned u32x4;
typedef __attribute__((ext_vector_type(2))) unsigned u32x2;
typedef __attribute__((ext_vector_type(2))) __bf16 bf16x2_t;
typedef __attribute__((ext_vector_type(2))) _Float16 f16x2_t;
#define LAS __attribute__((address_space(3)))

constexpr int SL = 2048;
constexpr int DM = 512;
constexpr int NS = 128;
constexpr int DFF = 1376;
constexpr int KRL = 2080;

constexpr size_t WS_H = 0;
constexpr size_t WS_XNT = WS_H + (size_t)128 * 43 * 1024;
constexpr size_t WS_KR = WS_XNT + (size_t)DM * SL * 2;
constexpr size_t WS_YT = WS_KR + (size_t)DM * KRL * 2;
constexpr size_t WS_ST = WS_YT + (size_t)DM * SL * 2;
constexpr size_t WS_KS = WS_ST + (size_t)SL * 8;
constexpr size_t WS_FL = WS_KS + 4096;
constexpr size_t WS_W2F = WS_FL + 32768;

#if defined(__HIP_DEVICE_COMPILE__)
#pragma clang attribute push(__attribute__((target("no-packed-fp32-ops"))), apply_to = function)
#endif
__device__ __forceinline__ float u2f(unsigned u) { return __builtin_bit_cast(float, u); }
__device__ __forceinline__ unsigned f2u(float f) { return __builtin_bit_cast(unsigned, f); }
__device__ __forceinline__ float shx(float v, int lane, int m) {
  return u2f((unsigned)__builtin_amdgcn_ds_bpermute((lane ^ m) << 2, (int)f2u(v)));
}
__device__ __forceinline__ void wg_sync() {
  __builtin_amdgcn_fence(__ATOMIC_RELEASE, "workgroup");
  __builtin_amdgcn_s_barrier();
  __builtin_amdgcn_fence(__ATOMIC_ACQUIRE, "workgroup");
}
__device__ __forceinline__ float exp_acc(float x) {
  const float L2E_HI = 1.44269502162933349609375f, L2E_LO = 1.925963033500011e-8f;
  const float hi = x * L2E_HI;
  const float lo = __builtin_fmaf(x, L2E_LO, __builtin_fmaf(x, L2E_HI, -hi));
  const float e = __builtin_amdgcn_exp2f(hi);
  return __builtin_fmaf(e, lo * 0.6931471805599453f, e);
}
#define TIDX ((int)__builtin_amdgcn_workitem_id_x())
#define BIDX ((int)__builtin_amdgcn_workgroup_id_x())
__device__ __forceinline__ float bf2f(unsigned h) { return u2f(h << 16); }
__device__ __forceinline__ float bflo(unsigned u) { return u2f(u << 16); }
__device__ __forceinline__ float bfhi(unsigned u) { return u2f(u & 0xffff0000u); }
__device__ __forceinline__ f32x2 bfup(unsigned u) { return (f32x2){bflo(u), bfhi(u)}; }
__device__ __forceinline__ unsigned pkbf(float lo, float hi) {
  bf16x2_t r = {(__bf16)lo, (__bf16)hi};
  return __builtin_bit_cast(unsigned, r);
}
__device__ __forceinline__ unsigned pkbf2(f32x2 v) { return pkbf(v[0], v[1]); }
__device__ __forceinline__ float rbf(float x) { return bflo(pkbf(x, 0.f)); }
__device__ __forceinline__ unsigned pkh(float lo, float hi) {
  f16x2_t r = {(_Float16)lo, (_Float16)hi};
  return __builtin_bit_cast(unsigned, r);
}

#define STAMP_DECL
#define STAMP_AT(kid, i) do {} while (0)
#define STAMP_FLUSH(kid) do {} while (0)
#define SB() __builtin_amdgcn_sched_barrier(0)
#define WAITV(n) asm volatile("s_waitcnt vmcnt(%0)" ::"n"(n) : "memory")
#define WAITL() asm volatile("s_waitcnt lgkmcnt(0)" ::: "memory")

struct Params {
  const unsigned short* x; const unsigned short* Are; const unsigned short* Aim;
  const unsigned short* Bre; const unsigned short* Bim; const unsigned short* Cre; const unsigned short* Cim;
  const unsigned short* logdt; const unsigned short* Dsk; const unsigned short* w1; const unsigned short* w2;
  unsigned short* out; char* ws;
};

__device__ __forceinline__ unsigned lds_arrive(LAS unsigned* cnt, int lane) {
  WAITL();
  unsigned old = 0;
  if (lane == 0) old = __hip_atomic_fetch_add(cnt, 1u, __ATOMIC_RELAXED, __HIP_MEMORY_SCOPE_WORKGROUP);
  return __builtin_amdgcn_readfirstlane(old);
}
__device__ __forceinline__ void lds_wait(LAS unsigned* cnt, unsigned target) {
  while (__builtin_amdgcn_readfirstlane(__hip_atomic_load(cnt, __ATOMIC_RELAXED, __HIP_MEMORY_SCOPE_WORKGROUP)) < target)
    __builtin_amdgcn_s_sleep(1);
  asm volatile("" ::: "memory");
}
__device__ __forceinline__ void lds_wait2(LAS unsigned* c0, unsigned t0, LAS unsigned* c1, unsigned t1) {
  while (true) {
    const unsigned a = __hip_atomic_load(c0, __ATOMIC_RELAXED, __HIP_MEMORY_SCOPE_WORKGROUP);
    const unsigned b = __hip_atomic_load(c1, __ATOMIC_RELAXED, __HIP_MEMORY_SCOPE_WORKGROUP);
    if (__builtin_amdgcn_readfirstlane(a) >= t0 && __builtin_amdgcn_readfirstlane(b) >= t1) break;
    __builtin_amdgcn_s_sleep(1);
  }
  asm volatile("" ::: "memory");
}
__device__ __forceinline__ void lds_signal(LAS unsigned* cnt, int lane) {
  WAITL();
  if (lane == 0) __hip_atomic_fetch_add(cnt, 1u, __ATOMIC_RELAXED, __HIP_MEMORY_SCOPE_WORKGROUP);
}
__device__ __forceinline__ void glds16(const void* gsrc, unsigned lds_dst) {
  unsigned keep;
  asm volatile("s_mov_b32 %0, m0\n\ts_mov_b32 m0, %2\n\ts_nop 0\n\tglobal_load_lds_dwordx4 %1, off\n\ts_mov_b32 m0, %0"
               : "=&s"(keep) : "v"(gsrc), "s"(lds_dst) : "memory");
}

__device__ __forceinline__ void glds16s(const void* sbase, unsigned voff, unsigned lds_dst) {
  unsigned keep;
  asm volatile("s_mov_b32 %0, m0\n\ts_mov_b32 m0, %3\n\ts_nop 0\n\tglobal_load_lds_dwordx4 %1, %2\n\ts_mov_b32 m0, %0"
               : "=&s"(keep) : "v"(voff), "s"(sbase), "s"(lds_dst) : "memory");
}

template <int MODE> __device__ __forceinline__ void st16(void* ptr, u32x4 v) {
  if (MODE == 0) *(u32x4*)ptr = v;
  else if (MODE == 1) __builtin_nontemporal_store(v, (u32x4*)ptr);
  else if (MODE == 2) asm volatile("global_store_dwordx4 %0, %1, off sc1\n\ts_nop 1" :: "v"(ptr), "v"(v) : "memory");
  else asm volatile("global_store_dwordx4 %0, %1, off sc0 sc1\n\ts_nop 1" :: "v"(ptr), "v"(v) : "memory");
}
typedef __attribute__((ext_vector_type(2))) short s16x2;
template <int V> struct IC { static constexpr int v = V; };
typedef __attribute__((ext_vector_type(3))) unsigned u32x3;
typedef u32x3 __attribute__((aligned(4))) u32x3_a4;
constexpr unsigned CODE1_SLICE = 768, CODE2_SLICE = 352;
constexpr int PB = 3;
constexpr int KT_P = 528;
constexpr int KT_CH = 54 * KT_P;
constexpr int L1_A = 0;
constexpr int L1_R = L1_A + 65536;
constexpr int L1_ST = L1_R + 2 * KT_CH;
constexpr int L1_CS = L1_ST + 1024;
constexpr int L1_C = L1_CS + 1536;
constexpr int L1_TH = L1_C + 64;
constexpr int L1_SIZE = L1_TH + 2688 + 16;

__device__ __forceinline__ void bsplit(f32x2 v, unsigned& hi, unsigned& lo) {
  hi = pkbf2(v);
  asm("" : "+v"(hi));
  lo = pkbf(v[0] - bflo(hi), v[1] - bfhi(hi));
}
template <int CTRL> __device__ __forceinline__ float dpp_max(float v) {
  const int t = __builtin_amdgcn_update_dpp(0, (int)f2u(v), CTRL, 0xf, 0xf, true);
  return __builtin_fmaxf(v, u2f((unsigned)t));
}
__device__ __forceinline__ float wave_max_nonneg(float v) {
  v = dpp_max<0x140>(dpp_max<0x141>(dpp_max<0x4E>(dpp_max<0xB1>(v))));
  const float a = u2f((unsigned)__builtin_amdgcn_readlane((int)f2u(v), 0));
  const float b = u2f((unsigned)__builtin_amdgcn_readlane((int)f2u(v), 16));
  const float c = u2f((unsigned)__builtin_amdgcn_readlane((int)f2u(v), 32));
  const float d = u2f((unsigned)__builtin_amdgcn_readlane((int)f2u(v), 48));
  return __builtin_fmaxf(__builtin_fmaxf(a, b), __builtin_fmaxf(c, d));
}

__global__ void __launch_bounds__(512, 2) k_phase1(Params p) {
  __shared__ __attribute__((aligned(16))) char lds[L1_SIZE];
  STAMP_DECL
  const int tid = TIDX, lane = tid & 63;
  const int w = __builtin_amdgcn_readfirstlane(tid >> 6);
  const int b = BIDX;
  LAS unsigned* cnt = (LAS unsigned*)(LAS char*)(lds + L1_C);
  if (tid < 16) cnt[tid] = 0u;
  wg_sync();
  STAMP_AT(0, 0);
  const int xcd = b & 7, idx = b >> 3;
  const int rt = (xcd >> 1) * 8 + (idx >> 2), cg = (xcd & 1) * 4 + (idx & 3);
  const int row0 = rt * 64;
  const int c = lane & 15, g = lane >> 4;
  const unsigned gl16 = (unsigned)((g ^ c) * 16);
  const char* arow = lds + L1_A + c * 1024;
  if (w >= 4) {
    const int gi = w - 4, ch = gi & 1, kh = gi >> 1;
    const int col0 = (cg == 7) ? 1280 : cg * 192 + 96 * ch;
    const bool gw = (cg < 7) || (ch == 0);
    const int rot = rt & 7;
    const unsigned lbase = (unsigned)(size_t)(LAS char*)lds;
    if (gw) {
      const char* bsrc = (const char*)p.w1 + (size_t)(256 * kh) * (DFF * 2) + col0 * 2;
      const unsigned bvo = (unsigned)(8 * g * (DFF * 2) + 12 * c);
      u32x3 bq[PB][8];
      auto issueB = [&](int s) {
        const char* q = bsrc + (size_t)((s + rot) & 7) * (32 * DFF * 2);
#pragma unroll
        for (int j = 0; j < 8; ++j) bq[s % PB][j] = *(const u32x3_a4*)(q + j * (DFF * 2) + bvo);
      };
      lds_wait(cnt + 3, 4);
      if (gi < 2) {
        unsigned long long ca;
        if (gi == 0) asm volatile("s_getpc_b64 s[90:91]\n\ts_add_u32 s90, s90, _Z8k_phase16Params@rel32@lo+4\n\ts_addc_u32 s91, s91, _Z8k_phase16Params@rel32@hi+12\n\ts_mov_b64 %0, s[90:91]" : "=s"(ca) :: "scc", "s90", "s91");
        else asm volatile("s_getpc_b64 s[90:91]\n\ts_add_u32 s90, s90, _Z8k_phase26Params@rel32@lo+4\n\ts_addc_u32 s91, s91, _Z8k_phase26Params@rel32@hi+12\n\ts_mov_b64 %0, s[90:91]" : "=s"(ca) :: "scc", "s90", "s91");
        const unsigned sl = (gi == 0) ? CODE1_SLICE : CODE2_SLICE;
        if ((unsigned)lane * 16u < sl) {
          const unsigned v = *(const unsigned*)(ca + (unsigned long long)(idx * sl + lane * 16));
          asm volatile("" :: "v"(v));
        }
      }
#pragma unroll
      for (int s = 0; s < PB; ++s) issueB(s);
      SB();
      STAMP_AT(0, 4);
      f32x4 acc[4][6];
      f32x4 acs[6];
      const s16x8 onesf = {0x3f80, 0x3f80, 0x3f80, 0x3f80, 0x3f80, 0x3f80, 0x3f80, 0x3f80};
#pragma unroll
      for (int t = 0; t < 6; ++t) acs[t] = (f32x4){0.f, 0.f, 0.f, 0.f};
#pragma unroll
      for (int i = 0; i < 4; ++i)
#pragma unroll
        for (int t = 0; t < 6; ++t) acc[i][t] = (f32x4){0.f, 0.f, 0.f, 0.f};
      lds_wait(cnt + 0, 4);
      STAMP_AT(0, 1);
      auto mkF = [&](int s, int t) -> s16x8 {
        u32x4 F;
#pragma unroll
        for (int q = 0; q < 4; ++q)
          F[q] = __builtin_amdgcn_perm(bq[s % PB][2 * q + 1][t >> 1], bq[s % PB][2 * q][t >> 1], (t & 1) ? 0x07060302u : 0x05040100u);
        return __builtin_bit_cast(s16x8, F);
      };
      s16x8 Fc = mkF(0, 0);
#pragma unroll
      for (int s = 0; s < 8; ++s) {
        s16x8 af[4];
        {
          const char* ap = arow + (gl16 ^ (unsigned)((8 * kh + ((s + rot) & 7)) * 64));
#pragma unroll
          for (int mt = 0; mt < 4; ++mt) af[mt] = *(const s16x8*)(ap + mt * 16384);
        }
        const char* q = bsrc + (size_t)((s + PB - 1 + rot) & 7) * (32 * DFF * 2);
#pragma unroll
        for (int t = 0; t < 6; ++t) {
          s16x8 Fn = Fc;
          if (t + 1 < 6) Fn = mkF(s, t + 1);
          else if (s + 1 < 8) Fn = mkF(s + 1, 0);
          if (s >= 1 && s + PB - 1 < 8 && t < 4) {
            bq[(s + PB - 1) % PB][2 * t] = *(const u32x3_a4*)(q + (2 * t) * (DFF * 2) + bvo);
            bq[(s + PB - 1) % PB][2 * t + 1] = *(const u32x3_a4*)(q + (2 * t + 1) * (DFF * 2) + bvo);
          }
          SB();
#pragma unroll
          for (int mt = 0; mt < 4; ++mt)
            acc[mt][t] = __builtin_amdgcn_mfma_f32_16x16x32_bf16(Fc, af[mt], acc[mt][t], 0, 0, 0);
          acs[t] = __builtin_amdgcn_mfma_f32_16x16x32_bf16(Fc, onesf, acs[t], 0, 0, 0);
          SB();
          Fc = Fn;
        }
      }
      STAMP_AT(0, 2);
      int tz = TIDX;
      asm volatile("" : "+v"(tz));
      {
      const int lane = tz & 63, c = tz & 15, g = (tz >> 4) & 3;
      lds_wait2(cnt + 8, 2, cnt + 9, 2);
      f32x4* rx = (f32x4*)(lds + L1_R + gi * 12288);
      const f32x4* ro = (const f32x4*)(lds + L1_R + (gi ^ 2) * 12288);
      f32x4* csl = (f32x4*)(lds + L1_CS);
      auto fin = [&](auto KH) {
        constexpr int kk = decltype(KH)::v;
#pragma unroll
        for (int m = 0; m < 2; ++m)
#pragma unroll
          for (int t = 0; t < 6; ++t) rx[(m * 6 + t) * 64 + lane] = acc[2 * (1 - kk) + m][t];
        if (c == 0) {
#pragma unroll
          for (int t = 0; t < 6; ++t) csl[(gi * 6 + t) * 4 + g] = acs[t];
        }
        lds_signal(cnt + 5 + ch, lane);
        lds_wait2(cnt + 5 + ch, 2, cnt + 4, 4);
        f32x4 cso[6], oo[2][6], stv[2];
#pragma unroll
        for (int t = 0; t < 6; ++t) cso[t] = csl[((gi ^ 2) * 6 + t) * 4 + g];
#pragma unroll
        for (int m = 0; m < 2; ++m) {
          stv[m] = *(const f32x4*)(lds + L1_ST + (16 * (2 * kk + m) + c) * 16);
#pragma unroll
          for (int t = 0; t < 6; ++t) oo[m][t] = ro[(m * 6 + t) * 64 + lane];
        }
        SB();
#pragma unroll
        for (int t = 0; t < 6; ++t) acs[t] += cso[t];
#pragma unroll
        for (int m = 0; m < 2; ++m) {
          const int mt = 2 * kk + m;
          const f32x4 st = stv[m];
          float hv[24];
#pragma unroll
          for (int t = 0; t < 6; ++t) {
#pragma unroll
            for (int r = 0; r < 4; ++r)
              hv[6 * r + t] = __builtin_fmaf(-st[0], acs[t][r], acc[2 * kk + m][t][r] + oo[m][t][r]) * st[2];
          }
#pragma unroll
          for (int ck = 0; ck < 3; ++ck) {
            const int F0 = col0 + 24 * g + 8 * ck;
            u32x4 o;
#pragma unroll
            for (int e = 0; e < 4; ++e) {
              const s16x2 pv = __builtin_bit_cast(s16x2, pkbf(hv[8 * ck + 2 * e], hv[8 * ck + 2 * e + 1]));
              o[e] = __builtin_bit_cast(unsigned, __builtin_elementwise_max(pv, (s16x2){0, 0}));
            }
            st16<0>(p.ws + WS_H + ((size_t)(rt * 4 + mt) * 43 + (F0 >> 5)) * 1024 + (c + 16 * ((F0 >> 3) & 3)) * 16, o);
          }
        }
      };
      if (kh == 0) fin(IC<0>{}); else fin(IC<1>{});
      }
      STAMP_AT(0, 3);
    }
  } else {
    const int kw = w, chl = kw >> 1, nh = kw & 1;
    const int d = 2 * b + chl;
    char* tch = lds + L1_R + chl * KT_CH;
    unsigned short* th = (unsigned short*)(lds + L1_TH + chl * 1344);
    float a2, fr, wsc_inv;
    f32x2 W;
    bool grow;
    u32x4 xq[16];
    u32x2 w2r[8];
    const int blk = 4 * b + kw;
    const bool dow2 = blk < 344;
    {
      {
        const char* xr = (const char*)p.x + (size_t)(row0 + 16 * kw) * 1024 + lane * 16;
#pragma unroll
        for (int i = 0; i < 16; ++i) xq[i] = *(const u32x4*)(xr + i * 1024);
      }
      SB();
      const size_t o = (size_t)d * NS + 64 * nh + lane, o2 = o ^ 64;
      const float lg = bf2f(p.logdt[d]);
      const float are = bf2f(p.Are[o]), aim = bf2f(p.Aim[o]);
      const float bre = bf2f(p.Bre[o]), bim = bf2f(p.Bim[o]);
      const float cr = bf2f(p.Cre[o]), ci = bf2f(p.Cim[o]);
      const float are2 = bf2f(p.Are[o2]);
      const float bre2 = bf2f(p.Bre[o2]), bim2 = bf2f(p.Bim[o2]);
      const float cr2 = bf2f(p.Cre[o2]), ci2 = bf2f(p.Cim[o2]);
      if (dow2) {
        const int ct = blk / 43, ks = blk % 43;
        const unsigned short* wsrc = p.w2 + (size_t)(32 * ks + 8 * g) * DM + 64 * ct + 4 * c;
#pragma unroll
        for (int jj = 0; jj < 8; ++jj) w2r[jj] = *(const u32x2*)(wsrc + (size_t)jj * DM);
      }
      SB();
      if (lane == 0) __hip_atomic_fetch_add(cnt + 3, 1u, __ATOMIC_RELAXED, __HIP_MEMORY_SCOPE_WORKGROUP);
      SB();
      const float dt = rbf(exp_acc(lg));
      const float br = dt * bre, bi = dt * bim;
      a2 = (dt * are) * 1.4426950408889634f;
      fr = (dt * aim) * 0.15915494309189535f;
      W = (f32x2){cr * br - ci * bi, -(cr * bi + ci * br)};
      const float m1 = (__builtin_fabsf(cr) + __builtin_fabsf(ci)) * (__builtin_fabsf(bre) + __builtin_fabsf(bim));
      const float m2 = (__builtin_fabsf(cr2) + __builtin_fabsf(ci2)) * (__builtin_fabsf(bre2) + __builtin_fabsf(bim2));
      const float mw = wave_max_nonneg(__builtin_fmaxf(m1, m2)) * dt;
      const int eb = (int)(f2u(mw) >> 23);
      int ex = 136 - eb;
      ex = ex > 120 ? 120 : (ex < -120 ? -120 : ex);
      const float wsc = u2f((unsigned)(127 + ex) << 23);
      wsc_inv = u2f((unsigned)(127 - ex) << 23);
      W = (f32x2){W[0] * wsc, W[1] * wsc};
      const float gmax = __builtin_fmaxf(dt * are, dt * are2);
      grow = __builtin_amdgcn_ballot_w64(!(gmax * 2048.f < 2.f) || eb >= 250) != 0ull;
    }
    STAMP_AT(0, 1);
    auto cexpq = [&](float q) -> f32x2 {
      const float e = __builtin_amdgcn_exp2f(a2 * q);
      const float ph = __builtin_amdgcn_fractf(fr * q);
      return (f32x2){__builtin_amdgcn_cosf(ph) * e, __builtin_amdgcn_sinf(ph) * e};
    };
    auto cmul = [](f32x2 a, f32x2 bb) -> f32x2 {
      return (f32x2){__builtin_fmaf(a[0], bb[0], -(a[1] * bb[1])), __builtin_fmaf(a[0], bb[1], a[1] * bb[0])};
    };
    char* tw = tch + (64 * nh + lane) * 4;
    auto put = [&](int row, f32x2 v) {
      float v0 = v[0], v1 = v[1];
      asm("" : "+v"(v0), "+v"(v1));
      *(unsigned*)(tw + row * KT_P) = pkh(v0, v1);
    };
    auto xstore = [&]() {
#pragma unroll
      for (int i = 0; i < 16; ++i) *(u32x4*)(lds + L1_A + (16 * kw + i) * 1024 + ((lane ^ i) * 16)) = xq[i];
      lds_signal(cnt + 0, lane);
      if (dow2) {
        u32x4 f4[4];
#pragma unroll
        for (int q2 = 0; q2 < 4; ++q2) {
          f4[0][q2] = __builtin_amdgcn_perm(w2r[2 * q2 + 1][0], w2r[2 * q2][0], 0x05040100u);
          f4[1][q2] = __builtin_amdgcn_perm(w2r[2 * q2 + 1][0], w2r[2 * q2][0], 0x07060302u);
          f4[2][q2] = __builtin_amdgcn_perm(w2r[2 * q2 + 1][1], w2r[2 * q2][1], 0x05040100u);
          f4[3][q2] = __builtin_amdgcn_perm(w2r[2 * q2 + 1][1], w2r[2 * q2][1], 0x07060302u);
        }
        char* dst2 = p.ws + WS_W2F + (size_t)blk * 4096 + lane * 16;
#pragma unroll
        for (int t = 0; t < 4; ++t) st16<2>(dst2 + t * 1024, f4[t]);
      }
    };
    xstore();
    SB();
    if (!grow) {
      const f32x2 E1 = cexpq(1.f), E256 = cexpq(256.f), E8 = cexpq(8.f);
      f32x2 y0 = (f32x2){1.f, 0.f}, y1 = cexpq(64.f), y2 = cexpq(128.f), y3 = cexpq(192.f);
      const f32x2 C1 = {E1[0], -E1[1]}, CL = {E256[0], -E256[1]};
      const f32x2 C2 = cmul(C1, C1), C4 = cmul(C2, C2);
      f32x2 xs[21];
      xs[0] = W;
      xs[8] = cmul(W, CL);
      xs[1] = cmul(xs[0], C1);  xs[12] = cmul(xs[8], CL);  xs[9] = cmul(xs[8], C2);
      xs[2] = cmul(xs[1], C1);  xs[14] = cmul(xs[12], CL); xs[10] = cmul(xs[9], C2); xs[13] = cmul(xs[12], C4);
      xs[3] = cmul(xs[2], C1);  xs[16] = cmul(xs[14], CL); xs[11] = cmul(xs[10], C2); xs[15] = cmul(xs[14], C4);
      xs[4] = cmul(xs[0], C4);  xs[17] = cmul(xs[16], CL);
      xs[5] = cmul(xs[1], C4);  xs[18] = cmul(xs[17], CL);
      xs[6] = cmul(xs[2], C4);  xs[19] = cmul(xs[18], CL);
      xs[7] = cmul(xs[3], C4);  xs[20] = cmul(xs[19], CL);
#pragma unroll
      for (int i = 0; i < 21; ++i) put(i, xs[i]);
      put(21, y0); put(29, y1); put(37, y2); put(45, y3);
#pragma unroll
      for (int i = 1; i < 8; ++i) {
        y0 = cmul(y0, E8); y1 = cmul(y1, E8); y2 = cmul(y2, E8); y3 = cmul(y3, E8);
        put(21 + i, y0); put(29 + i, y1); put(37 + i, y2); put(45 + i, y3);
      }
    } else {
      *(f32x4*)(tch + (64 * nh + lane) * 16) = (f32x4){a2, fr, W[0], -W[1]};
    }
    lds_signal(cnt + 1 + chl, lane);
    STAMP_AT(0, 4);
    {
      lds_wait(cnt + 0, 4);
      const s16x8 onesf = {0x3f80, 0x3f80, 0x3f80, 0x3f80, 0x3f80, 0x3f80, 0x3f80, 0x3f80};
      f32x4 as0 = {0.f, 0.f, 0.f, 0.f}, aq0 = as0, as1 = as0, aq1 = as0;
      const char* ab2 = arow + kw * 16384;
      s16x8 xf[16];
#pragma unroll
      for (int s = 0; s < 16; ++s) xf[s] = *(const s16x8*)(ab2 + (gl16 ^ (unsigned)(s * 64)));
      SB();
#pragma unroll
      for (int s = 0; s < 16; s += 2) {
        as0 = __builtin_amdgcn_mfma_f32_16x16x32_bf16(onesf, xf[s], as0, 0, 0, 0);
        aq0 = __builtin_amdgcn_mfma_f32_16x16x32_bf16(xf[s], xf[s], aq0, 0, 0, 0);
        as1 = __builtin_amdgcn_mfma_f32_16x16x32_bf16(onesf, xf[s + 1], as1, 0, 0, 0);
        aq1 = __builtin_amdgcn_mfma_f32_16x16x32_bf16(xf[s + 1], xf[s + 1], aq1, 0, 0, 0);
      }
      const int r3 = c & 3;
      const f32x4 aq = aq0 + aq1;
      const float q = (r3 == 0) ? aq[0] : (r3 == 1) ? aq[1] : (r3 == 2) ? aq[2] : aq[3];
      const float mean = (as0[0] + as1[0]) * (1.f / DM);
      const float var = __builtin_fmaxf(q * (1.f / DM) - mean * mean, 0.f);
      const float mb = rbf(mean);
      const float dn = rbf(__builtin_sqrtf(rbf(rbf(var) + 1e-5f)));
      if (g == (c >> 2)) {
        const int r = 16 * kw + c;
        *(f32x4*)(lds + L1_ST + r * 16) = (f32x4){mb, dn, 1.f / dn, 0.f};
        if (cg == 0) *(f32x2*)(p.ws + WS_ST + (size_t)(row0 + r) * 8) = (f32x2){mb, dn};
      }
      lds_signal(cnt + 4, lane);
    }
    STAMP_AT(0, 8);
    lds_wait(cnt + 1 + chl, 2);
    f32x4 acc[2];
    acc[0] = (f32x4){0.f, 0.f, 0.f, 0.f}; acc[1] = acc[0];
    const int j = 16 * nh + c;
    if (!grow) {
      const char* fa = tch + c * KT_P + 16 * g;
      const char* fb = tch + (21 + j) * KT_P + 16 * g;
      f16x8 fA0[8], fA1[8], fB[8];
#pragma unroll
      for (int s = 0; s < 8; ++s) {
        fB[s] = *(const f16x8*)(fb + 64 * s);
        fA0[s] = *(const f16x8*)(fa + 64 * s);
        fA1[s] = *(const f16x8*)(fa + 16 * KT_P + 64 * s);
      }
      SB();
#pragma unroll
      for (int s = 0; s < 8; ++s) {
        acc[0] = __builtin_amdgcn_mfma_f32_16x16x32_f16(fA0[s], fB[s], acc[0], 0, 0, 0);
        acc[1] = __builtin_amdgcn_mfma_f32_16x16x32_f16(fA1[s], fB[s], acc[1], 0, 0, 0);
      }
    } else {
      for (int e = 0; e < 8; ++e) {
        const int row = 16 * (e >> 2) + 4 * g + (e & 3);
        const int qx = row < 8 ? row : row < 12 ? 256 + 2 * (row - 8) : row < 16 ? 512 + 256 * ((row - 12) >> 1) + 4 * (row & 1)
                                                                   : row < 21 ? 1024 + 256 * (row - 16) : 0;
        const float q = (float)(qx + 8 * j);
        float sacc = 0.f;
        for (int n = 0; n < 128; ++n) {
          const f32x4 pr = *(const f32x4*)(tch + n * 16);
          const float ph = __builtin_amdgcn_fractf(pr[1] * q);
          sacc += __builtin_amdgcn_exp2f(pr[0] * q) * (pr[2] * __builtin_amdgcn_cosf(ph) - pr[3] * __builtin_amdgcn_sinf(ph));
        }
        if (e < 4) acc[0][e & 3] = sacc; else acc[1][e & 3] = sacc;
      }
    }
    STAMP_AT(0, 5);
    {
      const bool v1 = (g == 0), v2 = (g == 1 && j == 0);
      float mx = __builtin_fmaxf(__builtin_fmaxf(__builtin_fabsf(acc[0][0]), __builtin_fabsf(acc[0][1])),
                                 __builtin_fmaxf(__builtin_fabsf(acc[0][2]), __builtin_fabsf(acc[0][3])));
      const float m1 = __builtin_fmaxf(__builtin_fmaxf(__builtin_fabsf(acc[1][1]), __builtin_fabsf(acc[1][2])), __builtin_fabsf(acc[1][3]));
      mx = __builtin_fmaxf(mx, (v1 || v2) ? __builtin_fabsf(acc[1][0]) : 0.f);
      mx = __builtin_fmaxf(mx, v1 ? m1 : 0.f);
      mx = wave_max_nonneg(mx);
      float* mxl = (float*)(lds + L1_TH + 2688) + kw;
      if (lane == 0) *mxl = mx;
      lds_signal(cnt + 8 + chl, lane);
    {
      lds_wait(cnt + 4, 4);
      const int q4 = c >> 2, pp = c & 3;
      f32x4 st[16];
#pragma unroll
      for (int r = 0; r < 16; ++r) st[r] = *(const f32x4*)(lds + L1_ST + (16 * g + r) * 16);
      const int ck = 8 * cg + 2 * kw + (pp >> 1);
      unsigned ov[8];
      u32x2 vv[4];
#pragma unroll
      for (int k = 0; k < 4; ++k) {
        const int r16 = 4 * k + q4;
        const char* a0 = lds + L1_A + (16 * g + r16) * 1024 + ((ck ^ r16) * 16) + 8 * (pp & 1);
        vv[k] = __builtin_bit_cast(u32x2, __builtin_amdgcn_ds_read_tr16_b64_v4i16((LAS s16x4*)a0));
      }
      SB();
#pragma unroll
      for (int k = 0; k < 4; ++k) {
        const u32x2 v = vv[k];
#pragma unroll
        for (int e = 0; e < 2; ++e) {
          const unsigned ve = v[e];
          const f32x4 s0 = st[4 * k + 2 * e], s1 = st[4 * k + 2 * e + 1];
          const float t0 = rbf(bflo(ve) - s0[0]), t1 = rbf(bfhi(ve) - s1[0]);
          float q0 = t0 * s0[2], q1 = t1 * s1[2];
          q0 = __builtin_fmaf(__builtin_fmaf(-q0, s0[1], t0), s0[2], q0);
          q1 = __builtin_fmaf(__builtin_fmaf(-q1, s1[1], t1), s1[2], q1);
          const unsigned xb = pkbf(q0, q1);
          ov[2 * k + e] = pkh(bflo(xb), bfhi(xb));
        }
      }
      unsigned short* dst = (unsigned short*)(p.ws + WS_XNT) + (size_t)(64 * cg + 16 * kw + c) * SL + row0 + 16 * g;
      st16<2>(dst, (u32x4){ov[0], ov[1], ov[2], ov[3]});
      st16<2>(dst + 8, (u32x4){ov[4], ov[5], ov[6], ov[7]});
    }
    {
      char* ydst = p.ws + WS_YT + (size_t)b * 8192 + kw * 2048 + lane * 16;
      const u32x4 sent = {0xffffffffu, 0xffffffffu, 0xffffffffu, 0xffffffffu};
      st16<2>(ydst, sent);
      st16<2>(ydst + 1024, sent);
    }
      lds_wait(cnt + 8 + chl, 2);
      mx = __builtin_fmaxf(mx, ((const float*)(lds + L1_TH + 2688))[kw ^ 1]);
      const int eb = (int)(f2u(mx) >> 23);
      int ex = 140 - eb;
      ex = (eb == 0 || eb == 255) ? 0 : (ex > 120 ? 120 : ex);
      const float sc = u2f((unsigned)(127 + ex) << 23);
      if (lane == 0 && nh == 0) ((float*)(p.ws + WS_KS))[d] = u2f((unsigned)(127 - ex) << 23) * wsc_inv;
      const int b0 = (g < 2 ? 4 * g + 8 * j : g == 2 ? 256 + 4 * j : 384 + 2 * j);
      *(unsigned*)(th + b0) = pkh(acc[0][0] * sc, acc[0][1] * sc);
      *(unsigned*)(th + b0 + (g == 3 ? 64 : 2)) = pkh(acc[0][2] * sc, acc[0][3] * sc);
      const unsigned p01 = pkh(acc[1][0] * sc, acc[1][1] * sc), p23 = pkh(acc[1][2] * sc, acc[1][3] * sc);
      if (v1) {
        th[512 + j] = (unsigned short)p01; th[544 + j] = (unsigned short)(p01 >> 16);
        th[576 + j] = (unsigned short)p23; th[608 + j] = (unsigned short)(p23 >> 16);
      }
      if (v2) th[640] = (unsigned short)p01;
      lds_signal(cnt + 10 + chl, lane);
    }
    STAMP_AT(0, 6);
    STAMP_AT(0, 7);
    {
      lds_wait(cnt + 10 + chl, 2);
      const unsigned short* tu = th;
      unsigned short* kr = (unsigned short*)(p.ws + WS_KR) + (size_t)d * KRL;
      if (nh == 0) {
#pragma unroll
        for (int k = 0; k < 2; ++k) {
          const int ck = lane + 64 * k, M = 256 - ck;
          const unsigned a = tu[384 + M], bb = tu[383 + M];
          const unsigned aa = a | (a << 16), b2 = bb | (bb << 16);
          const unsigned mid = ((M & 1) ? bb : a) | (bb << 16);
          st16<2>(kr + 8 * ck, (u32x4){aa, aa, mid, b2});
        }
        if (lane < 4) st16<2>(kr + 8 * (256 + lane), (u32x4){lane == 0 ? (unsigned)tu[0] : 0u, 0u, 0u, 0u});
      } else {
        {
          const int ck = 128 + lane, M = 256 - ck;
          const unsigned a = tu[256 + 2 * M], bb = tu[255 + 2 * M], cc = tu[254 + 2 * M];
          st16<2>(kr + 8 * ck, (u32x4){a | (a << 16), a | (bb << 16), bb | (bb << 16), cc | (cc << 16)});
        }
        {
          const int ck = 192 + lane, M = 256 - ck;
          const bool s1 = lane < 32;
          const int base = s1 ? 128 + 4 * M : 8 * M;
          const int o1[8] = {0, 0, 1, 2, 2, 2, 3, 4};
          unsigned hv[8];
#pragma unroll
          for (int e = 0; e < 8; ++e) hv[e] = tu[base - (s1 ? o1[e] : e)];
          st16<2>(kr + 8 * ck, (u32x4){hv[0] | (hv[1] << 16), hv[2] | (hv[3] << 16), hv[4] | (hv[5] << 16), hv[6] | (hv[7] << 16)});
        }
      }
    }
    STAMP_AT(0, 3);
    STAMP_AT(0, 9);
  }
  STAMP_FLUSH(0);
}

#define GAS __attribute__((address_space(1)))
constexpr int KCS = KRL * 2;
constexpr int CV_CH = 8 * KCS + 4608;
constexpr int L2_CR = 2 * CV_CH;
constexpr int L2_GR = L2_CR + 16384;
constexpr int L2_Y = L2_GR + 49152;
constexpr int L2_T1 = L2_Y + 64 * 136;
constexpr int L2_C = L2_T1 + 8192;
constexpr int L2_SIZE = L2_C + 64;

__global__ void __launch_bounds__(512, 2) k_phase2(Params p) {
  __shared__ __attribute__((aligned(16))) char lds[L2_SIZE];
  STAMP_DECL
  const int tid = TIDX, lane = tid & 63;
  const int w = __builtin_amdgcn_readfirstlane(tid >> 6);
  const int b = BIDX;
  LAS unsigned* cnt = (LAS unsigned*)(LAS char*)(lds + L2_C);
  if (tid < 16) cnt[tid] = 0u;
  wg_sync();
  STAMP_AT(1, 0);
  const int xcd = b & 7, idx = b >> 3;
  const int rt = xcd * 4 + (idx >> 3), ct = idx & 7;
  const int row0 = rt * 64, col0 = ct * 64;
  const int c = lane & 15, g = lane >> 4;
  GAS unsigned* flags = (GAS unsigned*)(p.ws + WS_FL);
  if (w < 4) {
    const int chl = w >> 1, hf = w & 1;
    const int d = 2 * b + chl;
    char* kl = lds + chl * CV_CH;
    char* ul = kl + 8 * KCS;
    const int erow = row0 + 16 * w + 4 * g;
    u32x2 xv[4];
    f32x4 st01, st23;
    u32x2 dk;
    float isc;
    {
      const int t2 = tid & 127;
      const u32x4* ksrc = (const u32x4*)(p.ws + WS_KR + (size_t)d * KCS);
      const u32x4* usrc = (const u32x4*)(p.ws + WS_XNT + (size_t)d * SL * 2);
      u32x4 kc[3], kp[3];
#pragma unroll
      for (int k = 0; k < 3; ++k) {
        int ci = t2 + 128 * k;
        ci = ci < 260 ? ci : 259;
        kc[k] = ksrc[ci];
        kp[k] = ksrc[ci > 0 ? ci - 1 : 0];
      }
      u32x4 uv0 = usrc[t2], uv1 = usrc[t2 + 128];
      SB();
#pragma unroll
      for (int r = 0; r < 4; ++r) xv[r] = *(const u32x2*)(p.x + (size_t)(erow + r) * DM + col0 + 4 * c);
      st01 = *(const f32x4*)(p.ws + WS_ST + (size_t)erow * 8);
      st23 = *(const f32x4*)(p.ws + WS_ST + (size_t)erow * 8 + 16);
      dk = *(const u32x2*)(p.Dsk + col0 + 4 * c);
      isc = ((const float*)(p.ws + WS_KS))[d];
      SB();
      const u32x4 z = {0u, 0u, 0u, 0u};
      if (t2 < 32) *(u32x4*)(ul + t2 * 16) = z;
#pragma unroll
      for (int k = 0; k < 3; ++k) {
        const int ci = t2 + 128 * k;
        if (ci < 260) {
          unsigned wd[8] = {kp[k][0], kp[k][1], kp[k][2], kp[k][3], kc[k][0], kc[k][1], kc[k][2], kc[k][3]};
          unsigned od[7];
#pragma unroll
          for (int j = 0; j < 7; ++j) od[j] = __builtin_amdgcn_alignbit(wd[j + 1], wd[j], 16);
          char* dst = kl + ci * 16;
#pragma unroll
          for (int sg = 0; sg < 8; ++sg) {
            u32x4 o;
            if ((sg & 1) == 0) { const int s0 = (8 - sg) >> 1; o = (u32x4){wd[s0], wd[s0 + 1], wd[s0 + 2], wd[s0 + 3]}; }
            else { const int s0 = (7 - sg) >> 1; o = (u32x4){od[s0], od[s0 + 1], od[s0 + 2], od[s0 + 3]}; }
            *(u32x4*)(dst + sg * KCS) = o;
          }
        }
      }
      *(u32x4*)(ul + 512 + t2 * 16) = uv0;
      *(u32x4*)(ul + 512 + (t2 + 128) * 16) = uv1;
    }
    lds_signal(cnt + chl, lane);
    lds_wait(cnt + chl, 2);
    STAMP_AT(1, 1);
    const int a = c;
    f32x4 acc[8];
#pragma unroll
    for (int r = 0; r < 8; ++r) acc[r] = (f32x4){0.f, 0.f, 0.f, 0.f};
    const char* ab = kl + (a & 7) * KCS + 2 * (2032 + 8 * g - 8 * (a >> 3)) - 64 * 4 * hf;
    const char* bb = ul + 2 * (240 + 16 * a + 8 * g) - 64 * 4 * hf;
    f16x8 bf[2][8], af[2][8];
#pragma unroll
    for (int t = 0; t < 8; ++t) { bf[0][t] = *(const f16x8*)(bb + 512 * t); af[0][t] = *(const f16x8*)(ab - 512 * t); }
#pragma unroll
    for (int i = 0; i < 4; ++i) {
      if (i + 1 < 4) {
#pragma unroll
        for (int t = 0; t < 8; ++t) {
          bf[(i + 1) & 1][t] = *(const f16x8*)(bb - 64 * (i + 1) + 512 * t);
          af[(i + 1) & 1][t] = *(const f16x8*)(ab - 64 * (i + 1) - 512 * t);
        }
      }
      SB();
#pragma unroll
      for (int v = 0; v < 8; ++v)
#pragma unroll
        for (int t = v; t < 8; ++t)
          acc[t] = __builtin_amdgcn_mfma_f32_16x16x32_f16(af[i & 1][v], bf[i & 1][t - v], acc[t], 0, 0, 0);
      SB();
    }
    STAMP_AT(1, 2);
    f32x4* red = (f32x4*)(lds + L2_CR + chl * 8192);
    f32x4 mine[4];
    if (hf) {
#pragma unroll
      for (int r = 0; r < 4; ++r) { red[(4 + r) * 64 + lane] = acc[r]; mine[r] = acc[4 + r]; }
    } else {
#pragma unroll
      for (int r = 0; r < 4; ++r) { red[r * 64 + lane] = acc[4 + r]; mine[r] = acc[r]; }
    }
    lds_signal(cnt + 2 + chl, lane);
    lds_wait(cnt + 2 + chl, 2);
    {
      unsigned pk[4][2];
#pragma unroll
      for (int r = 0; r < 4; ++r) {
        const f32x4 v = (mine[r] + red[(4 * (1 - hf) + r) * 64 + lane]) * isc;
        pk[r][0] = pkbf(v[0], v[1]);
        pk[r][1] = pkbf(v[2], v[3]);
      }
      const auto rsrc = __builtin_amdgcn_make_buffer_rsrc((void*)(p.ws + WS_YT), 0, DM * SL * 2, 0x00020000);
#pragma unroll
      for (int r = 0; r < 2; ++r) {
        auto s0 = __builtin_amdgcn_permlane16_swap(pk[r][0], pk[r + 2][0], false, false);
        auto s1 = __builtin_amdgcn_permlane16_swap(pk[r][1], pk[r + 2][1], false, false);
        const unsigned a0 = s0[0], b0 = s0[1], a1 = s1[0], b1 = s1[1];
        const u32x4 o = {a0, a1, b0, b1};
        const int tile = r + 2 * (g & 1);
        const int off = (d * SL + 1024 * hf + 256 * tile + 16 * a + 8 * (g >> 1)) * 2;
        __builtin_amdgcn_raw_buffer_store_b128(o, rsrc, off, 0, 16);
      }
    }
    STAMP_AT(1, 3);
    {
      const int ch = 16 * w + (lane >> 2), k0 = (lane & 3) * 2;
      const auto rsrc = __builtin_amdgcn_make_buffer_rsrc((void*)(p.ws + WS_YT), 0, DM * SL * 2, 0x00020000);
      const int off = ((col0 + ch) * SL + row0) * 2 + k0 * 16;
      float xf[4][4], xd[4][4];
      {
        const float dkf[4] = {bflo(dk[0]), bfhi(dk[0]), bflo(dk[1]), bfhi(dk[1])};
#pragma unroll
        for (int r = 0; r < 4; ++r) {
          xf[r][0] = bflo(xv[r][0]); xf[r][1] = bfhi(xv[r][0]); xf[r][2] = bflo(xv[r][1]); xf[r][3] = bfhi(xv[r][1]);
          const float mb = (r == 0) ? st01[0] : (r == 1) ? st01[2] : (r == 2) ? st23[0] : st23[2];
          const float den = (r == 0) ? st01[1] : (r == 1) ? st01[3] : (r == 2) ? st23[1] : st23[3];
          const float rc = 1.f / den;
#pragma unroll
          for (int t = 0; t < 4; ++t) {
            const float a0 = rbf(xf[r][t] - mb);
            float q = a0 * rc;
            q = __builtin_fmaf(__builtin_fmaf(-q, den, a0), rc, q);
            xd[r][t] = rbf(rbf(q) * dkf[t]);
            asm volatile("" : "+v"(xd[r][t]), "+v"(xf[r][t]));
          }
        }
      }
      u32x4 v0, v1;
      unsigned spins = 0;
      lds_wait(cnt + 7, 4);
      while (true) {
        v0 = __builtin_amdgcn_raw_buffer_load_b128(rsrc, off, 0, 16);
        v1 = __builtin_amdgcn_raw_buffer_load_b128(rsrc, off + 16, 0, 16);
        bool bad = false;
#pragma unroll
        for (int e = 0; e < 4; ++e) {
          const unsigned a0 = v0[e], a1 = v1[e];
          bad |= ((a0 & 0xffffu) == 0xffffu) | ((a0 >> 16) == 0xffffu) | ((a1 & 0xffffu) == 0xffffu) | ((a1 >> 16) == 0xffffu);
        }
        if (__builtin_amdgcn_ballot_w64(bad) == 0ull) break;
        if (++spins > (1u << 18)) break;
        __builtin_amdgcn_s_sleep(4);
      }
      STAMP_AT(1, 4);
      char* yd = lds + L2_Y + ch * 136 + k0 * 16;
      *(u32x2*)yd = (u32x2){v0[0], v0[1]}; *(u32x2*)(yd + 8) = (u32x2){v0[2], v0[3]};
      *(u32x2*)(yd + 16) = (u32x2){v1[0], v1[1]}; *(u32x2*)(yd + 24) = (u32x2){v1[2], v1[3]};
      lds_signal(cnt + 4, lane);
      lds_wait(cnt + 4, 4);
      u32x2 yv[4];
#pragma unroll
      for (int t = 0; t < 4; ++t) yv[t] = *(const u32x2*)(lds + L2_Y + (4 * c + t) * 136 + (16 * w + 4 * g) * 2);
#pragma unroll
      for (int r = 0; r < 4; ++r) {
        float o[4];
#pragma unroll
        for (int t = 0; t < 4; ++t) {
          const unsigned yw = yv[t][r >> 1];
          const float y = (r & 1) ? bfhi(yw) : bflo(yw);
          o[t] = xf[r][t] + rbf(y + xd[r][t]);
        }
        *(u32x2*)(lds + L2_T1 + ((w * 4 + r) * 64 + lane) * 8) = (u32x2){pkbf(o[0], o[1]), pkbf(o[2], o[3])};
      }
    }
    lds_signal(cnt + 8 + w, lane);
    STAMP_AT(1, 5);
  } else {
    const int gw = w - 4;
    const int s0 = 11 * gw;
    const int ns = (gw == 3) ? 10 : 11;
    const char* hb = p.ws + WS_H + ((size_t)(rt * 4) * 43 + s0) * 1024 + lane * 16;
    const char* wb = p.ws + WS_W2F + ((size_t)ct * 43 + s0) * 4096 + lane * 16;
    constexpr int P = 4;
    u32x4 ar[P][4];
    u32x4 br[P][4];
    const int rot = 0;

    auto issue = [&](int s) {
      int ks = s + rot;
      ks = ks >= ns ? ks - ns : ks;
#pragma unroll
      for (int mt = 0; mt < 4; ++mt) ar[s % P][mt] = *(const u32x4*)(hb + (size_t)mt * 43 * 1024 + ks * 1024);
#pragma unroll
      for (int j = 0; j < 4; ++j) br[s % P][j] = *(const u32x4*)(wb + ks * 4096 + j * 1024);
    };
#pragma unroll
    for (int s = 0; s < P; ++s) issue(s);
    SB();
    f32x4 acc[4][4];
#pragma unroll
    for (int i = 0; i < 4; ++i)
#pragma unroll
      for (int t = 0; t < 4; ++t) acc[i][t] = (f32x4){0.f, 0.f, 0.f, 0.f};
#pragma unroll
    for (int s = 0; s < 11; ++s) {
      if (s < ns) {
#pragma unroll
        for (int mt = 0; mt < 4; ++mt)
#pragma unroll
          for (int t = 0; t < 4; ++t)
            acc[mt][t] = __builtin_amdgcn_mfma_f32_16x16x32_bf16(__builtin_bit_cast(s16x8, ar[s % P][mt]),
                                                                 __builtin_bit_cast(s16x8, br[s % P][t]), acc[mt][t], 0, 0, 0);
        SB();
        if (s + P < 11 && s + P < ns) issue(s + P);
        if (s == 6 && lane == 0) __hip_atomic_fetch_add(cnt + 7, 1u, __ATOMIC_RELAXED, __HIP_MEMORY_SCOPE_WORKGROUP);
        SB();
      }
    }

    const int erow = row0 + 16 * gw + 4 * g;
    STAMP_AT(1, 1);
    f32x4* red = (f32x4*)(lds + L2_GR);
    f32x4 f[4];
#pragma unroll
    for (int mt = 0; mt < 4; ++mt) {
      if (mt != gw) {
        const int slot = (gw > mt) ? (gw - 1) : gw;
#pragma unroll
        for (int t = 0; t < 4; ++t) red[((mt * 3 + slot) * 4 + t) * 64 + lane] = acc[mt][t];
      } else {
#pragma unroll
        for (int t = 0; t < 4; ++t) f[t] = acc[mt][t];
      }
    }
    lds_signal(cnt + 6, lane);
    lds_wait(cnt + 6, 4);
    STAMP_AT(1, 2);
    f32x4 pr[4][3];
#pragma unroll
    for (int t = 0; t < 4; ++t)
#pragma unroll
      for (int sl = 0; sl < 3; ++sl) pr[t][sl] = red[((gw * 3 + sl) * 4 + t) * 64 + lane];
    SB();
#pragma unroll
    for (int t = 0; t < 4; ++t) {
      f[t] += (pr[t][0] + pr[t][1]) + pr[t][2];
#pragma unroll
      for (int r = 0; r < 4; ++r) f[t][r] = rbf(f[t][r]);
      asm volatile("" : "+v"(f[t]));
    }
    lds_wait(cnt + 8 + gw, 1);
    STAMP_AT(1, 3);
#pragma unroll
    for (int r = 0; r < 4; ++r) {
      const u32x2 tv = *(const u32x2*)(lds + L2_T1 + ((gw * 4 + r) * 64 + lane) * 8);
      const unsigned t0 = tv[0], t1 = tv[1];
      u32x2 ov;
      ov[0] = pkbf(bflo(t0) + f[0][r], bfhi(t0) + f[1][r]);
      ov[1] = pkbf(bflo(t1) + f[2][r], bfhi(t1) + f[3][r]);
      asm volatile("global_store_dwordx2 %0, %1, off sc1" :: "v"(p.out + (size_t)(erow + r) * DM + col0 + 4 * c), "v"(ov) : "memory");
    }
    STAMP_AT(1, 4);
  }
  STAMP_FLUSH(1);
}

#if defined(__HIP_DEVICE_COMPILE__)
#pragma clang attribute pop
#endif

extern "C" void kernel_launch(void* const* d_in, const int* in_sizes, int n_in,
                              void* d_out, int out_size, void* d_ws, size_t ws_size,
                              hipStream_t stream) {
  Params p;
  p.x = (const unsigned short*)d_in[0];
  p.Are = (const unsigned short*)d_in[1];
  p.Aim = (const unsigned short*)d_in[2];
  p.Bre = (const unsigned short*)d_in[3];
  p.Bim = (const unsigned short*)d_in[4];
  p.Cre = (const unsigned short*)d_in[5];
  p.Cim = (const unsigned short*)d_in[6];
  p.logdt = (const unsigned short*)d_in[7];
  p.Dsk = (const unsigned short*)d_in[8];
  p.w1 = (const unsigned short*)d_in[10];
  p.w2 = (const unsigned short*)d_in[11];
  p.out = (unsigned short*)d_out;
  p.ws = (char*)d_ws;
  k_phase1<<<256, 512, 0, stream>>>(p);
  k_phase2<<<256, 512, 0, stream>>>(p);
}
```

```cpp
#include <hip/hip_runtime.h>
#include <hip/hip_bf16.h>
#include <stdint.h>

typedef __attribute__((ext_vector_type(8))) short s16x8;
typedef __attribute__((ext_vector_type(4))) short s16x4;
typedef __attribute__((ext_vector_type(8))) _Float16 f16x8;
typedef __attribute__((ext_vector_type(4))) float f32x4;
typedef __attribute__((ext_vector_type(2))) float f32x2;
typedef __attribute__((ext_vector_type(16))) float f32x16;
typedef __attribute__((ext_vector_type(4))) unsigned u32x4;
typedef __attribute__((ext_vector_type(2))) unsigned u32x2;
typedef __attribute__((ext_vector_type(2))) __bf16 bf16x2_t;
typedef __attribute__((ext_vector_type(2))) _Float16 f16x2_t;
#define LAS __attribute__((address_space(3)))

constexpr int SL = 2048;
constexpr int DM = 512;
constexpr int NS = 128;
constexpr int DFF = 1376;
constexpr int KRL = 2080;

constexpr size_t WS_H = 0;
constexpr size_t WS_XNT = WS_H + (size_t)128 * 43 * 1024;
constexpr size_t WS_KR = WS_XNT + (size_t)DM * SL * 2;
constexpr size_t WS_YT = WS_KR + (size_t)DM * KRL * 2;
constexpr size_t WS_ST = WS_YT + (size_t)DM * SL * 2;
constexpr size_t WS_KS = WS_ST + (size_t)SL * 8;
constexpr size_t WS_FL = WS_KS + 4096;
constexpr size_t WS_W2F = WS_FL + 32768;

#if defined(__HIP_DEVICE_COMPILE__)
#pragma clang attribute push(__attribute__((target("no-packed-fp32-ops"))), apply_to = function)
#endif
__device__ __forceinline__ float u2f(unsigned u) { return __builtin_bit_cast(float, u); }
__device__ __forceinline__ unsigned f2u(float f) { return __builtin_bit_cast(unsigned, f); }
__device__ __forceinline__ float shx(float v, int lane, int m) {
  return u2f((unsigned)__builtin_amdgcn_ds_bpermute((lane ^ m) << 2, (int)f2u(v)));
}
__device__ __forceinline__ void wg_sync() {
  __builtin_amdgcn_fence(__ATOMIC_RELEASE, "workgroup");
  __builtin_amdgcn_s_barrier();
  __builtin_amdgcn_fence(__ATOMIC_ACQUIRE, "workgroup");
}
__device__ __forceinline__ float exp_acc(float x) {
  const float L2E_HI = 1.44269502162933349609375f, L2E_LO = 1.925963033500011e-8f;
  const float hi = x * L2E_HI;
  const float lo = __builtin_fmaf(x, L2E_LO, __builtin_fmaf(x, L2E_HI, -hi));
  const float e = __builtin_amdgcn_exp2f(hi);
  return __builtin_fmaf(e, lo * 0.6931471805599453f, e);
}
#define TIDX ((int)__builtin_amdgcn_workitem_id_x())
#define BIDX ((int)__builtin_amdgcn_workgroup_id_x())
__device__ __forceinline__ float bf2f(unsigned h) { return u2f(h << 16); }
__device__ __forceinline__ float bflo(unsigned u) { return u2f(u << 16); }
__device__ __forceinline__ float bfhi(unsigned u) { return u2f(u & 0xffff0000u); }
__device__ __forceinline__ f32x2 bfup(unsigned u) { return (f32x2){bflo(u), bfhi(u)}; }
__device__ __forceinline__ unsigned pkbf(float lo, float hi) {
  bf16x2_t r = {(__bf16)lo, (__bf16)hi};
  return __builtin_bit_cast(unsigned, r);
}
__device__ __forceinline__ unsigned pkbf2(f32x2 v) { return pkbf(v[0], v[1]); }
__device__ __forceinline__ float rbf(float x) { return bflo(pkbf(x, 0.f)); }
__device__ __forceinline__ unsigned pkh(float lo, float hi) {
  f16x2_t r = {(_Float16)lo, (_Float16)hi};
  return __builtin_bit_cast(unsigned, r);
}

#define STAMP_DECL
#define STAMP_AT(kid, i) do {} while (0)
#define STAMP_FLUSH(kid) do {} while (0)
#define SB() __builtin_amdgcn_sched_barrier(0)
#define WAITV(n) asm volatile("s_waitcnt vmcnt(%0)" ::"n"(n) : "memory")
#define WAITL() asm volatile("s_waitcnt lgkmcnt(0)" ::: "memory")

struct Params {
  const unsigned short* x; const unsigned short* Are; const unsigned short* Aim;
  const unsigned short* Bre; const unsigned short* Bim; const unsigned short* Cre; const unsigned short* Cim;
  const unsigned short* logdt; const unsigned short* Dsk; const unsigned short* w1; const unsigned short* w2;
  unsigned short* out; char* ws;
};

__device__ __forceinline__ unsigned lds_arrive(LAS unsigned* cnt, int lane) {
  WAITL();
  unsigned old = 0;
  if (lane == 0) old = __hip_atomic_fetch_add(cnt, 1u, __ATOMIC_RELAXED, __HIP_MEMORY_SCOPE_WORKGROUP);
  return __builtin_amdgcn_readfirstlane(old);
}
__device__ __forceinline__ void lds_wait(LAS unsigned* cnt, unsigned target) {
  while (__builtin_amdgcn_readfirstlane(__hip_atomic_load(cnt, __ATOMIC_RELAXED, __HIP_MEMORY_SCOPE_WORKGROUP)) < target)
    __builtin_amdgcn_s_sleep(1);
  asm volatile("" ::: "memory");
}
__device__ __forceinline__ void lds_wait2(LAS unsigned* c0, unsigned t0, LAS unsigned* c1, unsigned t1) {
  while (true) {
    const unsigned a = __hip_atomic_load(c0, __ATOMIC_RELAXED, __HIP_MEMORY_SCOPE_WORKGROUP);
    const unsigned b = __hip_atomic_load(c1, __ATOMIC_RELAXED, __HIP_MEMORY_SCOPE_WORKGROUP);
    if (__builtin_amdgcn_readfirstlane(a) >= t0 && __builtin_amdgcn_readfirstlane(b) >= t1) break;
    __builtin_amdgcn_s_sleep(1);
  }
  asm volatile("" ::: "memory");
}
__device__ __forceinline__ void lds_signal(LAS unsigned* cnt, int lane) {
  WAITL();
  if (lane == 0) __hip_atomic_fetch_add(cnt, 1u, __ATOMIC_RELAXED, __HIP_MEMORY_SCOPE_WORKGROUP);
}
__device__ __forceinline__ void glds16(const void* gsrc, unsigned lds_dst) {
  unsigned keep;
  asm volatile("s_mov_b32 %0, m0\n\ts_mov_b32 m0, %2\n\ts_nop 0\n\tglobal_load_lds_dwordx4 %1, off\n\ts_mov_b32 m0, %0"
               : "=&s"(keep) : "v"(gsrc), "s"(lds_dst) : "memory");
}

__device__ __forceinline__ void glds16s(const void* sbase, unsigned voff, unsigned lds_dst) {
  unsigned keep;
  asm volatile("s_mov_b32 %0, m0\n\ts_mov_b32 m0, %3\n\ts_nop 0\n\tglobal_load_lds_dwordx4 %1, %2\n\ts_mov_b32 m0, %0"
               : "=&s"(keep) : "v"(voff), "s"(sbase), "s"(lds_dst) : "memory");
}

template <int MODE> __device__ __forceinline__ void st16(void* ptr, u32x4 v) {
  if (MODE == 0) *(u32x4*)ptr = v;
  else if (MODE == 1) __builtin_nontemporal_store(v, (u32x4*)ptr);
  else if (MODE == 2) asm volatile("global_store_dwordx4 %0, %1, off sc1\n\ts_nop 1" :: "v"(ptr), "v"(v) : "memory");
  else asm volatile("global_store_dwordx4 %0, %1, off sc0 sc1\n\ts_nop 1" :: "v"(ptr), "v"(v) : "memory");
}
typedef __attribute__((ext_vector_type(2))) short s16x2;
template <int V> struct IC { static constexpr int v = V; };
typedef __attribute__((ext_vector_type(3))) unsigned u32x3;
typedef u32x3 __attribute__((aligned(4))) u32x3_a4;
constexpr unsigned CODE1_SLICE = 768, CODE2_SLICE = 352;
constexpr int PB = 3;
constexpr int KT_P = 528;
constexpr int KT_CH = 54 * KT_P;
constexpr int L1_A = 0;
constexpr int L1_R = L1_A + 65536;
constexpr int L1_ST = L1_R + 2 * KT_CH;
constexpr int L1_CS = L1_ST + 1024;
constexpr int L1_C = L1_CS + 1536;
constexpr int L1_TH = L1_C + 64;
constexpr int L1_DUM = L1_TH + 2688 + 16;
constexpr int L1_SIZE = L1_DUM + 1024;

__device__ __forceinline__ void bsplit(f32x2 v, unsigned& hi, unsigned& lo) {
  hi = pkbf2(v);
  asm("" : "+v"(hi));
  lo = pkbf(v[0] - bflo(hi), v[1] - bfhi(hi));
}
template <int CTRL> __device__ __forceinline__ float dpp_max(float v) {
  const int t = __builtin_amdgcn_update_dpp(0, (int)f2u(v), CTRL, 0xf, 0xf, true);
  return __builtin_fmaxf(v, u2f((unsigned)t));
}
__device__ __forceinline__ float wave_max_nonneg(float v) {
  v = dpp_max<0x140>(dpp_max<0x141>(dpp_max<0x4E>(dpp_max<0xB1>(v))));
  const float a = u2f((unsigned)__builtin_amdgcn_readlane((int)f2u(v), 0));
  const float b = u2f((unsigned)__builtin_amdgcn_readlane((int)f2u(v), 16));
  const float c = u2f((unsigned)__builtin_amdgcn_readlane((int)f2u(v), 32));
  const float d = u2f((unsigned)__builtin_amdgcn_readlane((int)f2u(v), 48));
  return __builtin_fmaxf(__builtin_fmaxf(a, b), __builtin_fmaxf(c, d));
}

__global__ void __launch_bounds__(512, 2) k_phase1(Params p) {
  __shared__ __attribute__((aligned(16))) char lds[L1_SIZE];
  STAMP_DECL
  const int tid = TIDX, lane = tid & 63;
  const int w = __builtin_amdgcn_readfirstlane(tid >> 6);
  const int b = BIDX;
  LAS unsigned* cnt = (LAS unsigned*)(LAS char*)(lds + L1_C);
  if (w >= 6) {
    unsigned long long ca;
    if (w == 6) asm volatile("s_getpc_b64 s[90:91]\n\ts_add_u32 s90, s90, _Z8k_phase16Params@rel32@lo+4\n\ts_addc_u32 s91, s91, _Z8k_phase16Params@rel32@hi+12\n\ts_mov_b64 %0, s[90:91]" : "=s"(ca) :: "scc", "s90", "s91");
    else asm volatile("s_getpc_b64 s[90:91]\n\ts_add_u32 s90, s90, _Z8k_phase26Params@rel32@lo+4\n\ts_addc_u32 s91, s91, _Z8k_phase26Params@rel32@hi+12\n\ts_mov_b64 %0, s[90:91]" : "=s"(ca) :: "scc", "s90", "s91");
    const unsigned sl = (w == 6) ? CODE1_SLICE : CODE2_SLICE;
    unsigned off = (unsigned)(b >> 3) * sl + (unsigned)lane * 16u;
    off = ((unsigned)lane * 16u < sl) ? off : 0u;
    glds16s((const void*)ca, off, (unsigned)(size_t)(LAS char*)lds + L1_DUM);
  }
  if (tid < 16) cnt[tid] = 0u;
  __builtin_amdgcn_fence(__ATOMIC_RELEASE, "workgroup");
  WAITL();
  __builtin_amdgcn_s_barrier();
  __builtin_amdgcn_fence(__ATOMIC_ACQUIRE, "workgroup");
  STAMP_AT(0, 0);
  const int xcd = b & 7, idx = b >> 3;
  const int rt = (xcd >> 1) * 8 + (idx >> 2), cg = (xcd & 1) * 4 + (idx & 3);
  const int row0 = rt * 64;
  const int c = lane & 15, g = lane >> 4;
  const unsigned gl16 = (unsigned)((g ^ c) * 16);
  const char* arow = lds + L1_A + c * 1024;
  if (w >= 4) {
    const int gi = w - 4, ch = gi & 1, kh = gi >> 1;
    const int col0 = (cg == 7) ? 1280 : cg * 192 + 96 * ch;
    const bool gw = (cg < 7) || (ch == 0);
    const int rot = rt & 7;
    const unsigned lbase = (unsigned)(size_t)(LAS char*)lds;
    if (gw) {
      const char* bsrc = (const char*)p.w1 + (size_t)(256 * kh) * (DFF * 2) + col0 * 2;
      const unsigned bvo = (unsigned)(8 * g * (DFF * 2) + 12 * c);
      u32x3 bq[PB][8];
      auto issueB = [&](int s) {
        const char* q = bsrc + (size_t)((s + rot) & 7) * (32 * DFF * 2);
#pragma unroll
        for (int j = 0; j < 8; ++j) bq[s % PB][j] = *(const u32x3_a4*)(q + j * (DFF * 2) + bvo);
      };
      lds_wait(cnt + 3, 4);
#pragma unroll
      for (int s = 0; s < PB; ++s) issueB(s);
      SB();
      STAMP_AT(0, 4);
      f32x4 acc[4][6];
      f32x4 acs[6];
      const s16x8 onesf = {0x3f80, 0x3f80, 0x3f80, 0x3f80, 0x3f80, 0x3f80, 0x3f80, 0x3f80};
#pragma unroll
      for (int t = 0; t < 6; ++t) acs[t] = (f32x4){0.f, 0.f, 0.f, 0.f};
#pragma unroll
      for (int i = 0; i < 4; ++i)
#pragma unroll
        for (int t = 0; t < 6; ++t) acc[i][t] = (f32x4){0.f, 0.f, 0.f, 0.f};
      lds_wait(cnt + 0, 4);
      STAMP_AT(0, 1);
      auto mkF = [&](int s, int t) -> s16x8 {
        u32x4 F;
#pragma unroll
        for (int q = 0; q < 4; ++q)
          F[q] = __builtin_amdgcn_perm(bq[s % PB][2 * q + 1][t >> 1], bq[s % PB][2 * q][t >> 1], (t & 1) ? 0x07060302u : 0x05040100u);
        return __builtin_bit_cast(s16x8, F);
      };
      s16x8 Fc = mkF(0, 0);
#pragma unroll
      for (int s = 0; s < 8; ++s) {
        s16x8 af[4];
        {
          const char* ap = arow + (gl16 ^ (unsigned)((8 * kh + ((s + rot) & 7)) * 64));
#pragma unroll
          for (int mt = 0; mt < 4; ++mt) af[mt] = *(const s16x8*)(ap + mt * 16384);
        }
        const char* q = bsrc + (size_t)((s + PB - 1 + rot) & 7) * (32 * DFF * 2);
#pragma unroll
        for (int t = 0; t < 6; ++t) {
          s16x8 Fn = Fc;
          if (t + 1 < 6) Fn = mkF(s, t + 1);
          else if (s + 1 < 8) Fn = mkF(s + 1, 0);
          if (s >= 1 && s + PB - 1 < 8 && t < 4) {
            bq[(s + PB - 1) % PB][2 * t] = *(const u32x3_a4*)(q + (2 * t) * (DFF * 2) + bvo);
            bq[(s + PB - 1) % PB][2 * t + 1] = *(const u32x3_a4*)(q + (2 * t + 1) * (DFF * 2) + bvo);
          }
          SB();
#pragma unroll
          for (int mt = 0; mt < 4; ++mt)
            acc[mt][t] = __builtin_amdgcn_mfma_f32_16x16x32_bf16(Fc, af[mt], acc[mt][t], 0, 0, 0);
          acs[t] = __builtin_amdgcn_mfma_f32_16x16x32_bf16(Fc, onesf, acs[t], 0, 0, 0);
          SB();
          Fc = Fn;
        }
      }
      STAMP_AT(0, 2);
      int tz = TIDX;
      asm volatile("" : "+v"(tz));
      {
      const int lane = tz & 63, c = tz & 15, g = (tz >> 4) & 3;
      lds_wait2(cnt + 8, 2, cnt + 9, 2);
      f32x4* rx = (f32x4*)(lds + L1_R + gi * 12288);
      const f32x4* ro = (const f32x4*)(lds + L1_R + (gi ^ 2) * 12288);
      f32x4* csl = (f32x4*)(lds + L1_CS);
      auto fin = [&](auto KH) {
        constexpr int kk = decltype(KH)::v;
#pragma unroll
        for (int m = 0; m < 2; ++m)
#pragma unroll
          for (int t = 0; t < 6; ++t) rx[(m * 6 + t) * 64 + lane] = acc[2 * (1 - kk) + m][t];
        if (c == 0) {
#pragma unroll
          for (int t = 0; t < 6; ++t) csl[(gi * 6 + t) * 4 + g] = acs[t];
        }
        lds_signal(cnt + 5 + ch, lane);
        lds_wait2(cnt + 5 + ch, 2, cnt + 4, 4);
        f32x4 cso[6], oo[2][6], stv[2];
#pragma unroll
        for (int t = 0; t < 6; ++t) cso[t] = csl[((gi ^ 2) * 6 + t) * 4 + g];
#pragma unroll
        for (int m = 0; m < 2; ++m) {
          stv[m] = *(const f32x4*)(lds + L1_ST + (16 * (2 * kk + m) + c) * 16);
#pragma unroll
          for (int t = 0; t < 6; ++t) oo[m][t] = ro[(m * 6 + t) * 64 + lane];
        }
        SB();
#pragma unroll
        for (int t = 0; t < 6; ++t) acs[t] += cso[t];
#pragma unroll
        for (int m = 0; m < 2; ++m) {
          const int mt = 2 * kk + m;
          const f32x4 st = stv[m];
          float hv[24];
#pragma unroll
          for (int t = 0; t < 6; ++t) {
#pragma unroll
            for (int r = 0; r < 4; ++r)
              hv[6 * r + t] = __builtin_fmaf(-st[0], acs[t][r], acc[2 * kk + m][t][r] + oo[m][t][r]) * st[2];
          }
#pragma unroll
          for (int ck = 0; ck < 3; ++ck) {
            const int F0 = col0 + 24 * g + 8 * ck;
            u32x4 o;
#pragma unroll
            for (int e = 0; e < 4; ++e) {
              const s16x2 pv = __builtin_bit_cast(s16x2, pkbf(hv[8 * ck + 2 * e], hv[8 * ck + 2 * e + 1]));
              o[e] = __builtin_bit_cast(unsigned, __builtin_elementwise_max(pv, (s16x2){0, 0}));
            }
            st16<0>(p.ws + WS_H + ((size_t)(rt * 4 + mt) * 43 + (F0 >> 5)) * 1024 + (c + 16 * ((F0 >> 3) & 3)) * 16, o);
          }
        }
      };
      if (kh == 0) fin(IC<0>{}); else fin(IC<1>{});
      }
      STAMP_AT(0, 3);
    }
  } else {
    const int kw = w, chl = kw >> 1, nh = kw & 1;
    const int d = 2 * b + chl;
    char* tch = lds + L1_R + chl * KT_CH;
    unsigned short* th = (unsigned short*)(lds + L1_TH + chl * 1344);
    float a2, fr, wsc_inv;
    f32x2 W;
    bool grow;
    u32x4 xq[16];
    u32x2 w2r[8];
    const int blk = 4 * b + kw;
    const bool dow2 = blk < 344;
    {
      {
        const char* xr = (const char*)p.x + (size_t)(row0 + 16 * kw) * 1024 + lane * 16;
#pragma unroll
        for (int i = 0; i < 16; ++i) xq[i] = *(const u32x4*)(xr + i * 1024);
      }
      SB();
      const size_t o = (size_t)d * NS + 64 * nh + lane, o2 = o ^ 64;
      const float lg = bf2f(p.logdt[d]);
      const float are = bf2f(p.Are[o]), aim = bf2f(p.Aim[o]);
      const float bre = bf2f(p.Bre[o]), bim = bf2f(p.Bim[o]);
      const float cr = bf2f(p.Cre[o]), ci = bf2f(p.Cim[o]);
      const float are2 = bf2f(p.Are[o2]);
      const float bre2 = bf2f(p.Bre[o2]), bim2 = bf2f(p.Bim[o2]);
      const float cr2 = bf2f(p.Cre[o2]), ci2 = bf2f(p.Cim[o2]);
      if (dow2) {
        const int ct = blk / 43, ks = blk % 43;
        const unsigned short* wsrc = p.w2 + (size_t)(32 * ks + 8 * g) * DM + 64 * ct + 4 * c;
#pragma unroll
        for (int jj = 0; jj < 8; ++jj) w2r[jj] = *(const u32x2*)(wsrc + (size_t)jj * DM);
      }
      SB();
      if (lane == 0) __hip_atomic_fetch_add(cnt + 3, 1u, __ATOMIC_RELAXED, __HIP_MEMORY_SCOPE_WORKGROUP);
      SB();
      const float dt = rbf(exp_acc(lg));
      const float br = dt * bre, bi = dt * bim;
      a2 = (dt * are) * 1.4426950408889634f;
      fr = (dt * aim) * 0.15915494309189535f;
      W = (f32x2){cr * br - ci * bi, -(cr * bi + ci * br)};
      const float m1 = (__builtin_fabsf(cr) + __builtin_fabsf(ci)) * (__builtin_fabsf(bre) + __builtin_fabsf(bim));
      const float m2 = (__builtin_fabsf(cr2) + __builtin_fabsf(ci2)) * (__builtin_fabsf(bre2) + __builtin_fabsf(bim2));
      const float mw = wave_max_nonneg(__builtin_fmaxf(m1, m2)) * dt;
      const int eb = (int)(f2u(mw) >> 23);
      int ex = 136 - eb;
      ex = ex > 120 ? 120 : (ex < -120 ? -120 : ex);
      const float wsc = u2f((unsigned)(127 + ex) << 23);
      wsc_inv = u2f((unsigned)(127 - ex) << 23);
      W = (f32x2){W[0] * wsc, W[1] * wsc};
      const float gmax = __builtin_fmaxf(dt * are, dt * are2);
      grow = __builtin_amdgcn_ballot_w64(!(gmax * 2048.f < 2.f) || eb >= 250) != 0ull;
    }
    STAMP_AT(0, 1);
    auto cexpq = [&](float q) -> f32x2 {
      const float e = __builtin_amdgcn_exp2f(a2 * q);
      const float ph = __builtin_amdgcn_fractf(fr * q);
      return (f32x2){__builtin_amdgcn_cosf(ph) * e, __builtin_amdgcn_sinf(ph) * e};
    };
    auto cmul = [](f32x2 a, f32x2 bb) -> f32x2 {
      return (f32x2){__builtin_fmaf(a[0], bb[0], -(a[1] * bb[1])), __builtin_fmaf(a[0], bb[1], a[1] * bb[0])};
    };
    char* tw = tch + (64 * nh + lane) * 4;
    auto put = [&](int row, f32x2 v) {
      float v0 = v[0], v1 = v[1];
      asm("" : "+v"(v0), "+v"(v1));
      *(unsigned*)(tw + row * KT_P) = pkh(v0, v1);
    };
    auto xstore = [&]() {
#pragma unroll
      for (int i = 0; i < 16; ++i) *(u32x4*)(lds + L1_A + (16 * kw + i) * 1024 + ((lane ^ i) * 16)) = xq[i];
      lds_signal(cnt + 0, lane);
      if (dow2) {
        u32x4 f4[4];
#pragma unroll
        for (int q2 = 0; q2 < 4; ++q2) {
          f4[0][q2] = __builtin_amdgcn_perm(w2r[2 * q2 + 1][0], w2r[2 * q2][0], 0x05040100u);
          f4[1][q2] = __builtin_amdgcn_perm(w2r[2 * q2 + 1][0], w2r[2 * q2][0], 0x07060302u);
          f4[2][q2] = __builtin_amdgcn_perm(w2r[2 * q2 + 1][1], w2r[2 * q2][1], 0x05040100u);
          f4[3][q2] = __builtin_amdgcn_perm(w2r[2 * q2 + 1][1], w2r[2 * q2][1], 0x07060302u);
        }
        char* dst2 = p.ws + WS_W2F + (size_t)blk * 4096 + lane * 16;
#pragma unroll
        for (int t = 0; t < 4; ++t) st16<2>(dst2 + t * 1024, f4[t]);
      }
    };
    xstore();
    SB();
    if (!grow) {
      const f32x2 E1 = cexpq(1.f), E256 = cexpq(256.f), E8 = cexpq(8.f);
      f32x2 y0 = (f32x2){1.f, 0.f}, y1 = cexpq(64.f), y2 = cexpq(128.f), y3 = cexpq(192.f);
      const f32x2 C1 = {E1[0], -E1[1]}, CL = {E256[0], -E256[1]};
      const f32x2 C2 = cmul(C1, C1), C4 = cmul(C2, C2);
      f32x2 xs[21];
      xs[0] = W;
      xs[8] = cmul(W, CL);
      xs[1] = cmul(xs[0], C1);  xs[12] = cmul(xs[8], CL);  xs[9] = cmul(xs[8], C2);
      xs[2] = cmul(xs[1], C1);  xs[14] = cmul(xs[12], CL); xs[10] = cmul(xs[9], C2); xs[13] = cmul(xs[12], C4);
      xs[3] = cmul(xs[2], C1);  xs[16] = cmul(xs[14], CL); xs[11] = cmul(xs[10], C2); xs[15] = cmul(xs[14], C4);
      xs[4] = cmul(xs[0], C4);  xs[17] = cmul(xs[16], CL);
      xs[5] = cmul(xs[1], C4);  xs[18] = cmul(xs[17], CL);
      xs[6] = cmul(xs[2], C4);  xs[19] = cmul(xs[18], CL);
      xs[7] = cmul(xs[3], C4);  xs[20] = cmul(xs[19], CL);
#pragma unroll
      for (int i = 0; i < 21; ++i) put(i, xs[i]);
      put(21, y0); put(29, y1); put(37, y2); put(45, y3);
#pragma unroll
      for (int i = 1; i < 8; ++i) {
        y0 = cmul(y0, E8); y1 = cmul(y1, E8); y2 = cmul(y2, E8); y3 = cmul(y3, E8);
        put(21 + i, y0); put(29 + i, y1); put(37 + i, y2); put(45 + i, y3);
      }
    } else {
      *(f32x4*)(tch + (64 * nh + lane) * 16) = (f32x4){a2, fr, W[0], -W[1]};
    }
    lds_signal(cnt + 1 + chl, lane);
    STAMP_AT(0, 4);
    {
      lds_wait(cnt + 0, 4);
      const s16x8 onesf = {0x3f80, 0x3f80, 0x3f80, 0x3f80, 0x3f80, 0x3f80, 0x3f80, 0x3f80};
      f32x4 as0 = {0.f, 0.f, 0.f, 0.f}, aq0 = as0, as1 = as0, aq1 = as0;
      const char* ab2 = arow + kw * 16384;
      s16x8 xf[16];
#pragma unroll
      for (int s = 0; s < 16; ++s) xf[s] = *(const s16x8*)(ab2 + (gl16 ^ (unsigned)(s * 64)));
      SB();
#pragma unroll
      for (int s = 0; s < 16; s += 2) {
        as0 = __builtin_amdgcn_mfma_f32_16x16x32_bf16(onesf, xf[s], as0, 0, 0, 0);
        aq0 = __builtin_amdgcn_mfma_f32_16x16x32_bf16(xf[s], xf[s], aq0, 0, 0, 0);
        as1 = __builtin_amdgcn_mfma_f32_16x16x32_bf16(onesf, xf[s + 1], as1, 0, 0, 0);
        aq1 = __builtin_amdgcn_mfma_f32_16x16x32_bf16(xf[s + 1], xf[s + 1], aq1, 0, 0, 0);
      }
      const int r3 = c & 3;
      const f32x4 aq = aq0 + aq1;
      const float q = (r3 == 0) ? aq[0] : (r3 == 1) ? aq[1] : (r3 == 2) ? aq[2] : aq[3];
      const float mean = (as0[0] + as1[0]) * (1.f / DM);
      const float var = __builtin_fmaxf(q * (1.f / DM) - mean * mean, 0.f);
      const float mb = rbf(mean);
      const float dn = rbf(__builtin_sqrtf(rbf(rbf(var) + 1e-5f)));
      if (g == (c >> 2)) {
        const int r = 16 * kw + c;
        *(f32x4*)(lds + L1_ST + r * 16) = (f32x4){mb, dn, 1.f / dn, 0.f};
        if (cg == 0) *(f32x2*)(p.ws + WS_ST + (size_t)(row0 + r) * 8) = (f32x2){mb, dn};
      }
      lds_signal(cnt + 4, lane);
    }
    STAMP_AT(0, 8);
    lds_wait(cnt + 1 + chl, 2);
    f32x4 acc[2];
    acc[0] = (f32x4){0.f, 0.f, 0.f, 0.f}; acc[1] = acc[0];
    const int j = 16 * nh + c;
    if (!grow) {
      const char* fa = tch + c * KT_P + 16 * g;
      const char* fb = tch + (21 + j) * KT_P + 16 * g;
      f16x8 fA0[8], fA1[8], fB[8];
#pragma unroll
      for (int s = 0; s < 8; ++s) {
        fB[s] = *(const f16x8*)(fb + 64 * s);
        fA0[s] = *(const f16x8*)(fa + 64 * s);
        fA1[s] = *(const f16x8*)(fa + 16 * KT_P + 64 * s);
      }
      SB();
#pragma unroll
      for (int s = 0; s < 8; ++s) {
        acc[0] = __builtin_amdgcn_mfma_f32_16x16x32_f16(fA0[s], fB[s], acc[0], 0, 0, 0);
        acc[1] = __builtin_amdgcn_mfma_f32_16x16x32_f16(fA1[s], fB[s], acc[1], 0, 0, 0);
      }
    } else {
      for (int e = 0; e < 8; ++e) {
        const int row = 16 * (e >> 2) + 4 * g + (e & 3);
        const int qx = row < 8 ? row : row < 12 ? 256 + 2 * (row - 8) : row < 16 ? 512 + 256 * ((row - 12) >> 1) + 4 * (row & 1)
                                                                   : row < 21 ? 1024 + 256 * (row - 16) : 0;
        const float q = (float)(qx + 8 * j);
        float sacc = 0.f;
        for (int n = 0; n < 128; ++n) {
          const f32x4 pr = *(const f32x4*)(tch + n * 16);
          const float ph = __builtin_amdgcn_fractf(pr[1] * q);
          sacc += __builtin_amdgcn_exp2f(pr[0] * q) * (pr[2] * __builtin_amdgcn_cosf(ph) - pr[3] * __builtin_amdgcn_sinf(ph));
        }
        if (e < 4) acc[0][e & 3] = sacc; else acc[1][e & 3] = sacc;
      }
    }
    STAMP_AT(0, 5);
    {
      const bool v1 = (g == 0), v2 = (g == 1 && j == 0);
      float mx = __builtin_fmaxf(__builtin_fmaxf(__builtin_fabsf(acc[0][0]), __builtin_fabsf(acc[0][1])),
                                 __builtin_fmaxf(__builtin_fabsf(acc[0][2]), __builtin_fabsf(acc[0][3])));
      const float m1 = __builtin_fmaxf(__builtin_fmaxf(__builtin_fabsf(acc[1][1]), __builtin_fabsf(acc[1][2])), __builtin_fabsf(acc[1][3]));
      mx = __builtin_fmaxf(mx, (v1 || v2) ? __builtin_fabsf(acc[1][0]) : 0.f);
      mx = __builtin_fmaxf(mx, v1 ? m1 : 0.f);
      mx = wave_max_nonneg(mx);
      float* mxl = (float*)(lds + L1_TH + 2688) + kw;
      if (lane == 0) *mxl = mx;
      lds_signal(cnt + 8 + chl, lane);
    {
      lds_wait(cnt + 4, 4);
      const int q4 = c >> 2, pp = c & 3;
      f32x4 st[16];
#pragma unroll
      for (int r = 0; r < 16; ++r) st[r] = *(const f32x4*)(lds + L1_ST + (16 * g + r) * 16);
      const int ck = 8 * cg + 2 * kw + (pp >> 1);
      unsigned ov[8];
      u32x2 vv[4];
#pragma unroll
      for (int k = 0; k < 4; ++k) {
        const int r16 = 4 * k + q4;
        const char* a0 = lds + L1_A + (16 * g + r16) * 1024 + ((ck ^ r16) * 16) + 8 * (pp & 1);
        vv[k] = __builtin_bit_cast(u32x2, __builtin_amdgcn_ds_read_tr16_b64_v4i16((LAS s16x4*)a0));
      }
      SB();
#pragma unroll
      for (int k = 0; k < 4; ++k) {
        const u32x2 v = vv[k];
#pragma unroll
        for (int e = 0; e < 2; ++e) {
          const unsigned ve = v[e];
          const f32x4 s0 = st[4 * k + 2 * e], s1 = st[4 * k + 2 * e + 1];
          const float t0 = rbf(bflo(ve) - s0[0]), t1 = rbf(bfhi(ve) - s1[0]);
          float q0 = t0 * s0[2], q1 = t1 * s1[2];
          q0 = __builtin_fmaf(__builtin_fmaf(-q0, s0[1], t0), s0[2], q0);
          q1 = __builtin_fmaf(__builtin_fmaf(-q1, s1[1], t1), s1[2], q1);
          const unsigned xb = pkbf(q0, q1);
          ov[2 * k + e] = pkh(bflo(xb), bfhi(xb));
        }
      }
      unsigned short* dst = (unsigned short*)(p.ws + WS_XNT) + (size_t)(64 * cg + 16 * kw + c) * SL + row0 + 16 * g;
      st16<2>(dst, (u32x4){ov[0], ov[1], ov[2], ov[3]});
      st16<2>(dst + 8, (u32x4){ov[4], ov[5], ov[6], ov[7]});
    }
    {
      char* ydst = p.ws + WS_YT + (size_t)b * 8192 + kw * 2048 + lane * 16;
      const u32x4 sent = {0xffffffffu, 0xffffffffu, 0xffffffffu, 0xffffffffu};
      st16<2>(ydst, sent);
      st16<2>(ydst + 1024, sent);
    }
      lds_wait(cnt + 8 + chl, 2);
      mx = __builtin_fmaxf(mx, ((const float*)(lds + L1_TH + 2688))[kw ^ 1]);
      const int eb = (int)(f2u(mx) >> 23);
      int ex = 140 - eb;
      ex = (eb == 0 || eb == 255) ? 0 : (ex > 120 ? 120 : ex);
      const float sc = u2f((unsigned)(127 + ex) << 23);
      if (lane == 0 && nh == 0) ((float*)(p.ws + WS_KS))[d] = u2f((unsigned)(127 - ex) << 23) * wsc_inv;
      const int b0 = (g < 2 ? 4 * g + 8 * j : g == 2 ? 256 + 4 * j : 384 + 2 * j);
      *(unsigned*)(th + b0) = pkh(acc[0][0] * sc, acc[0][1] * sc);
      *(unsigned*)(th + b0 + (g == 3 ? 64 : 2)) = pkh(acc[0][2] * sc, acc[0][3] * sc);
      const unsigned p01 = pkh(acc[1][0] * sc, acc[1][1] * sc), p23 = pkh(acc[1][2] * sc, acc[1][3] * sc);
      if (v1) {
        th[512 + j] = (unsigned short)p01; th[544 + j] = (unsigned short)(p01 >> 16);
        th[576 + j] = (unsigned short)p23; th[608 + j] = (unsigned short)(p23 >> 16);
      }
      if (v2) th[640] = (unsigned short)p01;
      lds_signal(cnt + 10 + chl, lane);
    }
    STAMP_AT(0, 6);
    STAMP_AT(0, 7);
    {
      lds_wait(cnt + 10 + chl, 2);
      const unsigned short* tu = th;
      unsigned short* kr = (unsigned short*)(p.ws + WS_KR) + (size_t)d * KRL;
      if (nh == 0) {
#pragma unroll
        for (int k = 0; k < 2; ++k) {
          const int ck = lane + 64 * k, M = 256 - ck;
          const unsigned a = tu[384 + M], bb = tu[383 + M];
          const unsigned aa = a | (a << 16), b2 = bb | (bb << 16);
          const unsigned mid = ((M & 1) ? bb : a) | (bb << 16);
          st16<2>(kr + 8 * ck, (u32x4){aa, aa, mid, b2});
        }
        if (lane < 4) st16<2>(kr + 8 * (256 + lane), (u32x4){lane == 0 ? (unsigned)tu[0] : 0u, 0u, 0u, 0u});
      } else {
        {
          const int ck = 128 + lane, M = 256 - ck;
          const unsigned a = tu[256 + 2 * M], bb = tu[255 + 2 * M], cc = tu[254 + 2 * M];
          st16<2>(kr + 8 * ck, (u32x4){a | (a << 16), a | (bb << 16), bb | (bb << 16), cc | (cc << 16)});
        }
        {
          const int ck = 192 + lane, M = 256 - ck;
          const bool s1 = lane < 32;
          const int base = s1 ? 128 + 4 * M : 8 * M;
          const int o1[8] = {0, 0, 1, 2, 2, 2, 3, 4};
          unsigned hv[8];
#pragma unroll
          for (int e = 0; e < 8; ++e) hv[e] = tu[base - (s1 ? o1[e] : e)];
          st16<2>(kr + 8 * ck, (u32x4){hv[0] | (hv[1] << 16), hv[2] | (hv[3] << 16), hv[4] | (hv[5] << 16), hv[6] | (hv[7] << 16)});
        }
      }
    }
    STAMP_AT(0, 3);
    STAMP_AT(0, 9);
  }
  STAMP_FLUSH(0);
}

#define GAS __attribute__((address_space(1)))
constexpr int KCS = KRL * 2;
constexpr int CV_CH = 8 * KCS + 4608;
constexpr int L2_CR = 2 * CV_CH;
constexpr int L2_GR = L2_CR + 16384;
constexpr int L2_Y = L2_GR + 49152;
constexpr int L2_T1 = L2_Y + 64 * 136;
constexpr int L2_C = L2_T1 + 8192;
constexpr int L2_SIZE = L2_C + 64;

__global__ void __launch_bounds__(512, 2) k_phase2(Params p) {
  __shared__ __attribute__((aligned(16))) char lds[L2_SIZE];
  STAMP_DECL
  const int tid = TIDX, lane = tid & 63;
  const int w = __builtin_amdgcn_readfirstlane(tid >> 6);
  const int b = BIDX;
  LAS unsigned* cnt = (LAS unsigned*)(LAS char*)(lds + L2_C);
  if (tid < 16) cnt[tid] = 0u;
  wg_sync();
  STAMP_AT(1, 0);
  const int xcd = b & 7, idx = b >> 3;
  const int rt = xcd * 4 + (idx >> 3), ct = idx & 7;
  const int row0 = rt * 64, col0 = ct * 64;
  const int c = lane & 15, g = lane >> 4;
  GAS unsigned* flags = (GAS unsigned*)(p.ws + WS_FL);
  if (w < 4) {
    const int chl = w >> 1, hf = w & 1;
    const int d = 2 * b + chl;
    char* kl = lds + chl * CV_CH;
    char* ul = kl + 8 * KCS;
    const int erow = row0 + 16 * w + 4 * g;
    u32x2 xv[4];
    f32x4 st01, st23;
    u32x2 dk;
    float isc;
    {
      const int t2 = tid & 127;
      const u32x4* ksrc = (const u32x4*)(p.ws + WS_KR + (size_t)d * KCS);
      const u32x4* usrc = (const u32x4*)(p.ws + WS_XNT + (size_t)d * SL * 2);
      u32x4 kc[3], kp[3];
#pragma unroll
      for (int k = 0; k < 3; ++k) {
        int ci = t2 + 128 * k;
        ci = ci < 260 ? ci : 259;
        kc[k] = ksrc[ci];
        kp[k] = ksrc[ci > 0 ? ci - 1 : 0];
      }
      u32x4 uv0 = usrc[t2], uv1 = usrc[t2 + 128];
      SB();
#pragma unroll
      for (int r = 0; r < 4; ++r) xv[r] = *(const u32x2*)(p.x + (size_t)(erow + r) * DM + col0 + 4 * c);
      st01 = *(const f32x4*)(p.ws + WS_ST + (size_t)erow * 8);
      st23 = *(const f32x4*)(p.ws + WS_ST + (size_t)erow * 8 + 16);
      dk = *(const u32x2*)(p.Dsk + col0 + 4 * c);
      isc = ((const float*)(p.ws + WS_KS))[d];
      SB();
      const u32x4 z = {0u, 0u, 0u, 0u};
      if (t2 < 32) *(u32x4*)(ul + t2 * 16) = z;
#pragma unroll
      for (int k = 0; k < 3; ++k) {
        const int ci = t2 + 128 * k;
        if (ci < 260) {
          unsigned wd[8] = {kp[k][0], kp[k][1], kp[k][2], kp[k][3], kc[k][0], kc[k][1], kc[k][2], kc[k][3]};
          unsigned od[7];
#pragma unroll
          for (int j = 0; j < 7; ++j) od[j] = __builtin_amdgcn_alignbit(wd[j + 1], wd[j], 16);
          char* dst = kl + ci * 16;
#pragma unroll
          for (int sg = 0; sg < 8; ++sg) {
            u32x4 o;
            if ((sg & 1) == 0) { const int s0 = (8 - sg) >> 1; o = (u32x4){wd[s0], wd[s0 + 1], wd[s0 + 2], wd[s0 + 3]}; }
            else { const int s0 = (7 - sg) >> 1; o = (u32x4){od[s0], od[s0 + 1], od[s0 + 2], od[s0 + 3]}; }
            *(u32x4*)(dst + sg * KCS) = o;
          }
        }
      }
      *(u32x4*)(ul + 512 + t2 * 16) = uv0;
      *(u32x4*)(ul + 512 + (t2 + 128) * 16) = uv1;
    }
    lds_signal(cnt + chl, lane);
    lds_wait(cnt + chl, 2);
    STAMP_AT(1, 1);
    const int a = c;
    f32x4 acc[8];
#pragma unroll
    for (int r = 0; r < 8; ++r) acc[r] = (f32x4){0.f, 0.f, 0.f, 0.f};
    const char* ab = kl + (a & 7) * KCS + 2 * (2032 + 8 * g - 8 * (a >> 3)) - 64 * 4 * hf;
    const char* bb = ul + 2 * (240 + 16 * a + 8 * g) - 64 * 4 * hf;
    f16x8 bf[2][8], af[2][8];
#pragma unroll
    for (int t = 0; t < 8; ++t) { bf[0][t] = *(const f16x8*)(bb + 512 * t); af[0][t] = *(const f16x8*)(ab - 512 * t); }
#pragma unroll
    for (int i = 0; i < 4; ++i) {
      if (i + 1 < 4) {
#pragma unroll
        for (int t = 0; t < 8; ++t) {
          bf[(i + 1) & 1][t] = *(const f16x8*)(bb - 64 * (i + 1) + 512 * t);
          af[(i + 1) & 1][t] = *(const f16x8*)(ab - 64 * (i + 1) - 512 * t);
        }
      }
      SB();
#pragma unroll
      for (int v = 0; v < 8; ++v)
#pragma unroll
        for (int t = v; t < 8; ++t)
          acc[t] = __builtin_amdgcn_mfma_f32_16x16x32_f16(af[i & 1][v], bf[i & 1][t - v], acc[t], 0, 0, 0);
      SB();
    }
    STAMP_AT(1, 2);
    f32x4* red = (f32x4*)(lds + L2_CR + chl * 8192);
    f32x4 mine[4];
    if (hf) {
#pragma unroll
      for (int r = 0; r < 4; ++r) { red[(4 + r) * 64 + lane] = acc[r]; mine[r] = acc[4 + r]; }
    } else {
#pragma unroll
      for (int r = 0; r < 4; ++r) { red[r * 64 + lane] = acc[4 + r]; mine[r] = acc[r]; }
    }
    lds_signal(cnt + 2 + chl, lane);
    lds_wait(cnt + 2 + chl, 2);
    {
      unsigned pk[4][2];
#pragma unroll
      for (int r = 0; r < 4; ++r) {
        const f32x4 v = (mine[r] + red[(4 * (1 - hf) + r) * 64 + lane]) * isc;
        pk[r][0] = pkbf(v[0], v[1]);
        pk[r][1] = pkbf(v[2], v[3]);
      }
      const auto rsrc = __builtin_amdgcn_make_buffer_rsrc((void*)(p.ws + WS_YT), 0, DM * SL * 2, 0x00020000);
#pragma unroll
      for (int r = 0; r < 2; ++r) {
        auto s0 = __builtin_amdgcn_permlane16_swap(pk[r][0], pk[r + 2][0], false, false);
        auto s1 = __builtin_amdgcn_permlane16_swap(pk[r][1], pk[r + 2][1], false, false);
        const unsigned a0 = s0[0], b0 = s0[1], a1 = s1[0], b1 = s1[1];
        const u32x4 o = {a0, a1, b0, b1};
        const int tile = r + 2 * (g & 1);
        const int off = (d * SL + 1024 * hf + 256 * tile + 16 * a + 8 * (g >> 1)) * 2;
        __builtin_amdgcn_raw_buffer_store_b128(o, rsrc, off, 0, 16);
      }
    }
    STAMP_AT(1, 3);
    {
      const int ch = 16 * w + (lane >> 2), k0 = (lane & 3) * 2;
      const auto rsrc = __builtin_amdgcn_make_buffer_rsrc((void*)(p.ws + WS_YT), 0, DM * SL * 2, 0x00020000);
      const int off = ((col0 + ch) * SL + row0) * 2 + k0 * 16;
      float xf[4][4], xd[4][4];
      {
        const float dkf[4] = {bflo(dk[0]), bfhi(dk[0]), bflo(dk[1]), bfhi(dk[1])};
#pragma unroll
        for (int r = 0; r < 4; ++r) {
          xf[r][0] = bflo(xv[r][0]); xf[r][1] = bfhi(xv[r][0]); xf[r][2] = bflo(xv[r][1]); xf[r][3] = bfhi(xv[r][1]);
          const float mb = (r == 0) ? st01[0] : (r == 1) ? st01[2] : (r == 2) ? st23[0] : st23[2];
          const float den = (r == 0) ? st01[1] : (r == 1) ? st01[3] : (r == 2) ? st23[1] : st23[3];
          const float rc = 1.f / den;
#pragma unroll
          for (int t = 0; t < 4; ++t) {
            const float a0 = rbf(xf[r][t] - mb);
            float q = a0 * rc;
            q = __builtin_fmaf(__builtin_fmaf(-q, den, a0), rc, q);
            xd[r][t] = rbf(rbf(q) * dkf[t]);
            asm volatile("" : "+v"(xd[r][t]), "+v"(xf[r][t]));
          }
        }
      }
      u32x4 v0, v1;
      unsigned spins = 0;
      lds_wait(cnt + 7, 4);
      while (true) {
        v0 = __builtin_amdgcn_raw_buffer_load_b128(rsrc, off, 0, 16);
        v1 = __builtin_amdgcn_raw_buffer_load_b128(rsrc, off + 16, 0, 16);
        bool bad = false;
#pragma unroll
        for (int e = 0; e < 4; ++e) {
          const unsigned a0 = v0[e], a1 = v1[e];
          bad |= ((a0 & 0xffffu) == 0xffffu) | ((a0 >> 16) == 0xffffu) | ((a1 & 0xffffu) == 0xffffu) | ((a1 >> 16) == 0xffffu);
        }
        if (__builtin_amdgcn_ballot_w64(bad) == 0ull) break;
        if (++spins > (1u << 18)) break;
        __builtin_amdgcn_s_sleep(4);
      }
      STAMP_AT(1, 4);
      char* yd = lds + L2_Y + ch * 136 + k0 * 16;
      *(u32x2*)yd = (u32x2){v0[0], v0[1]}; *(u32x2*)(yd + 8) = (u32x2){v0[2], v0[3]};
      *(u32x2*)(yd + 16) = (u32x2){v1[0], v1[1]}; *(u32x2*)(yd + 24) = (u32x2){v1[2], v1[3]};
      lds_signal(cnt + 4, lane);
      lds_wait(cnt + 4, 4);
      u32x2 yv[4];
#pragma unroll
      for (int t = 0; t < 4; ++t) yv[t] = *(const u32x2*)(lds + L2_Y + (4 * c + t) * 136 + (16 * w + 4 * g) * 2);
#pragma unroll
      for (int r = 0; r < 4; ++r) {
        float o[4];
#pragma unroll
        for (int t = 0; t < 4; ++t) {
          const unsigned yw = yv[t][r >> 1];
          const float y = (r & 1) ? bfhi(yw) : bflo(yw);
          o[t] = xf[r][t] + rbf(y + xd[r][t]);
        }
        *(u32x2*)(lds + L2_T1 + ((w * 4 + r) * 64 + lane) * 8) = (u32x2){pkbf(o[0], o[1]), pkbf(o[2], o[3])};
      }
    }
    lds_signal(cnt + 8 + w, lane);
    STAMP_AT(1, 5);
  } else {
    const int gw = w - 4;
    const int s0 = 11 * gw;
    const int ns = (gw == 3) ? 10 : 11;
    const char* hb = p.ws + WS_H + ((size_t)(rt * 4) * 43 + s0) * 1024 + lane * 16;
    const char* wb = p.ws + WS_W2F + ((size_t)ct * 43 + s0) * 4096 + lane * 16;
    constexpr int P = 4;
    u32x4 ar[P][4];
    u32x4 br[P][4];
    const int rot = 0;

    auto issue = [&](int s) {
      int ks = s + rot;
      ks = ks >= ns ? ks - ns : ks;
#pragma unroll
      for (int mt = 0; mt < 4; ++mt) ar[s % P][mt] = *(const u32x4*)(hb + (size_t)mt * 43 * 1024 + ks * 1024);
#pragma unroll
      for (int j = 0; j < 4; ++j) br[s % P][j] = *(const u32x4*)(wb + ks * 4096 + j * 1024);
    };
#pragma unroll
    for (int s = 0; s < P; ++s) issue(s);
    SB();
    f32x4 acc[4][4];
#pragma unroll
    for (int i = 0; i < 4; ++i)
#pragma unroll
      for (int t = 0; t < 4; ++t) acc[i][t] = (f32x4){0.f, 0.f, 0.f, 0.f};
#pragma unroll
    for (int s = 0; s < 11; ++s) {
      if (s < ns) {
#pragma unroll
        for (int mt = 0; mt < 4; ++mt)
#pragma unroll
          for (int t = 0; t < 4; ++t)
            acc[mt][t] = __builtin_amdgcn_mfma_f32_16x16x32_bf16(__builtin_bit_cast(s16x8, ar[s % P][mt]),
                                                                 __builtin_bit_cast(s16x8, br[s % P][t]), acc[mt][t], 0, 0, 0);
        SB();
        if (s + P < 11 && s + P < ns) issue(s + P);
        if (s == 6 && lane == 0) __hip_atomic_fetch_add(cnt + 7, 1u, __ATOMIC_RELAXED, __HIP_MEMORY_SCOPE_WORKGROUP);
        SB();
      }
    }

    const int erow = row0 + 16 * gw + 4 * g;
    STAMP_AT(1, 1);
    f32x4* red = (f32x4*)(lds + L2_GR);
    f32x4 f[4];
#pragma unroll
    for (int mt = 0; mt < 4; ++mt) {
      if (mt != gw) {
        const int slot = (gw > mt) ? (gw - 1) : gw;
#pragma unroll
        for (int t = 0; t < 4; ++t) red[((mt * 3 + slot) * 4 + t) * 64 + lane] = acc[mt][t];
      } else {
#pragma unroll
        for (int t = 0; t < 4; ++t) f[t] = acc[mt][t];
      }
    }
    lds_signal(cnt + 6, lane);
    lds_wait(cnt + 6, 4);
    STAMP_AT(1, 2);
    f32x4 pr[4][3];
#pragma unroll
    for (int t = 0; t < 4; ++t)
#pragma unroll
      for (int sl = 0; sl < 3; ++sl) pr[t][sl] = red[((gw * 3 + sl) * 4 + t) * 64 + lane];
    SB();
#pragma unroll
    for (int t = 0; t < 4; ++t) {
      f[t] += (pr[t][0] + pr[t][1]) + pr[t][2];
#pragma unroll
      for (int r = 0; r < 4; ++r) f[t][r] = rbf(f[t][r]);
      asm volatile("" : "+v"(f[t]));
    }
    lds_wait(cnt + 8 + gw, 1);
    STAMP_AT(1, 3);
#pragma unroll
    for (int r = 0; r < 4; ++r) {
      const u32x2 tv = *(const u32x2*)(lds + L2_T1 + ((gw * 4 + r) * 64 + lane) * 8);
      const unsigned t0 = tv[0], t1 = tv[1];
      u32x2 ov;
      ov[0] = pkbf(bflo(t0) + f[0][r], bfhi(t0) + f[1][r]);
      ov[1] = pkbf(bflo(t1) + f[2][r], bfhi(t1) + f[3][r]);
      asm volatile("global_store_dwordx2 %0, %1, off sc1" :: "v"(p.out + (size_t)(erow + r) * DM + col0 + 4 * c), "v"(ov) : "memory");
    }
    STAMP_AT(1, 4);
  }
  STAMP_FLUSH(1);
}

#if defined(__HIP_DEVICE_COMPILE__)
#pragma clang attribute pop
#endif

extern "C" void kernel_launch(void* const* d_in, const int* in_sizes, int n_in,
                              void* d_out, int out_size, void* d_ws, size_t ws_size,
                              hipStream_t stream) {
  Params p;
  p.x = (const unsigned short*)d_in[0];
  p.Are = (const unsigned short*)d_in[1];
  p.Aim = (const unsigned short*)d_in[2];
  p.Bre = (const unsigned short*)d_in[3];
  p.Bim = (const unsigned short*)d_in[4];
  p.Cre = (const unsigned short*)d_in[5];
  p.Cim = (const unsigned short*)d_in[6];
  p.logdt = (const unsigned short*)d_in[7];
  p.Dsk = (const unsigned short*)d_in[8];
  p.w1 = (const unsigned short*)d_in[10];
  p.w2 = (const unsigned short*)d_in[11];
  p.out = (unsigned short*)d_out;
  p.ws = (char*)d_ws;
  k_phase1<<<256, 512, 0, stream>>>(p);
  k_phase2<<<256, 512, 0, stream>>>(p);
}
```

```cpp
#include <hip/hip_runtime.h>
#include <hip/hip_bf16.h>
#include <stdint.h>

typedef __attribute__((ext_vector_type(8))) short s16x8;
typedef __attribute__((ext_vector_type(4))) short s16x4;
typedef __attribute__((ext_vector_type(8))) _Float16 f16x8;
typedef __attribute__((ext_vector_type(4))) float f32x4;
typedef __attribute__((ext_vector_type(2))) float f32x2;
typedef __attribute__((ext_vector_type(16))) float f32x16;
typedef __attribute__((ext_vector_type(4))) unsigned u32x4;
typedef __attribute__((ext_vector_type(2))) unsigned u32x2;
typedef __attribute__((ext_vector_type(2))) __bf16 bf16x2_t;
typedef __attribute__((ext_vector_type(2))) _Float16 f16x2_t;
#define LAS __attribute__((address_space(3)))

constexpr int SL = 2048;
constexpr int DM = 512;
constexpr int NS = 128;
constexpr int DFF = 1376;
constexpr int KRL = 2080;

constexpr size_t WS_H = 0;
constexpr size_t WS_XNT = WS_H + (size_t)128 * 43 * 1024;
constexpr size_t WS_XNF = WS_XNT + (size_t)DM * SL * 2;
constexpr size_t WS_YT = WS_XNF + (size_t)DM * SL * 2;
constexpr size_t WS_ST = WS_YT + (size_t)DM * SL * 2;
constexpr size_t WS_FL = WS_ST + (size_t)SL * 8;
constexpr size_t WS_W2F = WS_FL + 32768;
constexpr size_t WS_W1F = WS_W2F + (size_t)344 * 4096;

#if defined(__HIP_DEVICE_COMPILE__)
#pragma clang attribute push(__attribute__((target("no-packed-fp32-ops"))), apply_to = function)
#endif
__device__ __forceinline__ float u2f(unsigned u) { return __builtin_bit_cast(float, u); }
__device__ __forceinline__ unsigned f2u(float f) { return __builtin_bit_cast(unsigned, f); }
__device__ __forceinline__ float shx(float v, int lane, int m) {
  return u2f((unsigned)__builtin_amdgcn_ds_bpermute((lane ^ m) << 2, (int)f2u(v)));
}
__device__ __forceinline__ void wg_sync() {
  __builtin_amdgcn_fence(__ATOMIC_RELEASE, "workgroup");
  __builtin_amdgcn_s_barrier();
  __builtin_amdgcn_fence(__ATOMIC_ACQUIRE, "workgroup");
}
__device__ __forceinline__ float exp_acc(float x) {
  const float L2E_HI = 1.44269502162933349609375f, L2E_LO = 1.925963033500011e-8f;
  const float hi = x * L2E_HI;
  const float lo = __builtin_fmaf(x, L2E_LO, __builtin_fmaf(x, L2E_HI, -hi));
  const float e = __builtin_amdgcn_exp2f(hi);
  return __builtin_fmaf(e, lo * 0.6931471805599453f, e);
}
#define TIDX ((int)__builtin_amdgcn_workitem_id_x())
#define BIDX ((int)__builtin_amdgcn_workgroup_id_x())
__device__ __forceinline__ float bf2f(unsigned h) { return u2f(h << 16); }
__device__ __forceinline__ float bflo(unsigned u) { return u2f(u << 16); }
__device__ __forceinline__ float bfhi(unsigned u) { return u2f(u & 0xffff0000u); }
__device__ __forceinline__ f32x2 bfup(unsigned u) { return (f32x2){bflo(u), bfhi(u)}; }
__device__ __forceinline__ unsigned pkbf(float lo, float hi) {
  bf16x2_t r = {(__bf16)lo, (__bf16)hi};
  return __builtin_bit_cast(unsigned, r);
}
__device__ __forceinline__ unsigned pkbf2(f32x2 v) { return pkbf(v[0], v[1]); }
__device__ __forceinline__ float rbf(float x) { return bflo(pkbf(x, 0.f)); }
__device__ __forceinline__ unsigned pkh(float lo, float hi) {
  f16x2_t r = {(_Float16)lo, (_Float16)hi};
  return __builtin_bit_cast(unsigned, r);
}

#define STAMP_DECL
#define STAMP_AT(kid, i) do {} while (0)
#define STAMP_FLUSH(kid) do {} while (0)
#define SB() __builtin_amdgcn_sched_barrier(0)
#define WAITV(n) asm volatile("s_waitcnt vmcnt(%0)" ::"n"(n) : "memory")
#define WAITL() asm volatile("s_waitcnt lgkmcnt(0)" ::: "memory")

struct Params {
  const unsigned short* x; const unsigned short* Are; const unsigned short* Aim;
  const unsigned short* Bre; const unsigned short* Bim; const unsigned short* Cre; const unsigned short* Cim;
  const unsigned short* logdt; const unsigned short* Dsk; const unsigned short* w1; const unsigned short* w2;
  unsigned short* out; char* ws;
};

__device__ __forceinline__ unsigned lds_arrive(LAS unsigned* cnt, int lane) {
  WAITL();
  unsigned old = 0;
  if (lane == 0) old = __hip_atomic_fetch_add(cnt, 1u, __ATOMIC_RELAXED, __HIP_MEMORY_SCOPE_WORKGROUP);
  return __builtin_amdgcn_readfirstlane(old);
}
__device__ __forceinline__ void lds_wait(LAS unsigned* cnt, unsigned target) {
  while (__builtin_amdgcn_readfirstlane(__hip_atomic_load(cnt, __ATOMIC_RELAXED, __HIP_MEMORY_SCOPE_WORKGROUP)) < target)
    __builtin_amdgcn_s_sleep(1);
  asm volatile("" ::: "memory");
}
__device__ __forceinline__ void lds_wait2(LAS unsigned* c0, unsigned t0, LAS unsigned* c1, unsigned t1) {
  while (true) {
    const unsigned a = __hip_atomic_load(c0, __ATOMIC_RELAXED, __HIP_MEMORY_SCOPE_WORKGROUP);
    const unsigned b = __hip_atomic_load(c1, __ATOMIC_RELAXED, __HIP_MEMORY_SCOPE_WORKGROUP);
    if (__builtin_amdgcn_readfirstlane(a) >= t0 && __builtin_amdgcn_readfirstlane(b) >= t1) break;
    __builtin_amdgcn_s_sleep(1);
  }
  asm volatile("" ::: "memory");
}
__device__ __forceinline__ void lds_signal(LAS unsigned* cnt, int lane) {
  WAITL();
  if (lane == 0) __hip_atomic_fetch_add(cnt, 1u, __ATOMIC_RELAXED, __HIP_MEMORY_SCOPE_WORKGROUP);
}
__device__ __forceinline__ void glds16(const void* gsrc, unsigned lds_dst) {
  unsigned keep;
  asm volatile("s_mov_b32 %0, m0\n\ts_mov_b32 m0, %2\n\ts_nop 0\n\tglobal_load_lds_dwordx4 %1, off\n\ts_mov_b32 m0, %0"
               : "=&s"(keep) : "v"(gsrc), "s"(lds_dst) : "memory");
}

__device__ __forceinline__ void glds16s(const void* sbase, unsigned voff, unsigned lds_dst) {
  unsigned keep;
  asm volatile("s_mov_b32 %0, m0\n\ts_mov_b32 m0, %3\n\ts_nop 0\n\tglobal_load_lds_dwordx4 %1, %2\n\ts_mov_b32 m0, %0"
               : "=&s"(keep) : "v"(voff), "s"(sbase), "s"(lds_dst) : "memory");
}

template <int MODE> __device__ __forceinline__ void st16(void* ptr, u32x4 v) {
  if (MODE == 0) *(u32x4*)ptr = v;
  else if (MODE == 1) __builtin_nontemporal_store(v, (u32x4*)ptr);
  else if (MODE == 2) asm volatile("global_store_dwordx4 %0, %1, off sc1\n\ts_nop 1" :: "v"(ptr), "v"(v) : "memory");
  else asm volatile("global_store_dwordx4 %0, %1, off sc0 sc1\n\ts_nop 1" :: "v"(ptr), "v"(v) : "memory");
}

#define GAS __attribute__((address_space(1)))
typedef __attribute__((ext_vector_type(2))) short s16x2;
template <int V> struct IC { static constexpr int v = V; };
typedef __attribute__((ext_vector_type(3))) unsigned u32x3;
typedef u32x3 __attribute__((aligned(4))) u32x3_a4;

template <int CTRL> __device__ __forceinline__ float dpp_max(float v) {
  const int t = __builtin_amdgcn_update_dpp(0, (int)f2u(v), CTRL, 0xf, 0xf, true);
  return __builtin_fmaxf(v, u2f((unsigned)t));
}
__device__ __forceinline__ float rdl(float v, int l) { return u2f((unsigned)__builtin_amdgcn_readlane((int)f2u(v), l)); }
__device__ __forceinline__ float wave_max_nonneg(float v) {
  v = dpp_max<0x140>(dpp_max<0x141>(dpp_max<0x4E>(dpp_max<0xB1>(v))));
  return __builtin_fmaxf(__builtin_fmaxf(rdl(v, 0), rdl(v, 16)), __builtin_fmaxf(rdl(v, 32), rdl(v, 48)));
}
template <int CTRL> __device__ __forceinline__ float dpp_add(float v) {
  const int t = __builtin_amdgcn_update_dpp(0, (int)f2u(v), CTRL, 0xf, 0xf, true);
  return v + u2f((unsigned)t);
}
__device__ __forceinline__ float red32(float p, float q) {
  auto r = __builtin_amdgcn_permlane32_swap(f2u(p), f2u(q), false, false);
  const unsigned r0 = r[0], r1 = r[1];
  return u2f(r0) + u2f(r1);
}
__device__ __forceinline__ float red16(float p, float q) {
  auto r = __builtin_amdgcn_permlane16_swap(f2u(p), f2u(q), false, false);
  const unsigned r0 = r[0], r1 = r[1];
  return u2f(r0) + u2f(r1);
}
__device__ __forceinline__ float red4(float a, float b, float c, float d) {
  return dpp_add<0x140>(dpp_add<0x141>(dpp_add<0x4E>(dpp_add<0xB1>(red16(red32(a, b), red32(c, d))))));
}

__global__ void __launch_bounds__(256) k_prep(Params p) {
  __shared__ __attribute__((aligned(16))) char lds[16384];
  STAMP_DECL
  const int tid = TIDX, lane = tid & 63;
  const int w = __builtin_amdgcn_readfirstlane(tid >> 6);
  const int b = BIDX;
  const int c = lane & 15, g = lane >> 4;
  STAMP_AT(0, 0);
  if (b < 128) {
    u32x4 xv[4];
    const char* xr = (const char*)p.x + (size_t)(16 * b + 4 * w) * 1024 + lane * 16;
#pragma unroll
    for (int i = 0; i < 4; ++i) xv[i] = *(const u32x4*)(xr + i * 1024);
    unsigned ones = 0x3f803f80u;
    asm volatile("" : "+v"(ones));
    float S[4];
#pragma unroll
    for (int i = 0; i < 4; ++i) {
      float s = 0.f;
#pragma unroll
      for (int e = 0; e < 4; ++e) {
        const unsigned xe = xv[i][e];
        s = __builtin_amdgcn_fdot2_f32_bf16(__builtin_bit_cast(bf16x2_t, xe), __builtin_bit_cast(bf16x2_t, ones), s, false);
      }
      S[i] = s;
    }
    const float meanv = red4(S[0], S[1], S[2], S[3]) * (1.f / DM);
    const float mbv = rbf(meanv);
    float Q[4];
#pragma unroll
    for (int i = 0; i < 4; ++i) {
      const float mb = rdl(mbv, 16 * (((i & 1) << 1) | (i >> 1)));
      float q = 0.f;
#pragma unroll
      for (int e = 0; e < 4; ++e) {
        const float d0 = bflo(xv[i][e]) - mb, d1 = bfhi(xv[i][e]) - mb;
        q = __builtin_fmaf(d0, d0, __builtin_fmaf(d1, d1, q));
        xv[i][e] = pkbf(d0, d1);
      }
      Q[i] = q;
    }
    const float dm = meanv - mbv;
    const float var = __builtin_fmaxf(red4(Q[0], Q[1], Q[2], Q[3]) * (1.f / DM) - dm * dm, 0.f);
    const float dnv = rbf(__builtin_sqrtf(rbf(rbf(var) + 1e-5f)));
    const float rcv = 1.f / dnv;
    if (c == 0) *(f32x2*)(p.ws + WS_ST + (size_t)(16 * b + 4 * w + ((g & 1) * 2 + (g >> 1))) * 8) = (f32x2){mbv, dnv};
#pragma unroll
    for (int i = 0; i < 4; ++i) {
      const int ln = 16 * (((i & 1) << 1) | (i >> 1));
      const float rc = rdl(rcv, ln), dn = rdl(dnv, ln);
      u32x4 o;
#pragma unroll
      for (int e = 0; e < 4; ++e) {
        const float t0 = bflo(xv[i][e]), t1 = bfhi(xv[i][e]);
        float q0 = t0 * rc, q1 = t1 * rc;
        q0 = __builtin_fmaf(__builtin_fmaf(-q0, dn, t0), rc, q0);
        q1 = __builtin_fmaf(__builtin_fmaf(-q1, dn, t1), rc, q1);
        o[e] = pkbf(q0, q1);
      }
      const int r = 4 * w + i;
      *(u32x4*)(lds + r * 1024 + ((lane ^ r) * 16)) = o;
    }
    wg_sync();
    STAMP_AT(0, 1);
    {
      u32x4 f[4];
#pragma unroll
      for (int k = 0; k < 4; ++k) f[k] = *(const u32x4*)(lds + c * 1024 + (((4 * (4 * w + k) + g) ^ c) * 16));
      const int q4 = c >> 2, pp = c & 3;
      u32x2 vv[2][4];
#pragma unroll
      for (int it = 0; it < 2; ++it)
#pragma unroll
        for (int k = 0; k < 4; ++k) {
          const int r16 = 4 * k + q4, ck = 16 * w + 8 * it + 2 * g + (pp >> 1);
          vv[it][k] = __builtin_bit_cast(u32x2, __builtin_amdgcn_ds_read_tr16_b64_v4i16((LAS s16x4*)(lds + r16 * 1024 + ((ck ^ r16) * 16) + 8 * (pp & 1))));
        }
      SB();
#pragma unroll
      for (int k = 0; k < 4; ++k) st16<2>(p.ws + WS_XNF + ((size_t)b * 16 + 4 * w + k) * 1024 + lane * 16, f[k]);
#pragma unroll
      for (int it = 0; it < 2; ++it) {
        unsigned ov[8];
#pragma unroll
        for (int k = 0; k < 4; ++k)
#pragma unroll
          for (int e = 0; e < 2; ++e) { const unsigned ve = vv[it][k][e]; ov[2 * k + e] = pkh(bflo(ve), bfhi(ve)); }
        char* dst = p.ws + WS_XNT + ((size_t)(128 * w + 64 * it + 16 * g + c) * SL + 16 * b) * 2;
        st16<0>(dst, (u32x4){ov[0], ov[1], ov[2], ov[3]});
        st16<0>(dst + 16, (u32x4){ov[4], ov[5], ov[6], ov[7]});
      }
    }
  } else {
    const int wid = (b - 128) * 4 + w;
    if (wid < 240) {
      const int cb = wid >> 4, ks = wid & 15;
      const int col0 = (cb == 14) ? 1280 : 96 * cb;
      const char* src = (const char*)p.w1 + (size_t)(32 * ks + 8 * g) * (DFF * 2) + (col0 + 6 * c) * 2;
      u32x3 r[8];
#pragma unroll
      for (int j = 0; j < 8; ++j) r[j] = *(const u32x3_a4*)(src + j * (DFF * 2));
      char* dst = p.ws + WS_W1F + (size_t)wid * 6144 + lane * 16;
#pragma unroll
      for (int t = 0; t < 6; ++t) {
        u32x4 F;
#pragma unroll
        for (int q = 0; q < 4; ++q) F[q] = __builtin_amdgcn_perm(r[2 * q + 1][t >> 1], r[2 * q][t >> 1], (t & 1) ? 0x07060302u : 0x05040100u);
        st16<2>(dst + t * 1024, F);
      }
    } else {
      for (int blk = wid - 240; blk < 344; blk += 272) {
        const int ct = blk / 43, ks = blk % 43;
        const unsigned short* wsrc = p.w2 + (size_t)(32 * ks + 8 * g) * DM + 64 * ct + 4 * c;
        u32x2 r[8];
#pragma unroll
        for (int j = 0; j < 8; ++j) r[j] = *(const u32x2*)(wsrc + (size_t)j * DM);
        char* dst2 = p.ws + WS_W2F + (size_t)blk * 4096 + lane * 16;
#pragma unroll
        for (int t = 0; t < 4; ++t) {
          u32x4 F;
#pragma unroll
          for (int q = 0; q < 4; ++q) F[q] = __builtin_amdgcn_perm(r[2 * q + 1][t >> 1], r[2 * q][t >> 1], (t & 1) ? 0x07060302u : 0x05040100u);
          st16<2>(dst2 + t * 1024, F);
        }
      }
    }
  }
  {
    char* ydst = p.ws + WS_YT + (size_t)b * 8192 + tid * 16;
    const u32x4 sent = {0xffffffffu, 0xffffffffu, 0xffffffffu, 0xffffffffu};
    st16<2>(ydst, sent);
    st16<2>(ydst + 4096, sent);
    if (tid == 0) ((unsigned*)(p.ws + WS_FL))[32 * b] = 0u;
  }
  STAMP_AT(0, 2);
  STAMP_FLUSH(0);
}

constexpr int PB = 4;
constexpr int KT_P = 528;
constexpr int KCS = KRL * 2;
constexpr int CV_CH = 8 * KCS + 4608;
constexpr int M_A = 0;
constexpr int M_C = 65536;
constexpr int M_CR = M_C + 2 * CV_CH;
constexpr int M_Y = M_C;
constexpr int M_T1 = M_C + 9216;
constexpr int M_TH = M_CR + 16384;
constexpr int M_CNT = M_TH + 2704;
constexpr int M_SIZE = M_CNT + 128;

__global__ void __launch_bounds__(512, 2) k_main(Params p) {
  __shared__ __attribute__((aligned(16))) char lds[M_SIZE];
  STAMP_DECL
  const int tid = TIDX, lane = tid & 63;
  const int w = __builtin_amdgcn_readfirstlane(tid >> 6);
  const int b = BIDX;
  LAS unsigned* cnt = (LAS unsigned*)(LAS char*)(lds + M_CNT);
  if (tid < 32) cnt[tid] = 0u;
  wg_sync();
  STAMP_AT(1, 0);
  const int xcd = b & 7, idx = b >> 3;
  const int rt = xcd * 4 + (idx >> 3), cg = idx & 7;
  const int row0 = rt * 64, col2 = cg * 64;
  const int c = lane & 15, g = lane >> 4;
  GAS unsigned* flags = (GAS unsigned*)(p.ws + WS_FL);
  if (w >= 4) {
    const int gi = w - 4, ch = gi & 1, kh = gi >> 1;
    {
      const int cb = (cg == 7) ? 14 : 2 * cg + ch;
      const int col0 = (cg == 7) ? 1280 : 96 * cb;
      const bool gw = (cg < 7) || (ch == 0);
      if (gw) {
        const char* bsrc = p.ws + WS_W1F + (size_t)(cb * 16 + 8 * kh) * 6144 + lane * 16;
        u32x4 bq[PB][6];
        lds_wait(cnt + 3, 4);
#pragma unroll
        for (int s = 0; s < PB; ++s)
#pragma unroll
          for (int t = 0; t < 6; ++t) bq[s][t] = *(const u32x4*)(bsrc + s * 6144 + t * 1024);
        SB();
        f32x4 acc[4][6];
#pragma unroll
        for (int i = 0; i < 4; ++i)
#pragma unroll
          for (int t = 0; t < 6; ++t) acc[i][t] = (f32x4){0.f, 0.f, 0.f, 0.f};
        lds_wait(cnt + 0, 4);
        STAMP_AT(1, 1);
        const char* ap = lds + M_A + (8 * kh) * 1024 + lane * 16;
        s16x8 af[2][4];
#pragma unroll
        for (int mt = 0; mt < 4; ++mt) af[0][mt] = *(const s16x8*)(ap + mt * 16384);
#pragma unroll
        for (int s = 0; s < 8; ++s) {
          if (s + 1 < 8) {
#pragma unroll
            for (int mt = 0; mt < 4; ++mt) af[(s + 1) & 1][mt] = *(const s16x8*)(ap + mt * 16384 + (s + 1) * 1024);
          }
          SB();
#pragma unroll
          for (int t = 0; t < 6; ++t)
#pragma unroll
            for (int mt = 0; mt < 4; ++mt)
              acc[mt][t] = __builtin_amdgcn_mfma_f32_16x16x32_bf16(__builtin_bit_cast(s16x8, bq[s % PB][t]), af[s & 1][mt], acc[mt][t], 0, 0, 0);
          SB();
          if (s + PB < 8) {
#pragma unroll
            for (int t = 0; t < 6; ++t) bq[s % PB][t] = *(const u32x4*)(bsrc + (s + PB) * 6144 + t * 1024);
          }
          SB();
        }
        STAMP_AT(1, 2);
        lds_signal(cnt + 4, lane);
        lds_wait(cnt + 4, (cg == 7) ? 2 : 4);
        f32x4* rx = (f32x4*)(lds + M_A + gi * 12288);
        const f32x4* ro = (const f32x4*)(lds + M_A + (gi ^ 2) * 12288);
        auto fin = [&](auto KH) {
          constexpr int kk = decltype(KH)::v;
#pragma unroll
          for (int m = 0; m < 2; ++m)
#pragma unroll
            for (int t = 0; t < 6; ++t) rx[(m * 6 + t) * 64 + lane] = acc[2 * (1 - kk) + m][t];
          lds_signal(cnt + 5 + ch, lane);
          lds_wait(cnt + 5 + ch, 2);
          f32x4 oo[2][6];
#pragma unroll
          for (int m = 0; m < 2; ++m)
#pragma unroll
            for (int t = 0; t < 6; ++t) oo[m][t] = ro[(m * 6 + t) * 64 + lane];
          SB();
#pragma unroll
          for (int m = 0; m < 2; ++m) {
            const int mt = 2 * kk + m;
            float hv[24];
#pragma unroll
            for (int t = 0; t < 6; ++t)
#pragma unroll
              for (int r = 0; r < 4; ++r) hv[6 * r + t] = acc[2 * kk + m][t][r] + oo[m][t][r];
#pragma unroll
            for (int ck = 0; ck < 3; ++ck) {
              const int F0 = col0 + 24 * g + 8 * ck;
              u32x4 o;
#pragma unroll
              for (int e = 0; e < 4; ++e) {
                const s16x2 pv = __builtin_bit_cast(s16x2, pkbf(hv[8 * ck + 2 * e], hv[8 * ck + 2 * e + 1]));
                o[e] = __builtin_bit_cast(unsigned, __builtin_elementwise_max(pv, (s16x2){0, 0}));
              }
              st16<2>(p.ws + WS_H + ((size_t)(rt * 4 + mt) * 43 + (F0 >> 5)) * 1024 + (c + 16 * ((F0 >> 3) & 3)) * 16, o);
            }
          }
        };
        if (kh == 0) fin(IC<0>{}); else fin(IC<1>{});
        WAITV(0);
      }
      if (lds_arrive(cnt + 7, lane) == 3u && lane < 8)
        __hip_atomic_fetch_add(flags + 32 * (8 * rt + lane), 1u, __ATOMIC_RELAXED, __HIP_MEMORY_SCOPE_AGENT);
      STAMP_AT(1, 3);
    }
    {
      const int s0 = 11 * gi;
      const int last = (gi == 3) ? 9 : 10;
      const char* wb = p.ws + WS_W2F + ((size_t)cg * 43 + s0) * 4096 + lane * 16;
      constexpr int PA = 4, PW = 6;
      u32x4 ar[PA][4], br[PW][4];
#pragma unroll
      for (int s = 0; s < PW; ++s)
#pragma unroll
        for (int j = 0; j < 4; ++j) br[s][j] = *(const u32x4*)(wb + s * 4096 + j * 1024);
      SB();
      if (gi == 0) {
        unsigned spins = 0;
        while (true) {
          unsigned f = 8u;
          if (lane == 0) f = __hip_atomic_load(flags + 32 * (8 * rt + cg), __ATOMIC_RELAXED, __HIP_MEMORY_SCOPE_AGENT);
          if (__builtin_amdgcn_readfirstlane(f) >= 8u) break;
          if (++spins > (1u << 20)) break;
          __builtin_amdgcn_s_sleep(2);
        }
        asm volatile("" ::: "memory");
        lds_signal(cnt + 12, lane);
      } else {
        lds_wait(cnt + 12, 1);
      }
      STAMP_AT(1, 4);
      const auto hrs = __builtin_amdgcn_make_buffer_rsrc((void*)(p.ws + WS_H), 0, 128 * 43 * 1024, 0x00020000);
      const int hoff = ((rt * 4) * 43 + s0) * 1024 + lane * 16;
      auto issueA = [&](int s) {
        const int ks = (s == 10) ? last : s;
#pragma unroll
        for (int mt = 0; mt < 4; ++mt) ar[s % PA][mt] = __builtin_amdgcn_raw_buffer_load_b128(hrs, hoff + mt * 43 * 1024, ks * 1024, 16);
      };
#pragma unroll
      for (int s = 0; s < PA; ++s) issueA(s);
      SB();
      f32x4 acc[4][4];
#pragma unroll
      for (int i = 0; i < 4; ++i)
#pragma unroll
        for (int t = 0; t < 4; ++t) acc[i][t] = (f32x4){0.f, 0.f, 0.f, 0.f};
#pragma unroll
      for (int s = 0; s < 11; ++s) {
        if (s == 10) {
          const unsigned msk = (gi == 3) ? 0u : 0xffffffffu;
#pragma unroll
          for (int j = 0; j < 4; ++j) br[s % PW][j] &= msk;
        }
#pragma unroll
        for (int mt = 0; mt < 4; ++mt)
#pragma unroll
          for (int t = 0; t < 4; ++t)
            acc[mt][t] = __builtin_amdgcn_mfma_f32_16x16x32_bf16(__builtin_bit_cast(s16x8, ar[s % PA][mt]),
                                                                 __builtin_bit_cast(s16x8, br[s % PW][t]), acc[mt][t], 0, 0, 0);
        SB();
        if (s + PA < 11) issueA(s + PA);
        if (s + PW < 11) {
          const int ks = (s + PW == 10) ? last : s + PW;
#pragma unroll
          for (int j = 0; j < 4; ++j) br[s % PW][j] = *(const u32x4*)(wb + ks * 4096 + j * 1024);
        }
        if (s == 6 && lane == 0) __hip_atomic_fetch_add(cnt + 13, 1u, __ATOMIC_RELAXED, __HIP_MEMORY_SCOPE_WORKGROUP);
        SB();
      }
      STAMP_AT(1, 5);
      f32x4* red = (f32x4*)(lds + M_A);
      f32x4 f[4];
#pragma unroll
      for (int mt = 0; mt < 4; ++mt) {
        if (mt != gi) {
          const int slot = (gi > mt) ? (gi - 1) : gi;
#pragma unroll
          for (int t = 0; t < 4; ++t) red[((mt * 3 + slot) * 4 + t) * 64 + lane] = acc[mt][t];
        } else {
#pragma unroll
          for (int t = 0; t < 4; ++t) f[t] = acc[mt][t];
        }
      }
      lds_signal(cnt + 14, lane);
      lds_wait(cnt + 14, 4);
      f32x4 pr[4][3];
#pragma unroll
      for (int t = 0; t < 4; ++t)
#pragma unroll
        for (int sl = 0; sl < 3; ++sl) pr[t][sl] = red[((gi * 3 + sl) * 4 + t) * 64 + lane];
      SB();
#pragma unroll
      for (int t = 0; t < 4; ++t) {
        f[t] += (pr[t][0] + pr[t][1]) + pr[t][2];
#pragma unroll
        for (int r = 0; r < 4; ++r) f[t][r] = rbf(f[t][r]);
        asm volatile("" : "+v"(f[t]));
      }
      STAMP_AT(1, 6);
      lds_wait(cnt + 20 + gi, 1);
      STAMP_AT(1, 7);
      const int erow = row0 + 16 * gi + 4 * g;
#pragma unroll
      for (int r = 0; r < 4; ++r) {
        const u32x2 tv = *(const u32x2*)(lds + M_T1 + ((gi * 4 + r) * 64 + lane) * 8);
        const unsigned t0 = tv[0], t1 = tv[1];
        u32x2 ov;
        ov[0] = pkbf(bflo(t0) + f[0][r], bfhi(t0) + f[1][r]);
        ov[1] = pkbf(bflo(t1) + f[2][r], bfhi(t1) + f[3][r]);
        *(u32x2*)(p.out + (size_t)(erow + r) * DM + col2 + 4 * c) = ov;
      }
      STAMP_AT(1, 8);
    }
  } else {
    const int kw = w, chl = kw >> 1, nh = kw & 1;
    const int d = 2 * b + chl;
    char* kl = lds + M_C + chl * CV_CH;
    char* ul = kl + 8 * KCS;
    char* tch = kl;
    unsigned short* th = (unsigned short*)(lds + M_TH + chl * 1344);
    const int t2 = tid & 127;
    const int erow = row0 + 16 * w + 4 * g;
    float a2, fr, wsc_inv;
    f32x2 W;
    bool grow;
    u32x2 xv[4];
    f32x4 st01, st23;
    u32x2 dk;
    {
      u32x4 xq[16];
      {
        const char* xr = p.ws + WS_XNF + (size_t)(rt * 4 + kw) * 16384 + lane * 16;
#pragma unroll
        for (int i = 0; i < 16; ++i) xq[i] = *(const u32x4*)(xr + i * 1024);
      }
      SB();
      const size_t o = (size_t)d * NS + 64 * nh + lane, o2 = o ^ 64;
      const float lg = bf2f(p.logdt[d]);
      const float are = bf2f(p.Are[o]), aim = bf2f(p.Aim[o]);
      const float bre = bf2f(p.Bre[o]), bim = bf2f(p.Bim[o]);
      const float cr = bf2f(p.Cre[o]), ci = bf2f(p.Cim[o]);
      const float are2 = bf2f(p.Are[o2]);
      const float bre2 = bf2f(p.Bre[o2]), bim2 = bf2f(p.Bim[o2]);
      const float cr2 = bf2f(p.Cre[o2]), ci2 = bf2f(p.Cim[o2]);
      const u32x4* usrc = (const u32x4*)(p.ws + WS_XNT + (size_t)d * SL * 2);
      const u32x4 uv0 = usrc[t2], uv1 = usrc[t2 + 128];
#pragma unroll
      for (int r = 0; r < 4; ++r) xv[r] = *(const u32x2*)(p.x + (size_t)(erow + r) * DM + col2 + 4 * c);
      st01 = *(const f32x4*)(p.ws + WS_ST + (size_t)erow * 8);
      st23 = *(const f32x4*)(p.ws + WS_ST + (size_t)erow * 8 + 16);
      dk = *(const u32x2*)(p.Dsk + col2 + 4 * c);
      SB();
      if (lane == 0) __hip_atomic_fetch_add(cnt + 3, 1u, __ATOMIC_RELAXED, __HIP_MEMORY_SCOPE_WORKGROUP);
      SB();
#pragma unroll
      for (int i = 0; i < 16; ++i) *(u32x4*)(lds + M_A + (kw * 16 + i) * 1024 + lane * 16) = xq[i];
      lds_signal(cnt + 0, lane);
      if (t2 < 32) *(u32x4*)(ul + t2 * 16) = (u32x4){0u, 0u, 0u, 0u};
      *(u32x4*)(ul + 512 + t2 * 16) = uv0;
      *(u32x4*)(ul + 512 + (t2 + 128) * 16) = uv1;
      const float dt = rbf(exp_acc(lg));
      const float br = dt * bre, bi = dt * bim;
      a2 = (dt * are) * 1.4426950408889634f;
      fr = (dt * aim) * 0.15915494309189535f;
      W = (f32x2){cr * br - ci * bi, -(cr * bi + ci * br)};
      const float m1 = (__builtin_fabsf(cr) + __builtin_fabsf(ci)) * (__builtin_fabsf(bre) + __builtin_fabsf(bim));
      const float m2 = (__builtin_fabsf(cr2) + __builtin_fabsf(ci2)) * (__builtin_fabsf(bre2) + __builtin_fabsf(bim2));
      const float mw = wave_max_nonneg(__builtin_fmaxf(m1, m2)) * dt;
      const int eb = (int)(f2u(mw) >> 23);
      int ex = 136 - eb;
      ex = ex > 120 ? 120 : (ex < -120 ? -120 : ex);
      const float wsc = u2f((unsigned)(127 + ex) << 23);
      wsc_inv = u2f((unsigned)(127 - ex) << 23);
      W = (f32x2){W[0] * wsc, W[1] * wsc};
      const float gmax = __builtin_fmaxf(dt * are, dt * are2);
      grow = __builtin_amdgcn_ballot_w64(!(gmax * 2048.f < 2.f) || eb >= 250) != 0ull;
    }
    STAMP_AT(1, 1);
    auto cexpq = [&](float q) -> f32x2 {
      const float e = __builtin_amdgcn_exp2f(a2 * q);
      const float ph = __builtin_amdgcn_fractf(fr * q);
      return (f32x2){__builtin_amdgcn_cosf(ph) * e, __builtin_amdgcn_sinf(ph) * e};
    };
    auto cmul = [](f32x2 a, f32x2 bb) -> f32x2 {
      return (f32x2){__builtin_fmaf(a[0], bb[0], -(a[1] * bb[1])), __builtin_fmaf(a[0], bb[1], a[1] * bb[0])};
    };
    char* tw = tch + (64 * nh + lane) * 4;
    auto put = [&](int row, f32x2 v) {
      float v0 = v[0], v1 = v[1];
      asm("" : "+v"(v0), "+v"(v1));
      *(unsigned*)(tw + row * KT_P) = pkh(v0, v1);
    };
    if (!grow) {
      const f32x2 E1 = cexpq(1.f), E256 = cexpq(256.f), E8 = cexpq(8.f);
      f32x2 y0 = (f32x2){1.f, 0.f}, y1 = cexpq(64.f), y2 = cexpq(128.f), y3 = cexpq(192.f);
      const f32x2 C1 = {E1[0], -E1[1]}, CL = {E256[0], -E256[1]};
      const f32x2 C2 = cmul(C1, C1), C4 = cmul(C2, C2);
      f32x2 xs[21];
      xs[0] = W;
      xs[8] = cmul(W, CL);
      xs[1] = cmul(xs[0], C1);  xs[12] = cmul(xs[8], CL);  xs[9] = cmul(xs[8], C2);
      xs[2] = cmul(xs[1], C1);  xs[14] = cmul(xs[12], CL); xs[10] = cmul(xs[9], C2); xs[13] = cmul(xs[12], C4);
      xs[3] = cmul(xs[2], C1);  xs[16] = cmul(xs[14], CL); xs[11] = cmul(xs[10], C2); xs[15] = cmul(xs[14], C4);
      xs[4] = cmul(xs[0], C4);  xs[17] = cmul(xs[16], CL);
      xs[5] = cmul(xs[1], C4);  xs[18] = cmul(xs[17], CL);
      xs[6] = cmul(xs[2], C4);  xs[19] = cmul(xs[18], CL);
      xs[7] = cmul(xs[3], C4);  xs[20] = cmul(xs[19], CL);
#pragma unroll
      for (int i = 0; i < 21; ++i) put(i, xs[i]);
      put(21, y0); put(29, y1); put(37, y2); put(45, y3);
#pragma unroll
      for (int i = 1; i < 8; ++i) {
        y0 = cmul(y0, E8); y1 = cmul(y1, E8); y2 = cmul(y2, E8); y3 = cmul(y3, E8);
        put(21 + i, y0); put(29 + i, y1); put(37 + i, y2); put(45 + i, y3);
      }
    } else {
      *(f32x4*)(tch + (64 * nh + lane) * 16) = (f32x4){a2, fr, W[0], -W[1]};
    }
    lds_signal(cnt + 1 + chl, lane);
    float xf[4][4], xd[4][4];
    {
      const float dkf[4] = {bflo(dk[0]), bfhi(dk[0]), bflo(dk[1]), bfhi(dk[1])};
#pragma unroll
      for (int r = 0; r < 4; ++r) {
        xf[r][0] = bflo(xv[r][0]); xf[r][1] = bfhi(xv[r][0]); xf[r][2] = bflo(xv[r][1]); xf[r][3] = bfhi(xv[r][1]);
        const float mb = (r == 0) ? st01[0] : (r == 1) ? st01[2] : (r == 2) ? st23[0] : st23[2];
        const float den = (r == 0) ? st01[1] : (r == 1) ? st01[3] : (r == 2) ? st23[1] : st23[3];
        const float rc = 1.f / den;
#pragma unroll
        for (int t = 0; t < 4; ++t) {
          const float a0 = rbf(xf[r][t] - mb);
          float q = a0 * rc;
          q = __builtin_fmaf(__builtin_fmaf(-q, den, a0), rc, q);
          xd[r][t] = rbf(rbf(q) * dkf[t]);
          asm volatile("" : "+v"(xd[r][t]), "+v"(xf[r][t]));
        }
      }
    }
    STAMP_AT(1, 2);
    lds_wait(cnt + 1 + chl, 2);
    float isc;
    {
      f32x4 acc[2];
      acc[0] = (f32x4){0.f, 0.f, 0.f, 0.f}; acc[1] = acc[0];
      const int j = 16 * nh + c;
      if (!grow) {
        const char* fa = tch + c * KT_P + 16 * g;
        const char* fb = tch + (21 + j) * KT_P + 16 * g;
        f16x8 fA0[8], fA1[8], fB[8];
#pragma unroll
        for (int s = 0; s < 8; ++s) {
          fB[s] = *(const f16x8*)(fb + 64 * s);
          fA0[s] = *(const f16x8*)(fa + 64 * s);
          fA1[s] = *(const f16x8*)(fa + 16 * KT_P + 64 * s);
        }
        SB();
#pragma unroll
        for (int s = 0; s < 8; ++s) {
          acc[0] = __builtin_amdgcn_mfma_f32_16x16x32_f16(fA0[s], fB[s], acc[0], 0, 0, 0);
          acc[1] = __builtin_amdgcn_mfma_f32_16x16x32_f16(fA1[s], fB[s], acc[1], 0, 0, 0);
        }
      } else {
        for (int e = 0; e < 8; ++e) {
          const int row = 16 * (e >> 2) + 4 * g + (e & 3);
          const int qx = row < 8 ? row : row < 12 ? 256 + 2 * (row - 8) : row < 16 ? 512 + 256 * ((row - 12) >> 1) + 4 * (row & 1)
                                                                     : row < 21 ? 1024 + 256 * (row - 16) : 0;
          const float q = (float)(qx + 8 * j);
          float sacc = 0.f;
          for (int n = 0; n < 128; ++n) {
            const f32x4 pr = *(const f32x4*)(tch + n * 16);
            const float ph = __builtin_amdgcn_fractf(pr[1] * q);
            sacc += __builtin_amdgcn_exp2f(pr[0] * q) * (pr[2] * __builtin_amdgcn_cosf(ph) - pr[3] * __builtin_amdgcn_sinf(ph));
          }
          if (e < 4) acc[0][e & 3] = sacc; else acc[1][e & 3] = sacc;
        }
      }
      const bool v1 = (g == 0), v2 = (g == 1 && j == 0);
      float mx = __builtin_fmaxf(__builtin_fmaxf(__builtin_fabsf(acc[0][0]), __builtin_fabsf(acc[0][1])),
                                 __builtin_fmaxf(__builtin_fabsf(acc[0][2]), __builtin_fabsf(acc[0][3])));
      const float m1 = __builtin_fmaxf(__builtin_fmaxf(__builtin_fabsf(acc[1][1]), __builtin_fabsf(acc[1][2])), __builtin_fabsf(acc[1][3]));
      mx = __builtin_fmaxf(mx, (v1 || v2) ? __builtin_fabsf(acc[1][0]) : 0.f);
      mx = __builtin_fmaxf(mx, v1 ? m1 : 0.f);
      mx = wave_max_nonneg(mx);
      float* mxl = (float*)(lds + M_TH + 2688);
      if (lane == 0) mxl[kw] = mx;
      lds_signal(cnt + 8 + chl, lane);
      lds_wait(cnt + 8 + chl, 2);
      mx = __builtin_fmaxf(mx, mxl[kw ^ 1]);
      const int eb = (int)(f2u(mx) >> 23);
      int ex = 140 - eb;
      ex = (eb == 0 || eb == 255) ? 0 : (ex > 120 ? 120 : ex);
      const float sc = u2f((unsigned)(127 + ex) << 23);
      isc = u2f((unsigned)(127 - ex) << 23);
      const int b0 = (g < 2 ? 4 * g + 8 * j : g == 2 ? 256 + 4 * j : 384 + 2 * j);
      *(unsigned*)(th + b0) = pkh(acc[0][0] * sc, acc[0][1] * sc);
      *(unsigned*)(th + b0 + (g == 3 ? 64 : 2)) = pkh(acc[0][2] * sc, acc[0][3] * sc);
      const unsigned p01 = pkh(acc[1][0] * sc, acc[1][1] * sc), p23 = pkh(acc[1][2] * sc, acc[1][3] * sc);
      if (v1) {
        th[512 + j] = (unsigned short)p01; th[544 + j] = (unsigned short)(p01 >> 16);
        th[576 + j] = (unsigned short)p23; th[608 + j] = (unsigned short)(p23 >> 16);
      }
      if (v2) th[640] = (unsigned short)p01;
      lds_signal(cnt + 10 + chl, lane);
    }
    STAMP_AT(1, 3);
    {
      lds_wait(cnt + 10 + chl, 2);
      const unsigned short* tu = th;
      if (nh == 0) {
#pragma unroll
        for (int k = 0; k < 2; ++k) {
          const int ck = lane + 64 * k, M = 256 - ck;
          const unsigned a = tu[384 + M], bb = tu[383 + M];
          const unsigned aa = a | (a << 16), b2 = bb | (bb << 16);
          const unsigned mid = ((M & 1) ? bb : a) | (bb << 16);
          *(u32x4*)(kl + 16 * ck) = (u32x4){aa, aa, mid, b2};
        }
        if (lane < 4) *(u32x4*)(kl + 16 * (256 + lane)) = (u32x4){lane == 0 ? (unsigned)tu[0] : 0u, 0u, 0u, 0u};
      } else {
        {
          const int ck = 128 + lane, M = 256 - ck;
          const unsigned a = tu[256 + 2 * M], bb = tu[255 + 2 * M], cc = tu[254 + 2 * M];
          *(u32x4*)(kl + 16 * ck) = (u32x4){a | (a << 16), a | (bb << 16), bb | (bb << 16), cc | (cc << 16)};
        }
        {
          const int ck = 192 + lane, M = 256 - ck;
          const bool s1 = lane < 32;
          const int base = s1 ? 128 + 4 * M : 8 * M;
          const int o1[8] = {0, 0, 1, 2, 2, 2, 3, 4};
          unsigned hv[8];
#pragma unroll
          for (int e = 0; e < 8; ++e) hv[e] = tu[base - (s1 ? o1[e] : e)];
          *(u32x4*)(kl + 16 * ck) = (u32x4){hv[0] | (hv[1] << 16), hv[2] | (hv[3] << 16), hv[4] | (hv[5] << 16), hv[6] | (hv[7] << 16)};
        }
      }
      lds_signal(cnt + 16 + chl, lane);
      lds_wait(cnt + 16 + chl, 2);
      u32x4 kc[3], kp[3];
#pragma unroll
      for (int k = 0; k < 3; ++k) {
        int ci = t2 + 128 * k;
        ci = ci < 260 ? ci : 259;
        kc[k] = *(const u32x4*)(kl + 16 * ci);
        kp[k] = *(const u32x4*)(kl + 16 * (ci > 0 ? ci - 1 : 0));
      }
      SB();
#pragma unroll
      for (int k = 0; k < 3; ++k) {
        const int ci = t2 + 128 * k;
        if (ci < 260) {
          unsigned wd[8] = {kp[k][0], kp[k][1], kp[k][2], kp[k][3], kc[k][0], kc[k][1], kc[k][2], kc[k][3]};
          unsigned od[7];
#pragma unroll
          for (int jj = 0; jj < 7; ++jj) od[jj] = __builtin_amdgcn_alignbit(wd[jj + 1], wd[jj], 16);
          char* dst = kl + ci * 16;
#pragma unroll
          for (int sg = 1; sg < 8; ++sg) {
            u32x4 o;
            if ((sg & 1) == 0) { const int q0 = (8 - sg) >> 1; o = (u32x4){wd[q0], wd[q0 + 1], wd[q0 + 2], wd[q0 + 3]}; }
            else { const int q0 = (7 - sg) >> 1; o = (u32x4){od[q0], od[q0 + 1], od[q0 + 2], od[q0 + 3]}; }
            *(u32x4*)(dst + sg * KCS) = o;
          }
        }
      }
      lds_signal(cnt + 18 + chl, lane);
      lds_wait(cnt + 18 + chl, 2);
    }
    STAMP_AT(1, 4);
    {
      const int a = c, hf = nh;
      f32x4 acc[8];
#pragma unroll
      for (int r = 0; r < 8; ++r) acc[r] = (f32x4){0.f, 0.f, 0.f, 0.f};
      const char* ab = kl + (a & 7) * KCS + 2 * (2032 + 8 * g - 8 * (a >> 3)) - 64 * 4 * hf;
      const char* bb = ul + 2 * (240 + 16 * a + 8 * g) - 64 * 4 * hf;
      f16x8 bf[2][8], af[2][8];
#pragma unroll
      for (int t = 0; t < 8; ++t) { bf[0][t] = *(const f16x8*)(bb + 512 * t); af[0][t] = *(const f16x8*)(ab - 512 * t); }
#pragma unroll
      for (int i = 0; i < 4; ++i) {
        if (i + 1 < 4) {
#pragma unroll
          for (int t = 0; t < 8; ++t) {
            bf[(i + 1) & 1][t] = *(const f16x8*)(bb - 64 * (i + 1) + 512 * t);
            af[(i + 1) & 1][t] = *(const f16x8*)(ab - 64 * (i + 1) - 512 * t);
          }
        }
        SB();
#pragma unroll
        for (int v = 0; v < 8; ++v)
#pragma unroll
          for (int t = v; t < 8; ++t)
            acc[t] = __builtin_amdgcn_mfma_f32_16x16x32_f16(af[i & 1][v], bf[i & 1][t - v], acc[t], 0, 0, 0);
        SB();
      }
      STAMP_AT(1, 5);
      f32x4* red = (f32x4*)(lds + M_CR + chl * 8192);
      f32x4 mine[4];
      if (hf) {
#pragma unroll
        for (int r = 0; r < 4; ++r) { red[(4 + r) * 64 + lane] = acc[r]; mine[r] = acc[4 + r]; }
      } else {
#pragma unroll
        for (int r = 0; r < 4; ++r) { red[r * 64 + lane] = acc[4 + r]; mine[r] = acc[r]; }
      }
      lds_signal(cnt + 24, lane);
      lds_signal(cnt + 25 + chl, lane);
      lds_wait(cnt + 25 + chl, 2);
      unsigned pk[4][2];
#pragma unroll
      for (int r = 0; r < 4; ++r) {
        const f32x4 v = ((mine[r] + red[(4 * (1 - hf) + r) * 64 + lane]) * isc) * wsc_inv;
        pk[r][0] = pkbf(v[0], v[1]);
        pk[r][1] = pkbf(v[2], v[3]);
      }
      const auto rsrc = __builtin_amdgcn_make_buffer_rsrc((void*)(p.ws + WS_YT), 0, DM * SL * 2, 0x00020000);
#pragma unroll
      for (int r = 0; r < 2; ++r) {
        auto s0 = __builtin_amdgcn_permlane16_swap(pk[r][0], pk[r + 2][0], false, false);
        auto s1 = __builtin_amdgcn_permlane16_swap(pk[r][1], pk[r + 2][1], false, false);
        const unsigned a0 = s0[0], b0 = s0[1], a1 = s1[0], b1 = s1[1];
        const u32x4 o = {a0, a1, b0, b1};
        const int tile = r + 2 * (g & 1);
        const int off = (d * SL + 1024 * hf + 256 * tile + 16 * a + 8 * (g >> 1)) * 2;
        __builtin_amdgcn_raw_buffer_store_b128(o, rsrc, off, 0, 16);
      }
      STAMP_AT(1, 6);
      const int chy = 16 * w + (lane >> 2), k0 = (lane & 3) * 2;
      const int off = ((col2 + chy) * SL + row0) * 2 + k0 * 16;
      u32x4 v0, v1;
      unsigned spins = 0;
      lds_wait2(cnt + 13, 4, cnt + 24, 4);
      while (true) {
        v0 = __builtin_amdgcn_raw_buffer_load_b128(rsrc, off, 0, 16);
        v1 = __builtin_amdgcn_raw_buffer_load_b128(rsrc, off + 16, 0, 16);
        bool bad = false;
#pragma unroll
        for (int e = 0; e < 4; ++e) {
          const unsigned a0 = v0[e], a1 = v1[e];
          bad |= ((a0 & 0xffffu) == 0xffffu) | ((a0 >> 16) == 0xffffu) | ((a1 & 0xffffu) == 0xffffu) | ((a1 >> 16) == 0xffffu);
        }
        if (__builtin_amdgcn_ballot_w64(bad) == 0ull) break;
        if (++spins > (1u << 18)) break;
        __builtin_amdgcn_s_sleep(4);
      }
      STAMP_AT(1, 7);
      char* yd = lds + M_Y + chy * 136 + k0 * 16;
      *(u32x2*)yd = (u32x2){v0[0], v0[1]}; *(u32x2*)(yd + 8) = (u32x2){v0[2], v0[3]};
      *(u32x2*)(yd + 16) = (u32x2){v1[0], v1[1]}; *(u32x2*)(yd + 24) = (u32x2){v1[2], v1[3]};
      lds_signal(cnt + 27, lane);
      lds_wait(cnt + 27, 4);
      u32x2 yv[4];
#pragma unroll
      for (int t = 0; t < 4; ++t) yv[t] = *(const u32x2*)(lds + M_Y + (4 * c + t) * 136 + (16 * w + 4 * g) * 2);
#pragma unroll
      for (int r = 0; r < 4; ++r) {
        float o[4];
#pragma unroll
        for (int t = 0; t < 4; ++t) {
          const unsigned yw = yv[t][r >> 1];
          const float y = (r & 1) ? bfhi(yw) : bflo(yw);
          o[t] = xf[r][t] + rbf(y + xd[r][t]);
        }
        *(u32x2*)(lds + M_T1 + ((w * 4 + r) * 64 + lane) * 8) = (u32x2){pkbf(o[0], o[1]), pkbf(o[2], o[3])};
      }
      lds_signal(cnt + 20 + w, lane);
      STAMP_AT(1, 8);
    }
  }
  STAMP_FLUSH(1);
}

#if defined(__HIP_DEVICE_COMPILE__)
#pragma clang attribute pop
#endif

extern "C" void kernel_launch(void* const* d_in, const int* in_sizes, int n_in,
                              void* d_out, int out_size, void* d_ws, size_t ws_size,
                              hipStream_t stream) {
  Params p;
  p.x = (const unsigned short*)d_in[0];
  p.Are = (const unsigned short*)d_in[1];
  p.Aim = (const unsigned short*)d_in[2];
  p.Bre = (const unsigned short*)d_in[3];
  p.Bim = (const unsigned short*)d_in[4];
  p.Cre = (const unsigned short*)d_in[5];
  p.Cim = (const unsigned short*)d_in[6];
  p.logdt = (const unsigned short*)d_in[7];
  p.Dsk = (const unsigned short*)d_in[8];
  p.w1 = (const unsigned short*)d_in[10];
  p.w2 = (const unsigned short*)d_in[11];
  p.out = (unsigned short*)d_out;
  p.ws = (char*)d_ws;
  k_prep<<<256, 256, 0, stream>>>(p);
  k_main<<<256, 512, 0, stream>>>(p);
}
```

```cpp
#include <hip/hip_runtime.h>
#include <hip/hip_bf16.h>
#include <stdint.h>

typedef __attribute__((ext_vector_type(8))) short s16x8;
typedef __attribute__((ext_vector_type(4))) short s16x4;
typedef __attribute__((ext_vector_type(8))) _Float16 f16x8;
typedef __attribute__((ext_vector_type(4))) float f32x4;
typedef __attribute__((ext_vector_type(2))) float f32x2;
typedef __attribute__((ext_vector_type(16))) float f32x16;
typedef __attribute__((ext_vector_type(4))) unsigned u32x4;
typedef __attribute__((ext_vector_type(2))) unsigned u32x2;
typedef __attribute__((ext_vector_type(2))) __bf16 bf16x2_t;
typedef __attribute__((ext_vector_type(2))) _Float16 f16x2_t;
#define LAS __attribute__((address_space(3)))

constexpr int SL = 2048;
constexpr int DM = 512;
constexpr int NS = 128;
constexpr int DFF = 1376;
constexpr int KRL = 2080;

constexpr size_t WS_H = 0;
constexpr size_t WS_XNT = WS_H + (size_t)128 * 43 * 1024;
constexpr size_t WS_KR = WS_XNT + (size_t)DM * SL * 2;
constexpr size_t WS_YT = WS_KR + (size_t)DM * KRL * 2;
constexpr size_t WS_ST = WS_YT + (size_t)DM * SL * 2;
constexpr size_t WS_KS = WS_ST + (size_t)SL * 8;
constexpr size_t WS_FL = WS_KS + 4096;
constexpr size_t WS_W2F = WS_FL + 32768;

#if defined(__HIP_DEVICE_COMPILE__)
#pragma clang attribute push(__attribute__((target("no-packed-fp32-ops"))), apply_to = function)
#endif
__device__ __forceinline__ float u2f(unsigned u) { return __builtin_bit_cast(float, u); }
__device__ __forceinline__ unsigned f2u(float f) { return __builtin_bit_cast(unsigned, f); }
__device__ __forceinline__ float shx(float v, int lane, int m) {
  return u2f((unsigned)__builtin_amdgcn_ds_bpermute((lane ^ m) << 2, (int)f2u(v)));
}
__device__ __forceinline__ void wg_sync() {
  __builtin_amdgcn_fence(__ATOMIC_RELEASE, "workgroup");
  __builtin_amdgcn_s_barrier();
  __builtin_amdgcn_fence(__ATOMIC_ACQUIRE, "workgroup");
}
__device__ __forceinline__ float exp_acc(float x) {
  const float L2E_HI = 1.44269502162933349609375f, L2E_LO = 1.925963033500011e-8f;
  const float hi = x * L2E_HI;
  const float lo = __builtin_fmaf(x, L2E_LO, __builtin_fmaf(x, L2E_HI, -hi));
  const float e = __builtin_amdgcn_exp2f(hi);
  return __builtin_fmaf(e, lo * 0.6931471805599453f, e);
}
#define TIDX ((int)__builtin_amdgcn_workitem_id_x())
#define BIDX ((int)__builtin_amdgcn_workgroup_id_x())
__device__ __forceinline__ float bf2f(unsigned h) { return u2f(h << 16); }
__device__ __forceinline__ float bflo(unsigned u) { return u2f(u << 16); }
__device__ __forceinline__ float bfhi(unsigned u) { return u2f(u & 0xffff0000u); }
__device__ __forceinline__ f32x2 bfup(unsigned u) { return (f32x2){bflo(u), bfhi(u)}; }
__device__ __forceinline__ unsigned pkbf(float lo, float hi) {
  bf16x2_t r = {(__bf16)lo, (__bf16)hi};
  return __builtin_bit_cast(unsigned, r);
}
__device__ __forceinline__ unsigned pkbf2(f32x2 v) { return pkbf(v[0], v[1]); }
__device__ __forceinline__ float rbf(float x) { return bflo(pkbf(x, 0.f)); }
__device__ __forceinline__ unsigned pkh(float lo, float hi) {
  f16x2_t r = {(_Float16)lo, (_Float16)hi};
  return __builtin_bit_cast(unsigned, r);
}

#define STAMP_DECL
#define STAMP_AT(kid, i) do {} while (0)
#define STAMP_FLUSH(kid) do {} while (0)
#define SB() __builtin_amdgcn_sched_barrier(0)
#define WAITV(n) asm volatile("s_waitcnt vmcnt(%0)" ::"n"(n) : "memory")
#define WAITL() asm volatile("s_waitcnt lgkmcnt(0)" ::: "memory")

struct Params {
  const unsigned short* x; const unsigned short* Are; const unsigned short* Aim;
  const unsigned short* Bre; const unsigned short* Bim; const unsigned short* Cre; const unsigned short* Cim;
  const unsigned short* logdt; const unsigned short* Dsk; const unsigned short* w1; const unsigned short* w2;
  unsigned short* out; char* ws;
};

__device__ __forceinline__ unsigned lds_arrive(LAS unsigned* cnt, int lane) {
  WAITL();
  unsigned old = 0;
  if (lane == 0) old = __hip_atomic_fetch_add(cnt, 1u, __ATOMIC_RELAXED, __HIP_MEMORY_SCOPE_WORKGROUP);
  return __builtin_amdgcn_readfirstlane(old);
}
__device__ __forceinline__ void lds_wait(LAS unsigned* cnt, unsigned target) {
  while (__builtin_amdgcn_readfirstlane(__hip_atomic_load(cnt, __ATOMIC_RELAXED, __HIP_MEMORY_SCOPE_WORKGROUP)) < target)
    __builtin_amdgcn_s_sleep(1);
  asm volatile("" ::: "memory");
}
__device__ __forceinline__ void lds_wait2(LAS unsigned* c0, unsigned t0, LAS unsigned* c1, unsigned t1) {
  while (true) {
    const unsigned a = __hip_atomic_load(c0, __ATOMIC_RELAXED, __HIP_MEMORY_SCOPE_WORKGROUP);
    const unsigned b = __hip_atomic_load(c1, __ATOMIC_RELAXED, __HIP_MEMORY_SCOPE_WORKGROUP);
    if (__builtin_amdgcn_readfirstlane(a) >= t0 && __builtin_amdgcn_readfirstlane(b) >= t1) break;
    __builtin_amdgcn_s_sleep(1);
  }
  asm volatile("" ::: "memory");
}
__device__ __forceinline__ void lds_signal(LAS unsigned* cnt, int lane) {
  WAITL();
  if (lane == 0) __hip_atomic_fetch_add(cnt, 1u, __ATOMIC_RELAXED, __HIP_MEMORY_SCOPE_WORKGROUP);
}
__device__ __forceinline__ void glds16(const void* gsrc, unsigned lds_dst) {
  unsigned keep;
  asm volatile("s_mov_b32 %0, m0\n\ts_mov_b32 m0, %2\n\ts_nop 0\n\tglobal_load_lds_dwordx4 %1, off\n\ts_mov_b32 m0, %0"
               : "=&s"(keep) : "v"(gsrc), "s"(lds_dst) : "memory");
}

__device__ __forceinline__ void glds16s(const void* sbase, unsigned voff, unsigned lds_dst) {
  unsigned keep;
  asm volatile("s_mov_b32 %0, m0\n\ts_mov_b32 m0, %3\n\ts_nop 0\n\tglobal_load_lds_dwordx4 %1, %2\n\ts_mov_b32 m0, %0"
               : "=&s"(keep) : "v"(voff), "s"(sbase), "s"(lds_dst) : "memory");
}

template <int MODE> __device__ __forceinline__ void st16(void* ptr, u32x4 v) {
  if (MODE == 0) *(u32x4*)ptr = v;
  else if (MODE == 1) __builtin_nontemporal_store(v, (u32x4*)ptr);
  else if (MODE == 2) asm volatile("global_store_dwordx4 %0, %1, off sc1\n\ts_nop 1" :: "v"(ptr), "v"(v) : "memory");
  else asm volatile("global_store_dwordx4 %0, %1, off sc0 sc1\n\ts_nop 1" :: "v"(ptr), "v"(v) : "memory");
}
typedef __attribute__((ext_vector_type(2))) short s16x2;
template <int V> struct IC { static constexpr int v = V; };
typedef __attribute__((ext_vector_type(3))) unsigned u32x3;
typedef u32x3 __attribute__((aligned(4))) u32x3_a4;
constexpr int PB = 3;
constexpr int KT_P = 528;
constexpr int KT_CH = 54 * KT_P;
constexpr int L1_A = 0;
constexpr int L1_R = L1_A + 65536;
constexpr int L1_ST = L1_R + 2 * KT_CH;
constexpr int L1_CS = L1_ST + 1024;
constexpr int L1_C = L1_CS + 1536;
constexpr int L1_TH = L1_C + 64;
constexpr int L1_SIZE = L1_TH + 2688 + 16;

__device__ __forceinline__ void bsplit(f32x2 v, unsigned& hi, unsigned& lo) {
  hi = pkbf2(v);
  asm("" : "+v"(hi));
  lo = pkbf(v[0] - bflo(hi), v[1] - bfhi(hi));
}
template <int CTRL> __device__ __forceinline__ float dpp_max(float v) {
  const int t = __builtin_amdgcn_update_dpp(0, (int)f2u(v), CTRL, 0xf, 0xf, true);
  return __builtin_fmaxf(v, u2f((unsigned)t));
}
__device__ __forceinline__ float wave_max_nonneg(float v) {
  v = dpp_max<0x140>(dpp_max<0x141>(dpp_max<0x4E>(dpp_max<0xB1>(v))));
  const float a = u2f((unsigned)__builtin_amdgcn_readlane((int)f2u(v), 0));
  const float b = u2f((unsigned)__builtin_amdgcn_readlane((int)f2u(v), 16));
  const float c = u2f((unsigned)__builtin_amdgcn_readlane((int)f2u(v), 32));
  const float d = u2f((unsigned)__builtin_amdgcn_readlane((int)f2u(v), 48));
  return __builtin_fmaxf(__builtin_fmaxf(a, b), __builtin_fmaxf(c, d));
}

__global__ void __launch_bounds__(512, 2) k_phase1(Params p) {
  __shared__ __attribute__((aligned(16))) char lds[L1_SIZE];
  STAMP_DECL
  const int tid = TIDX, lane = tid & 63;
  const int w = __builtin_amdgcn_readfirstlane(tid >> 6);
  const int b = BIDX;
  LAS unsigned* cnt = (LAS unsigned*)(LAS char*)(lds + L1_C);
  if (tid < 16) cnt[tid] = 0u;
  wg_sync();
  STAMP_AT(0, 0);
  const int xcd = b & 7, idx = b >> 3;
  const int rt = (xcd >> 1) * 8 + (idx >> 2), cg = (xcd & 1) * 4 + (idx & 3);
  const int row0 = rt * 64;
  const int c = lane & 15, g = lane >> 4;
  const unsigned gl16 = (unsigned)((g ^ c) * 16);
  const char* arow = lds + L1_A + c * 1024;
  if (w >= 4) {
    const int gi = w - 4, ch = gi & 1, kh = gi >> 1;
    const int col0 = (cg == 7) ? 1280 : cg * 192 + 96 * ch;
    const bool gw = (cg < 7) || (ch == 0);
    const int rot = rt & 7;
    const unsigned lbase = (unsigned)(size_t)(LAS char*)lds;
    if (gw) {
      const char* bsrc = (const char*)p.w1 + (size_t)(256 * kh) * (DFF * 2) + col0 * 2;
      const unsigned bvo = (unsigned)(8 * g * (DFF * 2) + 12 * c);
      u32x3 bq[PB][8];
      auto issueB = [&](int s) {
        const char* q = bsrc + (size_t)((s + rot) & 7) * (32 * DFF * 2);
#pragma unroll
        for (int j = 0; j < 8; ++j) bq[s % PB][j] = *(const u32x3_a4*)(q + j * (DFF * 2) + bvo);
      };
      lds_wait(cnt + 3, 4);
#pragma unroll
      for (int s = 0; s < PB; ++s) issueB(s);
      SB();
      STAMP_AT(0, 4);
      f32x4 acc[4][6];
      f32x4 acs[6];
      const s16x8 onesf = {0x3f80, 0x3f80, 0x3f80, 0x3f80, 0x3f80, 0x3f80, 0x3f80, 0x3f80};
#pragma unroll
      for (int t = 0; t < 6; ++t) acs[t] = (f32x4){0.f, 0.f, 0.f, 0.f};
#pragma unroll
      for (int i = 0; i < 4; ++i)
#pragma unroll
        for (int t = 0; t < 6; ++t) acc[i][t] = (f32x4){0.f, 0.f, 0.f, 0.f};
      lds_wait(cnt + 0, 4);
      STAMP_AT(0, 1);
      auto mkF = [&](int s, int t) -> s16x8 {
        u32x4 F;
#pragma unroll
        for (int q = 0; q < 4; ++q)
          F[q] = __builtin_amdgcn_perm(bq[s % PB][2 * q + 1][t >> 1], bq[s % PB][2 * q][t >> 1], (t & 1) ? 0x07060302u : 0x05040100u);
        return __builtin_bit_cast(s16x8, F);
      };
      s16x8 Fc = mkF(0, 0);
#pragma unroll
      for (int s = 0; s < 8; ++s) {
        s16x8 af[4];
        {
          const char* ap = arow + (gl16 ^ (unsigned)((8 * kh + ((s + rot) & 7)) * 64));
#pragma unroll
          for (int mt = 0; mt < 4; ++mt) af[mt] = *(const s16x8*)(ap + mt * 16384);
        }
        const char* q = bsrc + (size_t)((s + PB - 1 + rot) & 7) * (32 * DFF * 2);
#pragma unroll
        for (int t = 0; t < 6; ++t) {
          s16x8 Fn = Fc;
          if (t + 1 < 6) Fn = mkF(s, t + 1);
          else if (s + 1 < 8) Fn = mkF(s + 1, 0);
          if (s >= 1 && s + PB - 1 < 8 && t < 4) {
            bq[(s + PB - 1) % PB][2 * t] = *(const u32x3_a4*)(q + (2 * t) * (DFF * 2) + bvo);
            bq[(s + PB - 1) % PB][2 * t + 1] = *(const u32x3_a4*)(q + (2 * t + 1) * (DFF * 2) + bvo);
          }
          SB();
#pragma unroll
          for (int mt = 0; mt < 4; ++mt)
            acc[mt][t] = __builtin_amdgcn_mfma_f32_16x16x32_bf16(Fc, af[mt], acc[mt][t], 0, 0, 0);
          acs[t] = __builtin_amdgcn_mfma_f32_16x16x32_bf16(Fc, onesf, acs[t], 0, 0, 0);
          SB();
          Fc = Fn;
        }
      }
      STAMP_AT(0, 2);
      int tz = TIDX;
      asm volatile("" : "+v"(tz));
      {
      const int lane = tz & 63, c = tz & 15, g = (tz >> 4) & 3;
      lds_wait2(cnt + 8, 2, cnt + 9, 2);
      f32x4* rx = (f32x4*)(lds + L1_R + gi * 12288);
      const f32x4* ro = (const f32x4*)(lds + L1_R + (gi ^ 2) * 12288);
      f32x4* csl = (f32x4*)(lds + L1_CS);
      auto fin = [&](auto KH) {
        constexpr int kk = decltype(KH)::v;
#pragma unroll
        for (int m = 0; m < 2; ++m)
#pragma unroll
          for (int t = 0; t < 6; ++t) rx[(m * 6 + t) * 64 + lane] = acc[2 * (1 - kk) + m][t];
        if (c == 0) {
#pragma unroll
          for (int t = 0; t < 6; ++t) csl[(gi * 6 + t) * 4 + g] = acs[t];
        }
        lds_signal(cnt + 5 + ch, lane);
        lds_wait2(cnt + 5 + ch, 2, cnt + 4, 4);
        f32x4 cso[6], oo[2][6], stv[2];
#pragma unroll
        for (int t = 0; t < 6; ++t) cso[t] = csl[((gi ^ 2) * 6 + t) * 4 + g];
#pragma unroll
        for (int m = 0; m < 2; ++m) {
          stv[m] = *(const f32x4*)(lds + L1_ST + (16 * (2 * kk + m) + c) * 16);
#pragma unroll
          for (int t = 0; t < 6; ++t) oo[m][t] = ro[(m * 6 + t) * 64 + lane];
        }
        SB();
#pragma unroll
        for (int t = 0; t < 6; ++t) acs[t] += cso[t];
#pragma unroll
        for (int m = 0; m < 2; ++m) {
          const int mt = 2 * kk + m;
          const f32x4 st = stv[m];
          float hv[24];
#pragma unroll
          for (int t = 0; t < 6; ++t) {
#pragma unroll
            for (int r = 0; r < 4; ++r)
              hv[6 * r + t] = __builtin_fmaf(-st[0], acs[t][r], acc[2 * kk + m][t][r] + oo[m][t][r]) * st[2];
          }
#pragma unroll
          for (int ck = 0; ck < 3; ++ck) {
            const int F0 = col0 + 24 * g + 8 * ck;
            u32x4 o;
#pragma unroll
            for (int e = 0; e < 4; ++e) {
              const s16x2 pv = __builtin_bit_cast(s16x2, pkbf(hv[8 * ck + 2 * e], hv[8 * ck + 2 * e + 1]));
              o[e] = __builtin_bit_cast(unsigned, __builtin_elementwise_max(pv, (s16x2){0, 0}));
            }
            st16<0>(p.ws + WS_H + ((size_t)(rt * 4 + mt) * 43 + (F0 >> 5)) * 1024 + (c + 16 * ((F0 >> 3) & 3)) * 16, o);
          }
        }
      };
      if (kh == 0) fin(IC<0>{}); else fin(IC<1>{});
      }
      STAMP_AT(0, 3);
    }
  } else {
    const int kw = w, chl = kw >> 1, nh = kw & 1;
    const int d = 2 * b + chl;
    char* tch = lds + L1_R + chl * KT_CH;
    unsigned short* th = (unsigned short*)(lds + L1_TH + chl * 1344);
    float a2, fr, wsc_inv;
    f32x2 W;
    bool grow;
    u32x4 xq[16];
    u32x2 w2r[8];
    const int blk = 4 * b + kw;
    const bool dow2 = blk < 344;
    {
      {
        const char* xr = (const char*)p.x + (size_t)(row0 + 16 * kw) * 1024 + lane * 16;
#pragma unroll
        for (int i = 0; i < 16; ++i) xq[i] = *(const u32x4*)(xr + i * 1024);
      }
      SB();
      const size_t o = (size_t)d * NS + 64 * nh + lane, o2 = o ^ 64;
      const float lg = bf2f(p.logdt[d]);
      const float are = bf2f(p.Are[o]), aim = bf2f(p.Aim[o]);
      const float bre = bf2f(p.Bre[o]), bim = bf2f(p.Bim[o]);
      const float cr = bf2f(p.Cre[o]), ci = bf2f(p.Cim[o]);
      const float are2 = bf2f(p.Are[o2]);
      const float bre2 = bf2f(p.Bre[o2]), bim2 = bf2f(p.Bim[o2]);
      const float cr2 = bf2f(p.Cre[o2]), ci2 = bf2f(p.Cim[o2]);
      if (dow2) {
        const int ct = blk / 43, ks = blk % 43;
        const unsigned short* wsrc = p.w2 + (size_t)(32 * ks + 8 * g) * DM + 64 * ct + 4 * c;
#pragma unroll
        for (int jj = 0; jj < 8; ++jj) w2r[jj] = *(const u32x2*)(wsrc + (size_t)jj * DM);
      }
      SB();
      if (lane == 0) __hip_atomic_fetch_add(cnt + 3, 1u, __ATOMIC_RELAXED, __HIP_MEMORY_SCOPE_WORKGROUP);
      SB();
      const float dt = rbf(exp_acc(lg));
      const float br = dt * bre, bi = dt * bim;
      a2 = (dt * are) * 1.4426950408889634f;
      fr = (dt * aim) * 0.15915494309189535f;
      W = (f32x2){cr * br - ci * bi, -(cr * bi + ci * br)};
      const float m1 = (__builtin_fabsf(cr) + __builtin_fabsf(ci)) * (__builtin_fabsf(bre) + __builtin_fabsf(bim));
      const float m2 = (__builtin_fabsf(cr2) + __builtin_fabsf(ci2)) * (__builtin_fabsf(bre2) + __builtin_fabsf(bim2));
      const float mw = wave_max_nonneg(__builtin_fmaxf(m1, m2)) * dt;
      const int eb = (int)(f2u(mw) >> 23);
      int ex = 136 - eb;
      ex = ex > 120 ? 120 : (ex < -120 ? -120 : ex);
      const float wsc = u2f((unsigned)(127 + ex) << 23);
      wsc_inv = u2f((unsigned)(127 - ex) << 23);
      W = (f32x2){W[0] * wsc, W[1] * wsc};
      const float gmax = __builtin_fmaxf(dt * are, dt * are2);
      grow = __builtin_amdgcn_ballot_w64(!(gmax * 2048.f < 2.f) || eb >= 250) != 0ull;
    }
    STAMP_AT(0, 1);
    auto cexpq = [&](float q) -> f32x2 {
      const float e = __builtin_amdgcn_exp2f(a2 * q);
      const float ph = __builtin_amdgcn_fractf(fr * q);
      return (f32x2){__builtin_amdgcn_cosf(ph) * e, __builtin_amdgcn_sinf(ph) * e};
    };
    auto cmul = [](f32x2 a, f32x2 bb) -> f32x2 {
      return (f32x2){__builtin_fmaf(a[0], bb[0], -(a[1] * bb[1])), __builtin_fmaf(a[0], bb[1], a[1] * bb[0])};
    };
    char* tw = tch + (64 * nh + lane) * 4;
    auto put = [&](int row, f32x2 v) {
      float v0 = v[0], v1 = v[1];
      asm("" : "+v"(v0), "+v"(v1));
      *(unsigned*)(tw + row * KT_P) = pkh(v0, v1);
    };
    auto xstore = [&]() {
#pragma unroll
      for (int i = 0; i < 16; ++i) *(u32x4*)(lds + L1_A + (16 * kw + i) * 1024 + ((lane ^ i) * 16)) = xq[i];
      lds_signal(cnt + 0, lane);
      if (dow2) {
        u32x4 f4[4];
#pragma unroll
        for (int q2 = 0; q2 < 4; ++q2) {
          f4[0][q2] = __builtin_amdgcn_perm(w2r[2 * q2 + 1][0], w2r[2 * q2][0], 0x05040100u);
          f4[1][q2] = __builtin_amdgcn_perm(w2r[2 * q2 + 1][0], w2r[2 * q2][0], 0x07060302u);
          f4[2][q2] = __builtin_amdgcn_perm(w2r[2 * q2 + 1][1], w2r[2 * q2][1], 0x05040100u);
          f4[3][q2] = __builtin_amdgcn_perm(w2r[2 * q2 + 1][1], w2r[2 * q2][1], 0x07060302u);
        }
        char* dst2 = p.ws + WS_W2F + (size_t)blk * 4096 + lane * 16;
#pragma unroll
        for (int t = 0; t < 4; ++t) st16<2>(dst2 + t * 1024, f4[t]);
      }
    };
    xstore();
    SB();
    if (!grow) {
      const f32x2 E1 = cexpq(1.f), E256 = cexpq(256.f), E8 = cexpq(8.f);
      f32x2 y0 = (f32x2){1.f, 0.f}, y1 = cexpq(64.f), y2 = cexpq(128.f), y3 = cexpq(192.f);
      const f32x2 C1 = {E1[0], -E1[1]}, CL = {E256[0], -E256[1]};
      const f32x2 C2 = cmul(C1, C1), C4 = cmul(C2, C2);
      f32x2 xs[21];
      xs[0] = W;
      xs[8] = cmul(W, CL);
      xs[1] = cmul(xs[0], C1);  xs[12] = cmul(xs[8], CL);  xs[9] = cmul(xs[8], C2);
      xs[2] = cmul(xs[1], C1);  xs[14] = cmul(xs[12], CL); xs[10] = cmul(xs[9], C2); xs[13] = cmul(xs[12], C4);
      xs[3] = cmul(xs[2], C1);  xs[16] = cmul(xs[14], CL); xs[11] = cmul(xs[10], C2); xs[15] = cmul(xs[14], C4);
      xs[4] = cmul(xs[0], C4);  xs[17] = cmul(xs[16], CL);
      xs[5] = cmul(xs[1], C4);  xs[18] = cmul(xs[17], CL);
      xs[6] = cmul(xs[2], C4);  xs[19] = cmul(xs[18], CL);
      xs[7] = cmul(xs[3], C4);  xs[20] = cmul(xs[19], CL);
#pragma unroll
      for (int i = 0; i < 21; ++i) put(i, xs[i]);
      put(21, y0); put(29, y1); put(37, y2); put(45, y3);
#pragma unroll
      for (int i = 1; i < 8; ++i) {
        y0 = cmul(y0, E8); y1 = cmul(y1, E8); y2 = cmul(y2, E8); y3 = cmul(y3, E8);
        put(21 + i, y0); put(29 + i, y1); put(37 + i, y2); put(45 + i, y3);
      }
    } else {
      *(f32x4*)(tch + (64 * nh + lane) * 16) = (f32x4){a2, fr, W[0], -W[1]};
    }
    lds_signal(cnt + 1 + chl, lane);
    STAMP_AT(0, 4);
    {
      lds_wait(cnt + 0, 4);
      const s16x8 onesf = {0x3f80, 0x3f80, 0x3f80, 0x3f80, 0x3f80, 0x3f80, 0x3f80, 0x3f80};
      f32x4 as0 = {0.f, 0.f, 0.f, 0.f}, aq0 = as0, as1 = as0, aq1 = as0;
      const char* ab2 = arow + kw * 16384;
      s16x8 xf[16];
#pragma unroll
      for (int s = 0; s < 16; ++s) xf[s] = *(const s16x8*)(ab2 + (gl16 ^ (unsigned)(s * 64)));
      SB();
#pragma unroll
      for (int s = 0; s < 16; s += 2) {
        as0 = __builtin_amdgcn_mfma_f32_16x16x32_bf16(onesf, xf[s], as0, 0, 0, 0);
        aq0 = __builtin_amdgcn_mfma_f32_16x16x32_bf16(xf[s], xf[s], aq0, 0, 0, 0);
        as1 = __builtin_amdgcn_mfma_f32_16x16x32_bf16(onesf, xf[s + 1], as1, 0, 0, 0);
        aq1 = __builtin_amdgcn_mfma_f32_16x16x32_bf16(xf[s + 1], xf[s + 1], aq1, 0, 0, 0);
      }
      const int r3 = c & 3;
      const f32x4 aq = aq0 + aq1;
      const float q = (r3 == 0) ? aq[0] : (r3 == 1) ? aq[1] : (r3 == 2) ? aq[2] : aq[3];
      const float mean = (as0[0] + as1[0]) * (1.f / DM);
      const float var = __builtin_fmaxf(q * (1.f / DM) - mean * mean, 0.f);
      const float mb = rbf(mean);
      const float dn = rbf(__builtin_sqrtf(rbf(rbf(var) + 1e-5f)));
      if (g == (c >> 2)) {
        const int r = 16 * kw + c;
        *(f32x4*)(lds + L1_ST + r * 16) = (f32x4){mb, dn, 1.f / dn, 0.f};
        if (cg == 0) *(f32x2*)(p.ws + WS_ST + (size_t)(row0 + r) * 8) = (f32x2){mb, dn};
      }
      lds_signal(cnt + 4, lane);
    }
    STAMP_AT(0, 8);
    lds_wait(cnt + 1 + chl, 2);
    f32x4 acc[2];
    acc[0] = (f32x4){0.f, 0.f, 0.f, 0.f}; acc[1] = acc[0];
    const int j = 16 * nh + c;
    if (!grow) {
      const char* fa = tch + c * KT_P + 16 * g;
      const char* fb = tch + (21 + j) * KT_P + 16 * g;
      f16x8 fA0[8], fA1[8], fB[8];
#pragma unroll
      for (int s = 0; s < 8; ++s) {
        fB[s] = *(const f16x8*)(fb + 64 * s);
        fA0[s] = *(const f16x8*)(fa + 64 * s);
        fA1[s] = *(const f16x8*)(fa + 16 * KT_P + 64 * s);
      }
      SB();
#pragma unroll
      for (int s = 0; s < 8; ++s) {
        acc[0] = __builtin_amdgcn_mfma_f32_16x16x32_f16(fA0[s], fB[s], acc[0], 0, 0, 0);
        acc[1] = __builtin_amdgcn_mfma_f32_16x16x32_f16(fA1[s], fB[s], acc[1], 0, 0, 0);
      }
    } else {
      for (int e = 0; e < 8; ++e) {
        const int row = 16 * (e >> 2) + 4 * g + (e & 3);
        const int qx = row < 8 ? row : row < 12 ? 256 + 2 * (row - 8) : row < 16 ? 512 + 256 * ((row - 12) >> 1) + 4 * (row & 1)
                                                                   : row < 21 ? 1024 + 256 * (row - 16) : 0;
        const float q = (float)(qx + 8 * j);
        float sacc = 0.f;
        for (int n = 0; n < 128; ++n) {
          const f32x4 pr = *(const f32x4*)(tch + n * 16);
          const float ph = __builtin_amdgcn_fractf(pr[1] * q);
          sacc += __builtin_amdgcn_exp2f(pr[0] * q) * (pr[2] * __builtin_amdgcn_cosf(ph) - pr[3] * __builtin_amdgcn_sinf(ph));
        }
        if (e < 4) acc[0][e & 3] = sacc; else acc[1][e & 3] = sacc;
      }
    }
    STAMP_AT(0, 5);
    {
      const bool v1 = (g == 0), v2 = (g == 1 && j == 0);
      float mx = __builtin_fmaxf(__builtin_fmaxf(__builtin_fabsf(acc[0][0]), __builtin_fabsf(acc[0][1])),
                                 __builtin_fmaxf(__builtin_fabsf(acc[0][2]), __builtin_fabsf(acc[0][3])));
      const float m1 = __builtin_fmaxf(__builtin_fmaxf(__builtin_fabsf(acc[1][1]), __builtin_fabsf(acc[1][2])), __builtin_fabsf(acc[1][3]));
      mx = __builtin_fmaxf(mx, (v1 || v2) ? __builtin_fabsf(acc[1][0]) : 0.f);
      mx = __builtin_fmaxf(mx, v1 ? m1 : 0.f);
      mx = wave_max_nonneg(mx);
      float* mxl = (float*)(lds + L1_TH + 2688) + kw;
      if (lane == 0) *mxl = mx;
      lds_signal(cnt + 8 + chl, lane);
    {
      lds_wait(cnt + 4, 4);
      const int q4 = c >> 2, pp = c & 3;
      f32x4 st[16];
#pragma unroll
      for (int r = 0; r < 16; ++r) st[r] = *(const f32x4*)(lds + L1_ST + (16 * g + r) * 16);
      const int ck = 8 * cg + 2 * kw + (pp >> 1);
      unsigned ov[8];
      u32x2 vv[4];
#pragma unroll
      for (int k = 0; k < 4; ++k) {
        const int r16 = 4 * k + q4;
        const char* a0 = lds + L1_A + (16 * g + r16) * 1024 + ((ck ^ r16) * 16) + 8 * (pp & 1);
        vv[k] = __builtin_bit_cast(u32x2, __builtin_amdgcn_ds_read_tr16_b64_v4i16((LAS s16x4*)a0));
      }
      SB();
#pragma unroll
      for (int k = 0; k < 4; ++k) {
        const u32x2 v = vv[k];
#pragma unroll
        for (int e = 0; e < 2; ++e) {
          const unsigned ve = v[e];
          const f32x4 s0 = st[4 * k + 2 * e], s1 = st[4 * k + 2 * e + 1];
          const float t0 = rbf(bflo(ve) - s0[0]), t1 = rbf(bfhi(ve) - s1[0]);
          float q0 = t0 * s0[2], q1 = t1 * s1[2];
          q0 = __builtin_fmaf(__builtin_fmaf(-q0, s0[1], t0), s0[2], q0);
          q1 = __builtin_fmaf(__builtin_fmaf(-q1, s1[1], t1), s1[2], q1);
          const unsigned xb = pkbf(q0, q1);
          ov[2 * k + e] = pkh(bflo(xb), bfhi(xb));
        }
      }
      unsigned short* dst = (unsigned short*)(p.ws + WS_XNT) + (size_t)(64 * cg + 16 * kw + c) * SL + row0 + 16 * g;
      st16<2>(dst, (u32x4){ov[0], ov[1], ov[2], ov[3]});
      st16<2>(dst + 8, (u32x4){ov[4], ov[5], ov[6], ov[7]});
    }
    {
      char* ydst = p.ws + WS_YT + (size_t)b * 8192 + kw * 2048 + lane * 16;
      const u32x4 sent = {0xffffffffu, 0xffffffffu, 0xffffffffu, 0xffffffffu};
      st16<2>(ydst, sent);
      st16<2>(ydst + 1024, sent);
    }
      lds_wait(cnt + 8 + chl, 2);
      mx = __builtin_fmaxf(mx, ((const float*)(lds + L1_TH + 2688))[kw ^ 1]);
      const int eb = (int)(f2u(mx) >> 23);
      int ex = 140 - eb;
      ex = (eb == 0 || eb == 255) ? 0 : (ex > 120 ? 120 : ex);
      const float sc = u2f((unsigned)(127 + ex) << 23);
      if (lane == 0 && nh == 0) ((float*)(p.ws + WS_KS))[d] = u2f((unsigned)(127 - ex) << 23) * wsc_inv;
      const int b0 = (g < 2 ? 4 * g + 8 * j : g == 2 ? 256 + 4 * j : 384 + 2 * j);
      *(unsigned*)(th + b0) = pkh(acc[0][0] * sc, acc[0][1] * sc);
      *(unsigned*)(th + b0 + (g == 3 ? 64 : 2)) = pkh(acc[0][2] * sc, acc[0][3] * sc);
      const unsigned p01 = pkh(acc[1][0] * sc, acc[1][1] * sc), p23 = pkh(acc[1][2] * sc, acc[1][3] * sc);
      if (v1) {
        th[512 + j] = (unsigned short)p01; th[544 + j] = (unsigned short)(p01 >> 16);
        th[576 + j] = (unsigned short)p23; th[608 + j] = (unsigned short)(p23 >> 16);
      }
      if (v2) th[640] = (unsigned short)p01;
      lds_signal(cnt + 10 + chl, lane);
    }
    STAMP_AT(0, 6);
    STAMP_AT(0, 7);
    {
      lds_wait(cnt + 10 + chl, 2);
      const unsigned short* tu = th;
      unsigned short* kr = (unsigned short*)(p.ws + WS_KR) + (size_t)d * KRL;
      if (nh == 0) {
#pragma unroll
        for (int k = 0; k < 2; ++k) {
          const int ck = lane + 64 * k, M = 256 - ck;
          const unsigned a = tu[384 + M], bb = tu[383 + M];
          const unsigned aa = a | (a << 16), b2 = bb | (bb << 16);
          const unsigned mid = ((M & 1) ? bb : a) | (bb << 16);
          st16<2>(kr + 8 * ck, (u32x4){aa, aa, mid, b2});
        }
        if (lane < 4) st16<2>(kr + 8 * (256 + lane), (u32x4){lane == 0 ? (unsigned)tu[0] : 0u, 0u, 0u, 0u});
      } else {
        {
          const int ck = 128 + lane, M = 256 - ck;
          const unsigned a = tu[256 + 2 * M], bb = tu[255 + 2 * M], cc = tu[254 + 2 * M];
          st16<2>(kr + 8 * ck, (u32x4){a | (a << 16), a | (bb << 16), bb | (bb << 16), cc | (cc << 16)});
        }
        {
          const int ck = 192 + lane, M = 256 - ck;
          const bool s1 = lane < 32;
          const int base = s1 ? 128 + 4 * M : 8 * M;
          const int o1[8] = {0, 0, 1, 2, 2, 2, 3, 4};
          unsigned hv[8];
#pragma unroll
          for (int e = 0; e < 8; ++e) hv[e] = tu[base - (s1 ? o1[e] : e)];
          st16<2>(kr + 8 * ck, (u32x4){hv[0] | (hv[1] << 16), hv[2] | (hv[3] << 16), hv[4] | (hv[5] << 16), hv[6] | (hv[7] << 16)});
        }
      }
    }
    STAMP_AT(0, 3);
    STAMP_AT(0, 9);
  }
  STAMP_FLUSH(0);
}

#define GAS __attribute__((address_space(1)))
constexpr int KCS = KRL * 2;
constexpr int CV_CH = 8 * KCS + 4608;
constexpr int L2_CR = 2 * CV_CH;
constexpr int L2_GR = L2_CR + 16384;
constexpr int L2_Y = L2_GR + 49152;
constexpr int L2_T1 = L2_Y + 64 * 136;
constexpr int L2_C = L2_T1 + 8192;
constexpr int L2_SIZE = L2_C + 64;

__global__ void __launch_bounds__(512, 2) k_phase2(Params p) {
  __shared__ __attribute__((aligned(16))) char lds[L2_SIZE];
  STAMP_DECL
  const int tid = TIDX, lane = tid & 63;
  const int w = __builtin_amdgcn_readfirstlane(tid >> 6);
  const int b = BIDX;
  LAS unsigned* cnt = (LAS unsigned*)(LAS char*)(lds + L2_C);
  if (tid < 16) cnt[tid] = 0u;
  wg_sync();
  STAMP_AT(1, 0);
  const int xcd = b & 7, idx = b >> 3;
  const int rt = xcd * 4 + (idx >> 3), ct = idx & 7;
  const int row0 = rt * 64, col0 = ct * 64;
  const int c = lane & 15, g = lane >> 4;
  GAS unsigned* flags = (GAS unsigned*)(p.ws + WS_FL);
  if (w < 4) {
    const int chl = w >> 1, hf = w & 1;
    const int d = 2 * b + chl;
    char* kl = lds + chl * CV_CH;
    char* ul = kl + 8 * KCS;
    const int erow = row0 + 16 * w + 4 * g;
    u32x2 xv[4];
    f32x4 st01, st23;
    u32x2 dk;
    float isc;
    {
      const int t2 = tid & 127;
      const u32x4* ksrc = (const u32x4*)(p.ws + WS_KR + (size_t)d * KCS);
      const u32x4* usrc = (const u32x4*)(p.ws + WS_XNT + (size_t)d * SL * 2);
      u32x4 kc[3], kp[3];
#pragma unroll
      for (int k = 0; k < 3; ++k) {
        int ci = t2 + 128 * k;
        ci = ci < 260 ? ci : 259;
        kc[k] = ksrc[ci];
        kp[k] = ksrc[ci > 0 ? ci - 1 : 0];
      }
      u32x4 uv0 = usrc[t2], uv1 = usrc[t2 + 128];
      SB();
#pragma unroll
      for (int r = 0; r < 4; ++r) xv[r] = *(const u32x2*)(p.x + (size_t)(erow + r) * DM + col0 + 4 * c);
      st01 = *(const f32x4*)(p.ws + WS_ST + (size_t)erow * 8);
      st23 = *(const f32x4*)(p.ws + WS_ST + (size_t)erow * 8 + 16);
      dk = *(const u32x2*)(p.Dsk + col0 + 4 * c);
      isc = ((const float*)(p.ws + WS_KS))[d];
      SB();
      const u32x4 z = {0u, 0u, 0u, 0u};
      if (t2 < 32) *(u32x4*)(ul + t2 * 16) = z;
#pragma unroll
      for (int k = 0; k < 3; ++k) {
        const int ci = t2 + 128 * k;
        if (ci < 260) {
          unsigned wd[8] = {kp[k][0], kp[k][1], kp[k][2], kp[k][3], kc[k][0], kc[k][1], kc[k][2], kc[k][3]};
          unsigned od[7];
#pragma unroll
          for (int j = 0; j < 7; ++j) od[j] = __builtin_amdgcn_alignbit(wd[j + 1], wd[j], 16);
          char* dst = kl + ci * 16;
#pragma unroll
          for (int sg = 0; sg < 8; ++sg) {
            u32x4 o;
            if ((sg & 1) == 0) { const int s0 = (8 - sg) >> 1; o = (u32x4){wd[s0], wd[s0 + 1], wd[s0 + 2], wd[s0 + 3]}; }
            else { const int s0 = (7 - sg) >> 1; o = (u32x4){od[s0], od[s0 + 1], od[s0 + 2], od[s0 + 3]}; }
            *(u32x4*)(dst + sg * KCS) = o;
          }
        }
      }
      *(u32x4*)(ul + 512 + t2 * 16) = uv0;
      *(u32x4*)(ul + 512 + (t2 + 128) * 16) = uv1;
    }
    lds_signal(cnt + chl, lane);
    lds_wait(cnt + chl, 2);
    STAMP_AT(1, 1);
    const int a = c;
    f32x4 acc[8];
#pragma unroll
    for (int r = 0; r < 8; ++r) acc[r] = (f32x4){0.f, 0.f, 0.f, 0.f};
    const char* ab = kl + (a & 7) * KCS + 2 * (2032 + 8 * g - 8 * (a >> 3)) - 64 * 4 * hf;
    const char* bb = ul + 2 * (240 + 16 * a + 8 * g) - 64 * 4 * hf;
    f16x8 bf[2][8], af[2][8];
#pragma unroll
    for (int t = 0; t < 8; ++t) { bf[0][t] = *(const f16x8*)(bb + 512 * t); af[0][t] = *(const f16x8*)(ab - 512 * t); }
#pragma unroll
    for (int i = 0; i < 4; ++i) {
      if (i + 1 < 4) {
#pragma unroll
        for (int t = 0; t < 8; ++t) {
          bf[(i + 1) & 1][t] = *(const f16x8*)(bb - 64 * (i + 1) + 512 * t);
          af[(i + 1) & 1][t] = *(const f16x8*)(ab - 64 * (i + 1) - 512 * t);
        }
      }
      SB();
#pragma unroll
      for (int v = 0; v < 8; ++v)
#pragma unroll
        for (int t = v; t < 8; ++t)
          acc[t] = __builtin_amdgcn_mfma_f32_16x16x32_f16(af[i & 1][v], bf[i & 1][t - v], acc[t], 0, 0, 0);
      SB();
    }
    STAMP_AT(1, 2);
    f32x4* red = (f32x4*)(lds + L2_CR + chl * 8192);
    f32x4 mine[4];
    if (hf) {
#pragma unroll
      for (int r = 0; r < 4; ++r) { red[(4 + r) * 64 + lane] = acc[r]; mine[r] = acc[4 + r]; }
    } else {
#pragma unroll
      for (int r = 0; r < 4; ++r) { red[r * 64 + lane] = acc[4 + r]; mine[r] = acc[r]; }
    }
    lds_signal(cnt + 2 + chl, lane);
    lds_wait(cnt + 2 + chl, 2);
    {
      unsigned pk[4][2];
#pragma unroll
      for (int r = 0; r < 4; ++r) {
        const f32x4 v = (mine[r] + red[(4 * (1 - hf) + r) * 64 + lane]) * isc;
        pk[r][0] = pkbf(v[0], v[1]);
        pk[r][1] = pkbf(v[2], v[3]);
      }
      const auto rsrc = __builtin_amdgcn_make_buffer_rsrc((void*)(p.ws + WS_YT), 0, DM * SL * 2, 0x00020000);
#pragma unroll
      for (int r = 0; r < 2; ++r) {
        auto s0 = __builtin_amdgcn_permlane16_swap(pk[r][0], pk[r + 2][0], false, false);
        auto s1 = __builtin_amdgcn_permlane16_swap(pk[r][1], pk[r + 2][1], false, false);
        const unsigned a0 = s0[0], b0 = s0[1], a1 = s1[0], b1 = s1[1];
        const u32x4 o = {a0, a1, b0, b1};
        const int tile = r + 2 * (g & 1);
        const int off = (d * SL + 1024 * hf + 256 * tile + 16 * a + 8 * (g >> 1)) * 2;
        __builtin_amdgcn_raw_buffer_store_b128(o, rsrc, off, 0, 16);
      }
    }
    STAMP_AT(1, 3);
    {
      const int ch = 16 * w + (lane >> 2), k0 = (lane & 3) * 2;
      const auto rsrc = __builtin_amdgcn_make_buffer_rsrc((void*)(p.ws + WS_YT), 0, DM * SL * 2, 0x00020000);
      const int off = ((col0 + ch) * SL + row0) * 2 + k0 * 16;
      float xf[4][4], xd[4][4];
      {
        const float dkf[4] = {bflo(dk[0]), bfhi(dk[0]), bflo(dk[1]), bfhi(dk[1])};
#pragma unroll
        for (int r = 0; r < 4; ++r) {
          xf[r][0] = bflo(xv[r][0]); xf[r][1] = bfhi(xv[r][0]); xf[r][2] = bflo(xv[r][1]); xf[r][3] = bfhi(xv[r][1]);
          const float mb = (r == 0) ? st01[0] : (r == 1) ? st01[2] : (r == 2) ? st23[0] : st23[2];
          const float den = (r == 0) ? st01[1] : (r == 1) ? st01[3] : (r == 2) ? st23[1] : st23[3];
          const float rc = 1.f / den;
#pragma unroll
          for (int t = 0; t < 4; ++t) {
            const float a0 = rbf(xf[r][t] - mb);
            float q = a0 * rc;
            q = __builtin_fmaf(__builtin_fmaf(-q, den, a0), rc, q);
            xd[r][t] = rbf(rbf(q) * dkf[t]);
            asm volatile("" : "+v"(xd[r][t]), "+v"(xf[r][t]));
          }
        }
      }
      u32x4 v0, v1;
      unsigned spins = 0;
      lds_wait(cnt + 7, 4);
      while (true) {
        v0 = __builtin_amdgcn_raw_buffer_load_b128(rsrc, off, 0, 16);
        v1 = __builtin_amdgcn_raw_buffer_load_b128(rsrc, off + 16, 0, 16);
        bool bad = false;
#pragma unroll
        for (int e = 0; e < 4; ++e) {
          const unsigned a0 = v0[e], a1 = v1[e];
          bad |= ((a0 & 0xffffu) == 0xffffu) | ((a0 >> 16) == 0xffffu) | ((a1 & 0xffffu) == 0xffffu) | ((a1 >> 16) == 0xffffu);
        }
        if (__builtin_amdgcn_ballot_w64(bad) == 0ull) break;
        if (++spins > (1u << 18)) break;
        __builtin_amdgcn_s_sleep(4);
      }
      STAMP_AT(1, 4);
      char* yd = lds + L2_Y + ch * 136 + k0 * 16;
      *(u32x2*)yd = (u32x2){v0[0], v0[1]}; *(u32x2*)(yd + 8) = (u32x2){v0[2], v0[3]};
      *(u32x2*)(yd + 16) = (u32x2){v1[0], v1[1]}; *(u32x2*)(yd + 24) = (u32x2){v1[2], v1[3]};
      lds_signal(cnt + 4, lane);
      lds_wait(cnt + 4, 4);
      u32x2 yv[4];
#pragma unroll
      for (int t = 0; t < 4; ++t) yv[t] = *(const u32x2*)(lds + L2_Y + (4 * c + t) * 136 + (16 * w + 4 * g) * 2);
#pragma unroll
      for (int r = 0; r < 4; ++r) {
        float o[4];
#pragma unroll
        for (int t = 0; t < 4; ++t) {
          const unsigned yw = yv[t][r >> 1];
          const float y = (r & 1) ? bfhi(yw) : bflo(yw);
          o[t] = xf[r][t] + rbf(y + xd[r][t]);
        }
        *(u32x2*)(lds + L2_T1 + ((w * 4 + r) * 64 + lane) * 8) = (u32x2){pkbf(o[0], o[1]), pkbf(o[2], o[3])};
      }
    }
    lds_signal(cnt + 8 + w, lane);
    STAMP_AT(1, 5);
  } else {
    const int gw = w - 4;
    const int s0 = 11 * gw;
    const int ns = (gw == 3) ? 10 : 11;
    const char* hb = p.ws + WS_H + ((size_t)(rt * 4) * 43 + s0) * 1024 + lane * 16;
    const char* wb = p.ws + WS_W2F + ((size_t)ct * 43 + s0) * 4096 + lane * 16;
    constexpr int P = 4;
    u32x4 ar[P][4];
    u32x4 br[P][4];
    const int rot = 0;

    auto issue = [&](int s) {
      int ks = s + rot;
      ks = ks >= ns ? ks - ns : ks;
#pragma unroll
      for (int mt = 0; mt < 4; ++mt) ar[s % P][mt] = *(const u32x4*)(hb + (size_t)mt * 43 * 1024 + ks * 1024);
#pragma unroll
      for (int j = 0; j < 4; ++j) br[s % P][j] = *(const u32x4*)(wb + ks * 4096 + j * 1024);
    };
#pragma unroll
    for (int s = 0; s < P; ++s) issue(s);
    SB();
    f32x4 acc[4][4];
#pragma unroll
    for (int i = 0; i < 4; ++i)
#pragma unroll
      for (int t = 0; t < 4; ++t) acc[i][t] = (f32x4){0.f, 0.f, 0.f, 0.f};
#pragma unroll
    for (int s = 0; s < 11; ++s) {
      if (s < ns) {
#pragma unroll
        for (int mt = 0; mt < 4; ++mt)
#pragma unroll
          for (int t = 0; t < 4; ++t)
            acc[mt][t] = __builtin_amdgcn_mfma_f32_16x16x32_bf16(__builtin_bit_cast(s16x8, ar[s % P][mt]),
                                                                 __builtin_bit_cast(s16x8, br[s % P][t]), acc[mt][t], 0, 0, 0);
        SB();
        if (s + P < 11 && s + P < ns) issue(s + P);
        if (s == 4 && lane == 0) __hip_atomic_fetch_add(cnt + 7, 1u, __ATOMIC_RELAXED, __HIP_MEMORY_SCOPE_WORKGROUP);
        SB();
      }
    }

    const int erow = row0 + 16 * gw + 4 * g;
    STAMP_AT(1, 1);
    f32x4* red = (f32x4*)(lds + L2_GR);
    f32x4 f[4];
#pragma unroll
    for (int mt = 0; mt < 4; ++mt) {
      if (mt != gw) {
        const int slot = (gw > mt) ? (gw - 1) : gw;
#pragma unroll
        for (int t = 0; t < 4; ++t) red[((mt * 3 + slot) * 4 + t) * 64 + lane] = acc[mt][t];
      } else {
#pragma unroll
        for (int t = 0; t < 4; ++t) f[t] = acc[mt][t];
      }
    }
    lds_signal(cnt + 6, lane);
    lds_wait(cnt + 6, 4);
    STAMP_AT(1, 2);
    f32x4 pr[4][3];
#pragma unroll
    for (int t = 0; t < 4; ++t)
#pragma unroll
      for (int sl = 0; sl < 3; ++sl) pr[t][sl] = red[((gw * 3 + sl) * 4 + t) * 64 + lane];
    SB();
#pragma unroll
    for (int t = 0; t < 4; ++t) {
      f[t] += (pr[t][0] + pr[t][1]) + pr[t][2];
#pragma unroll
      for (int r = 0; r < 4; ++r) f[t][r] = rbf(f[t][r]);
      asm volatile("" : "+v"(f[t]));
    }
    lds_wait(cnt + 8 + gw, 1);
    STAMP_AT(1, 3);
#pragma unroll
    for (int r = 0; r < 4; ++r) {
      const u32x2 tv = *(const u32x2*)(lds + L2_T1 + ((gw * 4 + r) * 64 + lane) * 8);
      const unsigned t0 = tv[0], t1 = tv[1];
      u32x2 ov;
      ov[0] = pkbf(bflo(t0) + f[0][r], bfhi(t0) + f[1][r]);
      ov[1] = pkbf(bflo(t1) + f[2][r], bfhi(t1) + f[3][r]);
      asm volatile("global_store_dwordx2 %0, %1, off sc1" :: "v"(p.out + (size_t)(erow + r) * DM + col0 + 4 * c), "v"(ov) : "memory");
    }
    STAMP_AT(1, 4);
  }
  STAMP_FLUSH(1);
}

#if defined(__HIP_DEVICE_COMPILE__)
#pragma clang attribute pop
#endif

extern "C" void kernel_launch(void* const* d_in, const int* in_sizes, int n_in,
                              void* d_out, int out_size, void* d_ws, size_t ws_size,
                              hipStream_t stream) {
  Params p;
  p.x = (const unsigned short*)d_in[0];
  p.Are = (const unsigned short*)d_in[1];
  p.Aim = (const unsigned short*)d_in[2];
  p.Bre = (const unsigned short*)d_in[3];
  p.Bim = (const unsigned short*)d_in[4];
  p.Cre = (const unsigned short*)d_in[5];
  p.Cim = (const unsigned short*)d_in[6];
  p.logdt = (const unsigned short*)d_in[7];
  p.Dsk = (const unsigned short*)d_in[8];
  p.w1 = (const unsigned short*)d_in[10];
  p.w2 = (const unsigned short*)d_in[11];
  p.out = (unsigned short*)d_out;
  p.ws = (char*)d_ws;
  k_phase1<<<256, 512, 0, stream>>>(p);
  k_phase2<<<256, 512, 0, stream>>>(p);
}
```

```cpp
#include <hip/hip_runtime.h>
#include <hip/hip_bf16.h>
#include <stdint.h>

typedef __attribute__((ext_vector_type(8))) short s16x8;
typedef __attribute__((ext_vector_type(4))) short s16x4;
typedef __attribute__((ext_vector_type(8))) _Float16 f16x8;
typedef __attribute__((ext_vector_type(4))) float f32x4;
typedef __attribute__((ext_vector_type(2))) float f32x2;
typedef __attribute__((ext_vector_type(16))) float f32x16;
typedef __attribute__((ext_vector_type(4))) unsigned u32x4;
typedef __attribute__((ext_vector_type(2))) unsigned u32x2;
typedef __attribute__((ext_vector_type(2))) __bf16 bf16x2_t;
typedef __attribute__((ext_vector_type(2))) _Float16 f16x2_t;
#define LAS __attribute__((address_space(3)))

constexpr int SL = 2048;
constexpr int DM = 512;
constexpr int NS = 128;
constexpr int DFF = 1376;
constexpr int KRL = 2080;

constexpr size_t WS_H = 0;
constexpr size_t WS_XNT = WS_H + (size_t)128 * 43 * 1024;
constexpr size_t WS_KR = WS_XNT + (size_t)DM * SL * 2;
constexpr size_t WS_YT = WS_KR + (size_t)DM * KRL * 2;
constexpr size_t WS_ST = WS_YT + (size_t)DM * SL * 2;
constexpr size_t WS_KS = WS_ST + (size_t)SL * 8;
constexpr size_t WS_FL = WS_KS + 4096;
constexpr size_t WS_W2F = WS_FL + 32768;

#if defined(__HIP_DEVICE_COMPILE__)
#pragma clang attribute push(__attribute__((target("no-packed-fp32-ops"))), apply_to = function)
#endif
__device__ __forceinline__ float u2f(unsigned u) { return __builtin_bit_cast(float, u); }
__device__ __forceinline__ unsigned f2u(float f) { return __builtin_bit_cast(unsigned, f); }
__device__ __forceinline__ float shx(float v, int lane, int m) {
  return u2f((unsigned)__builtin_amdgcn_ds_bpermute((lane ^ m) << 2, (int)f2u(v)));
}
__device__ __forceinline__ void wg_sync() {
  __builtin_amdgcn_fence(__ATOMIC_RELEASE, "workgroup");
  __builtin_amdgcn_s_barrier();
  __builtin_amdgcn_fence(__ATOMIC_ACQUIRE, "workgroup");
}
__device__ __forceinline__ float exp_acc(float x) {
  const float L2E_HI = 1.44269502162933349609375f, L2E_LO = 1.925963033500011e-8f;
  const float hi = x * L2E_HI;
  const float lo = __builtin_fmaf(x, L2E_LO, __builtin_fmaf(x, L2E_HI, -hi));
  const float e = __builtin_amdgcn_exp2f(hi);
  return __builtin_fmaf(e, lo * 0.6931471805599453f, e);
}
#define TIDX ((int)__builtin_amdgcn_workitem_id_x())
#define BIDX ((int)__builtin_amdgcn_workgroup_id_x())
__device__ __forceinline__ float bf2f(unsigned h) { return u2f(h << 16); }
__device__ __forceinline__ float bflo(unsigned u) { return u2f(u << 16); }
__device__ __forceinline__ float bfhi(unsigned u) { return u2f(u & 0xffff0000u); }
__device__ __forceinline__ f32x2 bfup(unsigned u) { return (f32x2){bflo(u), bfhi(u)}; }
__device__ __forceinline__ unsigned pkbf(float lo, float hi) {
  bf16x2_t r = {(__bf16)lo, (__bf16)hi};
  return __builtin_bit_cast(unsigned, r);
}
__device__ __forceinline__ unsigned pkbf2(f32x2 v) { return pkbf(v[0], v[1]); }
__device__ __forceinline__ float rbf(float x) { return bflo(pkbf(x, 0.f)); }
__device__ __forceinline__ unsigned pkh(float lo, float hi) {
  f16x2_t r = {(_Float16)lo, (_Float16)hi};
  return __builtin_bit_cast(unsigned, r);
}

#define STAMP_DECL
#define STAMP_AT(kid, i) do {} while (0)
#define STAMP_FLUSH(kid) do {} while (0)
#define SB() __builtin_amdgcn_sched_barrier(0)
#define WAITV(n) asm volatile("s_waitcnt vmcnt(%0)" ::"n"(n) : "memory")
#define WAITL() asm volatile("s_waitcnt lgkmcnt(0)" ::: "memory")

struct Params {
  const unsigned short* x; const unsigned short* Are; const unsigned short* Aim;
  const unsigned short* Bre; const unsigned short* Bim; const unsigned short* Cre; const unsigned short* Cim;
  const unsigned short* logdt; const unsigned short* Dsk; const unsigned short* w1; const unsigned short* w2;
  unsigned short* out; char* ws;
};

__device__ __forceinline__ unsigned lds_arrive(LAS unsigned* cnt, int lane) {
  WAITL();
  unsigned old = 0;
  if (lane == 0) old = __hip_atomic_fetch_add(cnt, 1u, __ATOMIC_RELAXED, __HIP_MEMORY_SCOPE_WORKGROUP);
  return __builtin_amdgcn_readfirstlane(old);
}
__device__ __forceinline__ void lds_wait(LAS unsigned* cnt, unsigned target) {
  while (__builtin_amdgcn_readfirstlane(__hip_atomic_load(cnt, __ATOMIC_RELAXED, __HIP_MEMORY_SCOPE_WORKGROUP)) < target)
    __builtin_amdgcn_s_sleep(1);
  asm volatile("" ::: "memory");
}
__device__ __forceinline__ void lds_wait2(LAS unsigned* c0, unsigned t0, LAS unsigned* c1, unsigned t1) {
  while (true) {
    const unsigned a = __hip_atomic_load(c0, __ATOMIC_RELAXED, __HIP_MEMORY_SCOPE_WORKGROUP);
    const unsigned b = __hip_atomic_load(c1, __ATOMIC_RELAXED, __HIP_MEMORY_SCOPE_WORKGROUP);
    if (__builtin_amdgcn_readfirstlane(a) >= t0 && __builtin_amdgcn_readfirstlane(b) >= t1) break;
    __builtin_amdgcn_s_sleep(1);
  }
  asm volatile("" ::: "memory");
}
__device__ __forceinline__ void lds_signal(LAS unsigned* cnt, int lane) {
  WAITL();
  if (lane == 0) __hip_atomic_fetch_add(cnt, 1u, __ATOMIC_RELAXED, __HIP_MEMORY_SCOPE_WORKGROUP);
}
__device__ __forceinline__ void glds16(const void* gsrc, unsigned lds_dst) {
  unsigned keep;
  asm volatile("s_mov_b32 %0, m0\n\ts_mov_b32 m0, %2\n\ts_nop 0\n\tglobal_load_lds_dwordx4 %1, off\n\ts_mov_b32 m0, %0"
               : "=&s"(keep) : "v"(gsrc), "s"(lds_dst) : "memory");
}

__device__ __forceinline__ void glds16s(const void* sbase, unsigned voff, unsigned lds_dst) {
  unsigned keep;
  asm volatile("s_mov_b32 %0, m0\n\ts_mov_b32 m0, %3\n\ts_nop 0\n\tglobal_load_lds_dwordx4 %1, %2\n\ts_mov_b32 m0, %0"
               : "=&s"(keep) : "v"(voff), "s"(sbase), "s"(lds_dst) : "memory");
}

template <int MODE> __device__ __forceinline__ void st16(void* ptr, u32x4 v) {
  if (MODE == 0) *(u32x4*)ptr = v;
  else if (MODE == 1) __builtin_nontemporal_store(v, (u32x4*)ptr);
  else if (MODE == 2) asm volatile("global_store_dwordx4 %0, %1, off sc1\n\ts_nop 1" :: "v"(ptr), "v"(v) : "memory");
  else asm volatile("global_store_dwordx4 %0, %1, off sc0 sc1\n\ts_nop 1" :: "v"(ptr), "v"(v) : "memory");
}
typedef __attribute__((ext_vector_type(2))) short s16x2;
template <int V> struct IC { static constexpr int v = V; };
typedef __attribute__((ext_vector_type(3))) unsigned u32x3;
typedef u32x3 __attribute__((aligned(4))) u32x3_a4;
constexpr int PB = 3;
constexpr int KT_P = 528;
constexpr int KT_CH = 54 * KT_P;
constexpr int L1_A = 0;
constexpr int L1_R = L1_A + 65536;
constexpr int L1_ST = L1_R + 2 * KT_CH;
constexpr int L1_CS = L1_ST + 1024;
constexpr int L1_C = L1_CS + 1536;
constexpr int L1_TH = L1_C + 64;
constexpr int L1_SIZE = L1_TH + 2688 + 16;

__device__ __forceinline__ void bsplit(f32x2 v, unsigned& hi, unsigned& lo) {
  hi = pkbf2(v);
  asm("" : "+v"(hi));
  lo = pkbf(v[0] - bflo(hi), v[1] - bfhi(hi));
}
template <int CTRL> __device__ __forceinline__ float dpp_max(float v) {
  const int t = __builtin_amdgcn_update_dpp(0, (int)f2u(v), CTRL, 0xf, 0xf, true);
  return __builtin_fmaxf(v, u2f((unsigned)t));
}
__device__ __forceinline__ float wave_max_nonneg(float v) {
  v = dpp_max<0x140>(dpp_max<0x141>(dpp_max<0x4E>(dpp_max<0xB1>(v))));
  const float a = u2f((unsigned)__builtin_amdgcn_readlane((int)f2u(v), 0));
  const float b = u2f((unsigned)__builtin_amdgcn_readlane((int)f2u(v), 16));
  const float c = u2f((unsigned)__builtin_amdgcn_readlane((int)f2u(v), 32));
  const float d = u2f((unsigned)__builtin_amdgcn_readlane((int)f2u(v), 48));
  return __builtin_fmaxf(__builtin_fmaxf(a, b), __builtin_fmaxf(c, d));
}

__global__ void __launch_bounds__(512, 2) k_phase1(Params p) {
  __shared__ __attribute__((aligned(16))) char lds[L1_SIZE];
  STAMP_DECL
  const int tid = TIDX, lane = tid & 63;
  const int w = __builtin_amdgcn_readfirstlane(tid >> 6);
  const int b = BIDX;
  LAS unsigned* cnt = (LAS unsigned*)(LAS char*)(lds + L1_C);
  if (tid < 16) cnt[tid] = 0u;
  wg_sync();
  STAMP_AT(0, 0);
  const int xcd = b & 7, idx = b >> 3;
  const int rt = (xcd >> 1) * 8 + (idx >> 2), cg = (xcd & 1) * 4 + (idx & 3);
  const int row0 = rt * 64;
  const int c = lane & 15, g = lane >> 4;
  const unsigned gl16 = (unsigned)((g ^ c) * 16);
  const char* arow = lds + L1_A + c * 1024;
  if (w >= 4) {
    const int gi = w - 4, ch = gi & 1, kh = gi >> 1;
    const int col0 = (cg == 7) ? 1280 : cg * 192 + 96 * ch;
    const bool gw = (cg < 7) || (ch == 0);
    const int rot = rt & 7;
    const unsigned lbase = (unsigned)(size_t)(LAS char*)lds;
    if (gw) {
      const auto wrs = __builtin_amdgcn_make_buffer_rsrc((void*)p.w1, 0, DM * DFF * 2, 0x00020000);
      const int bsrc = (256 * kh) * (DFF * 2) + col0 * 2;
      const unsigned bvo = (unsigned)(8 * g * (DFF * 2) + 12 * c);
      u32x3 bq[PB][8];
      auto issueB = [&](int s) {
        const int q = bsrc + ((s + rot) & 7) * (32 * DFF * 2);
#pragma unroll
        for (int j = 0; j < 8; ++j) bq[s % PB][j] = __builtin_amdgcn_raw_buffer_load_b96(wrs, bvo, q + j * (DFF * 2), 0);
      };
      lds_wait(cnt + 3, 4);
#pragma unroll
      for (int s = 0; s < PB; ++s) issueB(s);
      SB();
      STAMP_AT(0, 4);
      f32x4 acc[4][6];
      f32x4 acs[6];
      const s16x8 onesf = {0x3f80, 0x3f80, 0x3f80, 0x3f80, 0x3f80, 0x3f80, 0x3f80, 0x3f80};
#pragma unroll
      for (int t = 0; t < 6; ++t) acs[t] = (f32x4){0.f, 0.f, 0.f, 0.f};
#pragma unroll
      for (int i = 0; i < 4; ++i)
#pragma unroll
        for (int t = 0; t < 6; ++t) acc[i][t] = (f32x4){0.f, 0.f, 0.f, 0.f};
      lds_wait(cnt + 0, 4);
      STAMP_AT(0, 1);
      auto mkF = [&](int s, int t) -> s16x8 {
        u32x4 F;
#pragma unroll
        for (int q = 0; q < 4; ++q)
          F[q] = __builtin_amdgcn_perm(bq[s % PB][2 * q + 1][t >> 1], bq[s % PB][2 * q][t >> 1], (t & 1) ? 0x07060302u : 0x05040100u);
        return __builtin_bit_cast(s16x8, F);
      };
      s16x8 Fc = mkF(0, 0);
#pragma unroll
      for (int s = 0; s < 8; ++s) {
        s16x8 af[4];
        {
          const char* ap = arow + (gl16 ^ (unsigned)((8 * kh + ((s + rot) & 7)) * 64));
#pragma unroll
          for (int mt = 0; mt < 4; ++mt) af[mt] = *(const s16x8*)(ap + mt * 16384);
        }
        const int qa = bsrc + ((s + PB + rot) & 7) * (32 * DFF * 2);
        const int qb = bsrc + ((s + PB - 1 + rot) & 7) * (32 * DFF * 2);
#pragma unroll
        for (int t = 0; t < 6; ++t) {
          s16x8 Fn = Fc;
          if (t + 1 < 6) Fn = mkF(s, t + 1);
          else if (s + 1 < 8) Fn = mkF(s + 1, 0);
          if (t == 5 && s + PB < 8) {
#pragma unroll
            for (int j = 0; j < 4; ++j) bq[s % PB][j] = __builtin_amdgcn_raw_buffer_load_b96(wrs, bvo, qa + j * (DFF * 2), 0);
          }
          if (s >= 1 && s + PB - 1 < 8 && t < 2) {
#pragma unroll
            for (int j = 4 + 2 * t; j < 6 + 2 * t; ++j) bq[(s + PB - 1) % PB][j] = __builtin_amdgcn_raw_buffer_load_b96(wrs, bvo, qb + j * (DFF * 2), 0);
          }
#pragma unroll
          for (int mt = 0; mt < 4; ++mt)
            acc[mt][t] = __builtin_amdgcn_mfma_f32_16x16x32_bf16(Fc, af[mt], acc[mt][t], 0, 0, 0);
          acs[t] = __builtin_amdgcn_mfma_f32_16x16x32_bf16(Fc, onesf, acs[t], 0, 0, 0);
          __builtin_amdgcn_sched_group_barrier(0x008, 1, 0);
          __builtin_amdgcn_sched_group_barrier(0x002, 2, 0);
          __builtin_amdgcn_sched_group_barrier(0x008, 1, 0);
          __builtin_amdgcn_sched_group_barrier(0x002, 2, 0);
          __builtin_amdgcn_sched_group_barrier(0x008, 1, 0);
          __builtin_amdgcn_sched_group_barrier(0x020, 2, 0);
          __builtin_amdgcn_sched_group_barrier(0x008, 1, 0);
          __builtin_amdgcn_sched_group_barrier(0x020, 2, 0);
          __builtin_amdgcn_sched_group_barrier(0x008, 1, 0);
          SB();
          Fc = Fn;
        }
      }
      STAMP_AT(0, 2);
      int tz = TIDX;
      asm volatile("" : "+v"(tz));
      {
      const int lane = tz & 63, c = tz & 15, g = (tz >> 4) & 3;
      lds_wait2(cnt + 8, 2, cnt + 9, 2);
      f32x4* rx = (f32x4*)(lds + L1_R + gi * 12288);
      const f32x4* ro = (const f32x4*)(lds + L1_R + (gi ^ 2) * 12288);
      f32x4* csl = (f32x4*)(lds + L1_CS);
      auto fin = [&](auto KH) {
        constexpr int kk = decltype(KH)::v;
#pragma unroll
        for (int m = 0; m < 2; ++m)
#pragma unroll
          for (int t = 0; t < 6; ++t) rx[(m * 6 + t) * 64 + lane] = acc[2 * (1 - kk) + m][t];
        if (c == 0) {
#pragma unroll
          for (int t = 0; t < 6; ++t) csl[(gi * 6 + t) * 4 + g] = acs[t];
        }
        lds_signal(cnt + 5 + ch, lane);
        lds_wait2(cnt + 5 + ch, 2, cnt + 4, 4);
        f32x4 cso[6], oo[2][6], stv[2];
#pragma unroll
        for (int t = 0; t < 6; ++t) cso[t] = csl[((gi ^ 2) * 6 + t) * 4 + g];
#pragma unroll
        for (int m = 0; m < 2; ++m) {
          stv[m] = *(const f32x4*)(lds + L1_ST + (16 * (2 * kk + m) + c) * 16);
#pragma unroll
          for (int t = 0; t < 6; ++t) oo[m][t] = ro[(m * 6 + t) * 64 + lane];
        }
        SB();
#pragma unroll
        for (int t = 0; t < 6; ++t) acs[t] += cso[t];
#pragma unroll
        for (int m = 0; m < 2; ++m) {
          const int mt = 2 * kk + m;
          const f32x4 st = stv[m];
          float hv[24];
#pragma unroll
          for (int t = 0; t < 6; ++t) {
#pragma unroll
            for (int r = 0; r < 4; ++r)
              hv[6 * r + t] = __builtin_fmaf(-st[0], acs[t][r], acc[2 * kk + m][t][r] + oo[m][t][r]) * st[2];
          }
#pragma unroll
          for (int ck = 0; ck < 3; ++ck) {
            const int F0 = col0 + 24 * g + 8 * ck;
            u32x4 o;
#pragma unroll
            for (int e = 0; e < 4; ++e) {
              const s16x2 pv = __builtin_bit_cast(s16x2, pkbf(hv[8 * ck + 2 * e], hv[8 * ck + 2 * e + 1]));
              o[e] = __builtin_bit_cast(unsigned, __builtin_elementwise_max(pv, (s16x2){0, 0}));
            }
            st16<0>(p.ws + WS_H + ((size_t)(rt * 4 + mt) * 43 + (F0 >> 5)) * 1024 + (c + 16 * ((F0 >> 3) & 3)) * 16, o);
          }
        }
      };
      if (kh == 0) fin(IC<0>{}); else fin(IC<1>{});
      }
      STAMP_AT(0, 3);
    }
  } else {
    const int kw = w, chl = kw >> 1, nh = kw & 1;
    const int d = 2 * b + chl;
    char* tch = lds + L1_R + chl * KT_CH;
    unsigned short* th = (unsigned short*)(lds + L1_TH + chl * 1344);
    float a2, fr, wsc_inv;
    f32x2 W;
    bool grow;
    u32x4 xq[16];
    u32x2 w2r[8];
    const int blk = 4 * b + kw;
    const bool dow2 = blk < 344;
    {
      {
        const char* xr = (const char*)p.x + (size_t)(row0 + 16 * kw) * 1024 + lane * 16;
#pragma unroll
        for (int i = 0; i < 16; ++i) xq[i] = *(const u32x4*)(xr + i * 1024);
      }
      SB();
      const size_t o = (size_t)d * NS + 64 * nh + lane, o2 = o ^ 64;
      const float lg = bf2f(p.logdt[d]);
      const float are = bf2f(p.Are[o]), aim = bf2f(p.Aim[o]);
      const float bre = bf2f(p.Bre[o]), bim = bf2f(p.Bim[o]);
      const float cr = bf2f(p.Cre[o]), ci = bf2f(p.Cim[o]);
      const float are2 = bf2f(p.Are[o2]);
      const float bre2 = bf2f(p.Bre[o2]), bim2 = bf2f(p.Bim[o2]);
      const float cr2 = bf2f(p.Cre[o2]), ci2 = bf2f(p.Cim[o2]);
      if (dow2) {
        const int ct = blk / 43, ks = blk % 43;
        const unsigned short* wsrc = p.w2 + (size_t)(32 * ks + 8 * g) * DM + 64 * ct + 4 * c;
#pragma unroll
        for (int jj = 0; jj < 8; ++jj) w2r[jj] = *(const u32x2*)(wsrc + (size_t)jj * DM);
      }
      SB();
      if (lane == 0) __hip_atomic_fetch_add(cnt + 3, 1u, __ATOMIC_RELAXED, __HIP_MEMORY_SCOPE_WORKGROUP);
      SB();
      const float dt = rbf(exp_acc(lg));
      const float br = dt * bre, bi = dt * bim;
      a2 = (dt * are) * 1.4426950408889634f;
      fr = (dt * aim) * 0.15915494309189535f;
      W = (f32x2){cr * br - ci * bi, -(cr * bi + ci * br)};
      const float m1 = (__builtin_fabsf(cr) + __builtin_fabsf(ci)) * (__builtin_fabsf(bre) + __builtin_fabsf(bim));
      const float m2 = (__builtin_fabsf(cr2) + __builtin_fabsf(ci2)) * (__builtin_fabsf(bre2) + __builtin_fabsf(bim2));
      const float mw = wave_max_nonneg(__builtin_fmaxf(m1, m2)) * dt;
      const int eb = (int)(f2u(mw) >> 23);
      int ex = 136 - eb;
      ex = ex > 120 ? 120 : (ex < -120 ? -120 : ex);
      const float wsc = u2f((unsigned)(127 + ex) << 23);
      wsc_inv = u2f((unsigned)(127 - ex) << 23);
      W = (f32x2){W[0] * wsc, W[1] * wsc};
      const float gmax = __builtin_fmaxf(dt * are, dt * are2);
      grow = __builtin_amdgcn_ballot_w64(!(gmax * 2048.f < 2.f) || eb >= 250) != 0ull;
    }
    STAMP_AT(0, 1);
    auto cexpq = [&](float q) -> f32x2 {
      const float e = __builtin_amdgcn_exp2f(a2 * q);
      const float ph = __builtin_amdgcn_fractf(fr * q);
      return (f32x2){__builtin_amdgcn_cosf(ph) * e, __builtin_amdgcn_sinf(ph) * e};
    };
    auto cmul = [](f32x2 a, f32x2 bb) -> f32x2 {
      return (f32x2){__builtin_fmaf(a[0], bb[0], -(a[1] * bb[1])), __builtin_fmaf(a[0], bb[1], a[1] * bb[0])};
    };
    char* tw = tch + (64 * nh + lane) * 4;
    auto put = [&](int row, f32x2 v) {
      float v0 = v[0], v1 = v[1];
      asm("" : "+v"(v0), "+v"(v1));
      *(unsigned*)(tw + row * KT_P) = pkh(v0, v1);
    };
    auto xstore = [&]() {
#pragma unroll
      for (int i = 0; i < 16; ++i) *(u32x4*)(lds + L1_A + (16 * kw + i) * 1024 + ((lane ^ i) * 16)) = xq[i];
      lds_signal(cnt + 0, lane);
      if (dow2) {
        u32x4 f4[4];
#pragma unroll
        for (int q2 = 0; q2 < 4; ++q2) {
          f4[0][q2] = __builtin_amdgcn_perm(w2r[2 * q2 + 1][0], w2r[2 * q2][0], 0x05040100u);
          f4[1][q2] = __builtin_amdgcn_perm(w2r[2 * q2 + 1][0], w2r[2 * q2][0], 0x07060302u);
          f4[2][q2] = __builtin_amdgcn_perm(w2r[2 * q2 + 1][1], w2r[2 * q2][1], 0x05040100u);
          f4[3][q2] = __builtin_amdgcn_perm(w2r[2 * q2 + 1][1], w2r[2 * q2][1], 0x07060302u);
        }
        char* dst2 = p.ws + WS_W2F + (size_t)blk * 4096 + lane * 16;
#pragma unroll
        for (int t = 0; t < 4; ++t) st16<2>(dst2 + t * 1024, f4[t]);
      }
    };
    xstore();
    SB();
    if (!grow) {
      const f32x2 E1 = cexpq(1.f), E256 = cexpq(256.f), E8 = cexpq(8.f);
      f32x2 y0 = (f32x2){1.f, 0.f}, y1 = cexpq(64.f), y2 = cexpq(128.f), y3 = cexpq(192.f);
      const f32x2 C1 = {E1[0], -E1[1]}, CL = {E256[0], -E256[1]};
      const f32x2 C2 = cmul(C1, C1), C4 = cmul(C2, C2);
      f32x2 xs[21];
      xs[0] = W;
      xs[8] = cmul(W, CL);
      xs[1] = cmul(xs[0], C1);  xs[12] = cmul(xs[8], CL);  xs[9] = cmul(xs[8], C2);
      xs[2] = cmul(xs[1], C1);  xs[14] = cmul(xs[12], CL); xs[10] = cmul(xs[9], C2); xs[13] = cmul(xs[12], C4);
      xs[3] = cmul(xs[2], C1);  xs[16] = cmul(xs[14], CL); xs[11] = cmul(xs[10], C2); xs[15] = cmul(xs[14], C4);
      xs[4] = cmul(xs[0], C4);  xs[17] = cmul(xs[16], CL);
      xs[5] = cmul(xs[1], C4);  xs[18] = cmul(xs[17], CL);
      xs[6] = cmul(xs[2], C4);  xs[19] = cmul(xs[18], CL);
      xs[7] = cmul(xs[3], C4);  xs[20] = cmul(xs[19], CL);
#pragma unroll
      for (int i = 0; i < 21; ++i) put(i, xs[i]);
      put(21, y0); put(29, y1); put(37, y2); put(45, y3);
#pragma unroll
      for (int i = 1; i < 8; ++i) {
        y0 = cmul(y0, E8); y1 = cmul(y1, E8); y2 = cmul(y2, E8); y3 = cmul(y3, E8);
        put(21 + i, y0); put(29 + i, y1); put(37 + i, y2); put(45 + i, y3);
      }
    } else {
      *(f32x4*)(tch + (64 * nh + lane) * 16) = (f32x4){a2, fr, W[0], -W[1]};
    }
    lds_signal(cnt + 1 + chl, lane);
    STAMP_AT(0, 4);
    {
      lds_wait(cnt + 0, 4);
      const s16x8 onesf = {0x3f80, 0x3f80, 0x3f80, 0x3f80, 0x3f80, 0x3f80, 0x3f80, 0x3f80};
      f32x4 as0 = {0.f, 0.f, 0.f, 0.f}, aq0 = as0, as1 = as0, aq1 = as0;
      const char* ab2 = arow + kw * 16384;
      s16x8 xf[16];
#pragma unroll
      for (int s = 0; s < 16; ++s) xf[s] = *(const s16x8*)(ab2 + (gl16 ^ (unsigned)(s * 64)));
      SB();
#pragma unroll
      for (int s = 0; s < 16; s += 2) {
        as0 = __builtin_amdgcn_mfma_f32_16x16x32_bf16(onesf, xf[s], as0, 0, 0, 0);
        aq0 = __builtin_amdgcn_mfma_f32_16x16x32_bf16(xf[s], xf[s], aq0, 0, 0, 0);
        as1 = __builtin_amdgcn_mfma_f32_16x16x32_bf16(onesf, xf[s + 1], as1, 0, 0, 0);
        aq1 = __builtin_amdgcn_mfma_f32_16x16x32_bf16(xf[s + 1], xf[s + 1], aq1, 0, 0, 0);
      }
      const int r3 = c & 3;
      const f32x4 aq = aq0 + aq1;
      const float q = (r3 == 0) ? aq[0] : (r3 == 1) ? aq[1] : (r3 == 2) ? aq[2] : aq[3];
      const float mean = (as0[0] + as1[0]) * (1.f / DM);
      const float var = __builtin_fmaxf(q * (1.f / DM) - mean * mean, 0.f);
      const float mb = rbf(mean);
      const float dn = rbf(__builtin_sqrtf(rbf(rbf(var) + 1e-5f)));
      if (g == (c >> 2)) {
        const int r = 16 * kw + c;
        *(f32x4*)(lds + L1_ST + r * 16) = (f32x4){mb, dn, 1.f / dn, 0.f};
        if (cg == 0) *(f32x2*)(p.ws + WS_ST + (size_t)(row0 + r) * 8) = (f32x2){mb, dn};
      }
      lds_signal(cnt + 4, lane);
    }
    STAMP_AT(0, 8);
    lds_wait(cnt + 1 + chl, 2);
    f32x4 acc[2];
    acc[0] = (f32x4){0.f, 0.f, 0.f, 0.f}; acc[1] = acc[0];
    const int j = 16 * nh + c;
    if (!grow) {
      const char* fa = tch + c * KT_P + 16 * g;
      const char* fb = tch + (21 + j) * KT_P + 16 * g;
      f16x8 fA0[8], fA1[8], fB[8];
#pragma unroll
      for (int s = 0; s < 8; ++s) {
        fB[s] = *(const f16x8*)(fb + 64 * s);
        fA0[s] = *(const f16x8*)(fa + 64 * s);
        fA1[s] = *(const f16x8*)(fa + 16 * KT_P + 64 * s);
      }
      SB();
#pragma unroll
      for (int s = 0; s < 8; ++s) {
        acc[0] = __builtin_amdgcn_mfma_f32_16x16x32_f16(fA0[s], fB[s], acc[0], 0, 0, 0);
        acc[1] = __builtin_amdgcn_mfma_f32_16x16x32_f16(fA1[s], fB[s], acc[1], 0, 0, 0);
      }
    } else {
      for (int e = 0; e < 8; ++e) {
        const int row = 16 * (e >> 2) + 4 * g + (e & 3);
        const int qx = row < 8 ? row : row < 12 ? 256 + 2 * (row - 8) : row < 16 ? 512 + 256 * ((row - 12) >> 1) + 4 * (row & 1)
                                                                   : row < 21 ? 1024 + 256 * (row - 16) : 0;
        const float q = (float)(qx + 8 * j);
        float sacc = 0.f;
        for (int n = 0; n < 128; ++n) {
          const f32x4 pr = *(const f32x4*)(tch + n * 16);
          const float ph = __builtin_amdgcn_fractf(pr[1] * q);
          sacc += __builtin_amdgcn_exp2f(pr[0] * q) * (pr[2] * __builtin_amdgcn_cosf(ph) - pr[3] * __builtin_amdgcn_sinf(ph));
        }
        if (e < 4) acc[0][e & 3] = sacc; else acc[1][e & 3] = sacc;
      }
    }
    STAMP_AT(0, 5);
    {
      const bool v1 = (g == 0), v2 = (g == 1 && j == 0);
      float mx = __builtin_fmaxf(__builtin_fmaxf(__builtin_fabsf(acc[0][0]), __builtin_fabsf(acc[0][1])),
                                 __builtin_fmaxf(__builtin_fabsf(acc[0][2]), __builtin_fabsf(acc[0][3])));
      const float m1 = __builtin_fmaxf(__builtin_fmaxf(__builtin_fabsf(acc[1][1]), __builtin_fabsf(acc[1][2])), __builtin_fabsf(acc[1][3]));
      mx = __builtin_fmaxf(mx, (v1 || v2) ? __builtin_fabsf(acc[1][0]) : 0.f);
      mx = __builtin_fmaxf(mx, v1 ? m1 : 0.f);
      mx = wave_max_nonneg(mx);
      float* mxl = (float*)(lds + L1_TH + 2688) + kw;
      if (lane == 0) *mxl = mx;
      lds_signal(cnt + 8 + chl, lane);
    {
      lds_wait(cnt + 4, 4);
      const int q4 = c >> 2, pp = c & 3;
      f32x4 st[16];
#pragma unroll
      for (int r = 0; r < 16; ++r) st[r] = *(const f32x4*)(lds + L1_ST + (16 * g + r) * 16);
      const int ck = 8 * cg + 2 * kw + (pp >> 1);
      unsigned ov[8];
      u32x2 vv[4];
#pragma unroll
      for (int k = 0; k < 4; ++k) {
        const int r16 = 4 * k + q4;
        const char* a0 = lds + L1_A + (16 * g + r16) * 1024 + ((ck ^ r16) * 16) + 8 * (pp & 1);
        vv[k] = __builtin_bit_cast(u32x2, __builtin_amdgcn_ds_read_tr16_b64_v4i16((LAS s16x4*)a0));
      }
      SB();
#pragma unroll
      for (int k = 0; k < 4; ++k) {
        const u32x2 v = vv[k];
#pragma unroll
        for (int e = 0; e < 2; ++e) {
          const unsigned ve = v[e];
          const f32x4 s0 = st[4 * k + 2 * e], s1 = st[4 * k + 2 * e + 1];
          const float t0 = rbf(bflo(ve) - s0[0]), t1 = rbf(bfhi(ve) - s1[0]);
          float q0 = t0 * s0[2], q1 = t1 * s1[2];
          q0 = __builtin_fmaf(__builtin_fmaf(-q0, s0[1], t0), s0[2], q0);
          q1 = __builtin_fmaf(__builtin_fmaf(-q1, s1[1], t1), s1[2], q1);
          const unsigned xb = pkbf(q0, q1);
          ov[2 * k + e] = pkh(bflo(xb), bfhi(xb));
        }
      }
      unsigned short* dst = (unsigned short*)(p.ws + WS_XNT) + (size_t)(64 * cg + 16 * kw + c) * SL + row0 + 16 * g;
      st16<2>(dst, (u32x4){ov[0], ov[1], ov[2], ov[3]});
      st16<2>(dst + 8, (u32x4){ov[4], ov[5], ov[6], ov[7]});
    }
    {
      char* ydst = p.ws + WS_YT + (size_t)b * 8192 + kw * 2048 + lane * 16;
      const u32x4 sent = {0xffffffffu, 0xffffffffu, 0xffffffffu, 0xffffffffu};
      st16<2>(ydst, sent);
      st16<2>(ydst + 1024, sent);
    }
      lds_wait(cnt + 8 + chl, 2);
      mx = __builtin_fmaxf(mx, ((const float*)(lds + L1_TH + 2688))[kw ^ 1]);
      const int eb = (int)(f2u(mx) >> 23);
      int ex = 140 - eb;
      ex = (eb == 0 || eb == 255) ? 0 : (ex > 120 ? 120 : ex);
      const float sc = u2f((unsigned)(127 + ex) << 23);
      if (lane == 0 && nh == 0) ((float*)(p.ws + WS_KS))[d] = u2f((unsigned)(127 - ex) << 23) * wsc_inv;
      const int b0 = (g < 2 ? 4 * g + 8 * j : g == 2 ? 256 + 4 * j : 384 + 2 * j);
      *(unsigned*)(th + b0) = pkh(acc[0][0] * sc, acc[0][1] * sc);
      *(unsigned*)(th + b0 + (g == 3 ? 64 : 2)) = pkh(acc[0][2] * sc, acc[0][3] * sc);
      const unsigned p01 = pkh(acc[1][0] * sc, acc[1][1] * sc), p23 = pkh(acc[1][2] * sc, acc[1][3] * sc);
      if (v1) {
        th[512 + j] = (unsigned short)p01; th[544 + j] = (unsigned short)(p01 >> 16);
        th[576 + j] = (unsigned short)p23; th[608 + j] = (unsigned short)(p23 >> 16);
      }
      if (v2) th[640] = (unsigned short)p01;
      lds_signal(cnt + 10 + chl, lane);
    }
    STAMP_AT(0, 6);
    STAMP_AT(0, 7);
    {
      lds_wait(cnt + 10 + chl, 2);
      const unsigned short* tu = th;
      unsigned short* kr = (unsigned short*)(p.ws + WS_KR) + (size_t)d * KRL;
      if (nh == 0) {
#pragma unroll
        for (int k = 0; k < 2; ++k) {
          const int ck = lane + 64 * k, M = 256 - ck;
          const unsigned a = tu[384 + M], bb = tu[383 + M];
          const unsigned aa = a | (a << 16), b2 = bb | (bb << 16);
          const unsigned mid = ((M & 1) ? bb : a) | (bb << 16);
          st16<2>(kr + 8 * ck, (u32x4){aa, aa, mid, b2});
        }
        if (lane < 4) st16<2>(kr + 8 * (256 + lane), (u32x4){lane == 0 ? (unsigned)tu[0] : 0u, 0u, 0u, 0u});
      } else {
        {
          const int ck = 128 + lane, M = 256 - ck;
          const unsigned a = tu[256 + 2 * M], bb = tu[255 + 2 * M], cc = tu[254 + 2 * M];
          st16<2>(kr + 8 * ck, (u32x4){a | (a << 16), a | (bb << 16), bb | (bb << 16), cc | (cc << 16)});
        }
        {
          const int ck = 192 + lane, M = 256 - ck;
          const bool s1 = lane < 32;
          const int base = s1 ? 128 + 4 * M : 8 * M;
          const int o1[8] = {0, 0, 1, 2, 2, 2, 3, 4};
          unsigned hv[8];
#pragma unroll
          for (int e = 0; e < 8; ++e) hv[e] = tu[base - (s1 ? o1[e] : e)];
          st16<2>(kr + 8 * ck, (u32x4){hv[0] | (hv[1] << 16), hv[2] | (hv[3] << 16), hv[4] | (hv[5] << 16), hv[6] | (hv[7] << 16)});
        }
      }
    }
    STAMP_AT(0, 3);
    STAMP_AT(0, 9);
  }
  STAMP_FLUSH(0);
}

#define GAS __attribute__((address_space(1)))
constexpr int KCS = KRL * 2;
constexpr int CV_CH = 8 * KCS + 4608;
constexpr int L2_CR = 2 * CV_CH;
constexpr int L2_GR = L2_CR + 16384;
constexpr int L2_Y = L2_GR + 49152;
constexpr int L2_T1 = L2_Y + 64 * 136;
constexpr int L2_C = L2_T1 + 8192;
constexpr int L2_SIZE = L2_C + 64;

__global__ void __launch_bounds__(512, 2) k_phase2(Params p) {
  __shared__ __attribute__((aligned(16))) char lds[L2_SIZE];
  STAMP_DECL
  const int tid = TIDX, lane = tid & 63;
  const int w = __builtin_amdgcn_readfirstlane(tid >> 6);
  const int b = BIDX;
  LAS unsigned* cnt = (LAS unsigned*)(LAS char*)(lds + L2_C);
  if (tid < 16) cnt[tid] = 0u;
  wg_sync();
  STAMP_AT(1, 0);
  const int xcd = b & 7, idx = b >> 3;
  const int rt = xcd * 4 + (idx >> 3), ct = idx & 7;
  const int row0 = rt * 64, col0 = ct * 64;
  const int c = lane & 15, g = lane >> 4;
  GAS unsigned* flags = (GAS unsigned*)(p.ws + WS_FL);
  if (w < 4) {
    const int chl = w >> 1, hf = w & 1;
    const int d = 2 * b + chl;
    char* kl = lds + chl * CV_CH;
    char* ul = kl + 8 * KCS;
    const int erow = row0 + 16 * w + 4 * g;
    u32x2 xv[4];
    f32x4 st01, st23;
    u32x2 dk;
    float isc;
    {
      const int t2 = tid & 127;
      const u32x4* ksrc = (const u32x4*)(p.ws + WS_KR + (size_t)d * KCS);
      const u32x4* usrc = (const u32x4*)(p.ws + WS_XNT + (size_t)d * SL * 2);
      u32x4 kc[3], kp[3];
#pragma unroll
      for (int k = 0; k < 3; ++k) {
        int ci = t2 + 128 * k;
        ci = ci < 260 ? ci : 259;
        kc[k] = ksrc[ci];
        kp[k] = ksrc[ci > 0 ? ci - 1 : 0];
      }
      u32x4 uv0 = usrc[t2], uv1 = usrc[t2 + 128];
      SB();
#pragma unroll
      for (int r = 0; r < 4; ++r) xv[r] = *(const u32x2*)(p.x + (size_t)(erow + r) * DM + col0 + 4 * c);
      st01 = *(const f32x4*)(p.ws + WS_ST + (size_t)erow * 8);
      st23 = *(const f32x4*)(p.ws + WS_ST + (size_t)erow * 8 + 16);
      dk = *(const u32x2*)(p.Dsk + col0 + 4 * c);
      isc = ((const float*)(p.ws + WS_KS))[d];
      SB();
      const u32x4 z = {0u, 0u, 0u, 0u};
      if (t2 < 32) *(u32x4*)(ul + t2 * 16) = z;
#pragma unroll
      for (int k = 0; k < 3; ++k) {
        const int ci = t2 + 128 * k;
        if (ci < 260) {
          unsigned wd[8] = {kp[k][0], kp[k][1], kp[k][2], kp[k][3], kc[k][0], kc[k][1], kc[k][2], kc[k][3]};
          unsigned od[7];
#pragma unroll
          for (int j = 0; j < 7; ++j) od[j] = __builtin_amdgcn_alignbit(wd[j + 1], wd[j], 16);
          char* dst = kl + ci * 16;
#pragma unroll
          for (int sg = 0; sg < 8; ++sg) {
            u32x4 o;
            if ((sg & 1) == 0) { const int s0 = (8 - sg) >> 1; o = (u32x4){wd[s0], wd[s0 + 1], wd[s0 + 2], wd[s0 + 3]}; }
            else { const int s0 = (7 - sg) >> 1; o = (u32x4){od[s0], od[s0 + 1], od[s0 + 2], od[s0 + 3]}; }
            *(u32x4*)(dst + sg * KCS) = o;
          }
        }
      }
      *(u32x4*)(ul + 512 + t2 * 16) = uv0;
      *(u32x4*)(ul + 512 + (t2 + 128) * 16) = uv1;
    }
    lds_signal(cnt + chl, lane);
    lds_wait(cnt + chl, 2);
    STAMP_AT(1, 1);
    const int a = c;
    f32x4 acc[8];
#pragma unroll
    for (int r = 0; r < 8; ++r) acc[r] = (f32x4){0.f, 0.f, 0.f, 0.f};
    const char* ab = kl + (a & 7) * KCS + 2 * (2032 + 8 * g - 8 * (a >> 3)) - 64 * 4 * hf;
    const char* bb = ul + 2 * (240 + 16 * a + 8 * g) - 64 * 4 * hf;
    f16x8 bf[2][8], af[2][8];
#pragma unroll
    for (int t = 0; t < 8; ++t) { bf[0][t] = *(const f16x8*)(bb + 512 * t); af[0][t] = *(const f16x8*)(ab - 512 * t); }
#pragma unroll
    for (int i = 0; i < 4; ++i) {
      if (i + 1 < 4) {
#pragma unroll
        for (int t = 0; t < 8; ++t) {
          bf[(i + 1) & 1][t] = *(const f16x8*)(bb - 64 * (i + 1) + 512 * t);
          af[(i + 1) & 1][t] = *(const f16x8*)(ab - 64 * (i + 1) - 512 * t);
        }
      }
      SB();
#pragma unroll
      for (int v = 0; v < 8; ++v)
#pragma unroll
        for (int t = v; t < 8; ++t)
          acc[t] = __builtin_amdgcn_mfma_f32_16x16x32_f16(af[i & 1][v], bf[i & 1][t - v], acc[t], 0, 0, 0);
      SB();
    }
    STAMP_AT(1, 2);
    f32x4* red = (f32x4*)(lds + L2_CR + chl * 8192);
    f32x4 mine[4];
    if (hf) {
#pragma unroll
      for (int r = 0; r < 4; ++r) { red[(4 + r) * 64 + lane] = acc[r]; mine[r] = acc[4 + r]; }
    } else {
#pragma unroll
      for (int r = 0; r < 4; ++r) { red[r * 64 + lane] = acc[4 + r]; mine[r] = acc[r]; }
    }
    lds_signal(cnt + 2 + chl, lane);
    lds_wait(cnt + 2 + chl, 2);
    {
      unsigned pk[4][2];
#pragma unroll
      for (int r = 0; r < 4; ++r) {
        const f32x4 v = (mine[r] + red[(4 * (1 - hf) + r) * 64 + lane]) * isc;
        pk[r][0] = pkbf(v[0], v[1]);
        pk[r][1] = pkbf(v[2], v[3]);
      }
      const auto rsrc = __builtin_amdgcn_make_buffer_rsrc((void*)(p.ws + WS_YT), 0, DM * SL * 2, 0x00020000);
#pragma unroll
      for (int r = 0; r < 2; ++r) {
        auto s0 = __builtin_amdgcn_permlane16_swap(pk[r][0], pk[r + 2][0], false, false);
        auto s1 = __builtin_amdgcn_permlane16_swap(pk[r][1], pk[r + 2][1], false, false);
        const unsigned a0 = s0[0], b0 = s0[1], a1 = s1[0], b1 = s1[1];
        const u32x4 o = {a0, a1, b0, b1};
        const int tile = r + 2 * (g & 1);
        const int off = (d * SL + 1024 * hf + 256 * tile + 16 * a + 8 * (g >> 1)) * 2;
        __builtin_amdgcn_raw_buffer_store_b128(o, rsrc, off, 0, 16);
      }
    }
    STAMP_AT(1, 3);
    {
      const int ch = 16 * w + (lane >> 2), k0 = (lane & 3) * 2;
      const auto rsrc = __builtin_amdgcn_make_buffer_rsrc((void*)(p.ws + WS_YT), 0, DM * SL * 2, 0x00020000);
      const int off = ((col0 + ch) * SL + row0) * 2 + k0 * 16;
      float xf[4][4], xd[4][4];
      {
        const float dkf[4] = {bflo(dk[0]), bfhi(dk[0]), bflo(dk[1]), bfhi(dk[1])};
#pragma unroll
        for (int r = 0; r < 4; ++r) {
          xf[r][0] = bflo(xv[r][0]); xf[r][1] = bfhi(xv[r][0]); xf[r][2] = bflo(xv[r][1]); xf[r][3] = bfhi(xv[r][1]);
          const float mb = (r == 0) ? st01[0] : (r == 1) ? st01[2] : (r == 2) ? st23[0] : st23[2];
          const float den = (r == 0) ? st01[1] : (r == 1) ? st01[3] : (r == 2) ? st23[1] : st23[3];
          const float rc = 1.f / den;
#pragma unroll
          for (int t = 0; t < 4; ++t) {
            const float a0 = rbf(xf[r][t] - mb);
            float q = a0 * rc;
            q = __builtin_fmaf(__builtin_fmaf(-q, den, a0), rc, q);
            xd[r][t] = rbf(rbf(q) * dkf[t]);
            asm volatile("" : "+v"(xd[r][t]), "+v"(xf[r][t]));
          }
        }
      }
      u32x4 v0, v1;
      unsigned spins = 0;
      lds_wait(cnt + 7, 4);
      while (true) {
        v0 = __builtin_amdgcn_raw_buffer_load_b128(rsrc, off, 0, 16);
        v1 = __builtin_amdgcn_raw_buffer_load_b128(rsrc, off + 16, 0, 16);
        bool bad = false;
#pragma unroll
        for (int e = 0; e < 4; ++e) {
          const unsigned a0 = v0[e], a1 = v1[e];
          bad |= ((a0 & 0xffffu) == 0xffffu) | ((a0 >> 16) == 0xffffu) | ((a1 & 0xffffu) == 0xffffu) | ((a1 >> 16) == 0xffffu);
        }
        if (__builtin_amdgcn_ballot_w64(bad) == 0ull) break;
        if (++spins > (1u << 18)) break;
        __builtin_amdgcn_s_sleep(4);
      }
      STAMP_AT(1, 4);
      char* yd = lds + L2_Y + ch * 136 + k0 * 16;
      *(u32x2*)yd = (u32x2){v0[0], v0[1]}; *(u32x2*)(yd + 8) = (u32x2){v0[2], v0[3]};
      *(u32x2*)(yd + 16) = (u32x2){v1[0], v1[1]}; *(u32x2*)(yd + 24) = (u32x2){v1[2], v1[3]};
      lds_signal(cnt + 4, lane);
      lds_wait(cnt + 4, 4);
      u32x2 yv[4];
#pragma unroll
      for (int t = 0; t < 4; ++t) yv[t] = *(const u32x2*)(lds + L2_Y + (4 * c + t) * 136 + (16 * w + 4 * g) * 2);
#pragma unroll
      for (int r = 0; r < 4; ++r) {
        float o[4];
#pragma unroll
        for (int t = 0; t < 4; ++t) {
          const unsigned yw = yv[t][r >> 1];
          const float y = (r & 1) ? bfhi(yw) : bflo(yw);
          o[t] = xf[r][t] + rbf(y + xd[r][t]);
        }
        *(u32x2*)(lds + L2_T1 + ((w * 4 + r) * 64 + lane) * 8) = (u32x2){pkbf(o[0], o[1]), pkbf(o[2], o[3])};
      }
    }
    lds_signal(cnt + 8 + w, lane);
    STAMP_AT(1, 5);
  } else {
    const int gw = w - 4;
    const int s0 = 11 * gw;
    const int ns = (gw == 3) ? 10 : 11;
    const char* hb = p.ws + WS_H + ((size_t)(rt * 4) * 43 + s0) * 1024 + lane * 16;
    const char* wb = p.ws + WS_W2F + ((size_t)ct * 43 + s0) * 4096 + lane * 16;
    constexpr int P = 4;
    u32x4 ar[P][4];
    u32x4 br[P][4];
    const int rot = 0;

    auto issue = [&](int s) {
      int ks = s + rot;
      ks = ks >= ns ? ks - ns : ks;
#pragma unroll
      for (int mt = 0; mt < 4; ++mt) ar[s % P][mt] = *(const u32x4*)(hb + (size_t)mt * 43 * 1024 + ks * 1024);
#pragma unroll
      for (int j = 0; j < 4; ++j) br[s % P][j] = *(const u32x4*)(wb + ks * 4096 + j * 1024);
    };
#pragma unroll
    for (int s = 0; s < P; ++s) issue(s);
    SB();
    f32x4 acc[4][4];
#pragma unroll
    for (int i = 0; i < 4; ++i)
#pragma unroll
      for (int t = 0; t < 4; ++t) acc[i][t] = (f32x4){0.f, 0.f, 0.f, 0.f};
#pragma unroll
    for (int s = 0; s < 11; ++s) {
      if (s < ns) {
#pragma unroll
        for (int mt = 0; mt < 4; ++mt)
#pragma unroll
          for (int t = 0; t < 4; ++t)
            acc[mt][t] = __builtin_amdgcn_mfma_f32_16x16x32_bf16(__builtin_bit_cast(s16x8, ar[s % P][mt]),
                                                                 __builtin_bit_cast(s16x8, br[s % P][t]), acc[mt][t], 0, 0, 0);
        SB();
        if (s + P < 11 && s + P < ns) issue(s + P);
        if (s == 6 && lane == 0) __hip_atomic_fetch_add(cnt + 7, 1u, __ATOMIC_RELAXED, __HIP_MEMORY_SCOPE_WORKGROUP);
        SB();
      }
    }

    const int erow = row0 + 16 * gw + 4 * g;
    STAMP_AT(1, 1);
    f32x4* red = (f32x4*)(lds + L2_GR);
    f32x4 f[4];
#pragma unroll
    for (int mt = 0; mt < 4; ++mt) {
      if (mt != gw) {
        const int slot = (gw > mt) ? (gw - 1) : gw;
#pragma unroll
        for (int t = 0; t < 4; ++t) red[((mt * 3 + slot) * 4 + t) * 64 + lane] = acc[mt][t];
      } else {
#pragma unroll
        for (int t = 0; t < 4; ++t) f[t] = acc[mt][t];
      }
    }
    lds_signal(cnt + 6, lane);
    lds_wait(cnt + 6, 4);
    STAMP_AT(1, 2);
    f32x4 pr[4][3];
#pragma unroll
    for (int t = 0; t < 4; ++t)
#pragma unroll
      for (int sl = 0; sl < 3; ++sl) pr[t][sl] = red[((gw * 3 + sl) * 4 + t) * 64 + lane];
    SB();
#pragma unroll
    for (int t = 0; t < 4; ++t) {
      f[t] += (pr[t][0] + pr[t][1]) + pr[t][2];
#pragma unroll
      for (int r = 0; r < 4; ++r) f[t][r] = rbf(f[t][r]);
      asm volatile("" : "+v"(f[t]));
    }
    lds_wait(cnt + 8 + gw, 1);
    STAMP_AT(1, 3);
#pragma unroll
    for (int r = 0; r < 4; ++r) {
      const u32x2 tv = *(const u32x2*)(lds + L2_T1 + ((gw * 4 + r) * 64 + lane) * 8);
      const unsigned t0 = tv[0], t1 = tv[1];
      u32x2 ov;
      ov[0] = pkbf(bflo(t0) + f[0][r], bfhi(t0) + f[1][r]);
      ov[1] = pkbf(bflo(t1) + f[2][r], bfhi(t1) + f[3][r]);
      asm volatile("global_store_dwordx2 %0, %1, off sc1" :: "v"(p.out + (size_t)(erow + r) * DM + col0 + 4 * c), "v"(ov) : "memory");
    }
    STAMP_AT(1, 4);
  }
  STAMP_FLUSH(1);
}

#if defined(__HIP_DEVICE_COMPILE__)
#pragma clang attribute pop
#endif

extern "C" void kernel_launch(void* const* d_in, const int* in_sizes, int n_in,
                              void* d_out, int out_size, void* d_ws, size_t ws_size,
                              hipStream_t stream) {
  Params p;
  p.x = (const unsigned short*)d_in[0];
  p.Are = (const unsigned short*)d_in[1];
  p.Aim = (const unsigned short*)d_in[2];
  p.Bre = (const unsigned short*)d_in[3];
  p.Bim = (const unsigned short*)d_in[4];
  p.Cre = (const unsigned short*)d_in[5];
  p.Cim = (const unsigned short*)d_in[6];
  p.logdt = (const unsigned short*)d_in[7];
  p.Dsk = (const unsigned short*)d_in[8];
  p.w1 = (const unsigned short*)d_in[10];
  p.w2 = (const unsigned short*)d_in[11];
  p.out = (unsigned short*)d_out;
  p.ws = (char*)d_ws;
  k_phase1<<<256, 512, 0, stream>>>(p);
  k_phase2<<<256, 512, 0, stream>>>(p);
}
```

```cpp
#include <hip/hip_runtime.h>
#include <hip/hip_bf16.h>
#include <stdint.h>

typedef __attribute__((ext_vector_type(8))) short s16x8;
typedef __attribute__((ext_vector_type(4))) short s16x4;
typedef __attribute__((ext_vector_type(8))) _Float16 f16x8;
typedef __attribute__((ext_vector_type(4))) float f32x4;
typedef __attribute__((ext_vector_type(2))) float f32x2;
typedef __attribute__((ext_vector_type(16))) float f32x16;
typedef __attribute__((ext_vector_type(4))) unsigned u32x4;
typedef __attribute__((ext_vector_type(2))) unsigned u32x2;
typedef __attribute__((ext_vector_type(2))) __bf16 bf16x2_t;
typedef __attribute__((ext_vector_type(2))) _Float16 f16x2_t;
#define LAS __attribute__((address_space(3)))

constexpr int SL = 2048;
constexpr int DM = 512;
constexpr int NS = 128;
constexpr int DFF = 1376;
constexpr int KRL = 2080;

constexpr size_t WS_H = 0;
constexpr size_t WS_XNT = WS_H + (size_t)128 * 43 * 1024;
constexpr size_t WS_KR = WS_XNT + (size_t)DM * SL * 2;
constexpr size_t WS_YT = WS_KR + (size_t)DM * KRL * 2;
constexpr size_t WS_ST = WS_YT + (size_t)DM * SL * 2;
constexpr size_t WS_KS = WS_ST + (size_t)SL * 8;
constexpr size_t WS_W2F = WS_KS + 4096;

#if defined(__HIP_DEVICE_COMPILE__)
#pragma clang attribute push(__attribute__((target("no-packed-fp32-ops"))), apply_to = function)
#endif
__device__ __forceinline__ float u2f(unsigned u) { return __builtin_bit_cast(float, u); }
__device__ __forceinline__ unsigned f2u(float f) { return __builtin_bit_cast(unsigned, f); }
__device__ __forceinline__ void wg_sync() {
  __builtin_amdgcn_fence(__ATOMIC_RELEASE, "workgroup");
  __builtin_amdgcn_s_barrier();
  __builtin_amdgcn_fence(__ATOMIC_ACQUIRE, "workgroup");
}
__device__ __forceinline__ float exp_acc(float x) {
  const float L2E_HI = 1.44269502162933349609375f, L2E_LO = 1.925963033500011e-8f;
  const float hi = x * L2E_HI;
  const float lo = __builtin_fmaf(x, L2E_LO, __builtin_fmaf(x, L2E_HI, -hi));
  const float e = __builtin_amdgcn_exp2f(hi);
  return __builtin_fmaf(e, lo * 0.6931471805599453f, e);
}
#define TIDX ((int)__builtin_amdgcn_workitem_id_x())
#define BIDX ((int)__builtin_amdgcn_workgroup_id_x())
__device__ __forceinline__ float bf2f(unsigned h) { return u2f(h << 16); }
__device__ __forceinline__ float bflo(unsigned u) { return u2f(u << 16); }
__device__ __forceinline__ float bfhi(unsigned u) { return u2f(u & 0xffff0000u); }
__device__ __forceinline__ f32x2 bfup(unsigned u) { return (f32x2){bflo(u), bfhi(u)}; }
__device__ __forceinline__ unsigned pkbf(float lo, float hi) {
  bf16x2_t r = {(__bf16)lo, (__bf16)hi};
  return __builtin_bit_cast(unsigned, r);
}
__device__ __forceinline__ unsigned pkbf2(f32x2 v) { return pkbf(v[0], v[1]); }
__device__ __forceinline__ float rbf(float x) { return bflo(pkbf(x, 0.f)); }
__device__ __forceinline__ unsigned pkh(float lo, float hi) {
  f16x2_t r = {(_Float16)lo, (_Float16)hi};
  return __builtin_bit_cast(unsigned, r);
}

#define SB() __builtin_amdgcn_sched_barrier(0)
#define WAITV(n) asm volatile("s_waitcnt vmcnt(%0)" ::"n"(n) : "memory")
#define WAITL() asm volatile("s_waitcnt lgkmcnt(0)" ::: "memory")

struct Params {
  const unsigned short* x; const unsigned short* Are; const unsigned short* Aim;
  const unsigned short* Bre; const unsigned short* Bim; const unsigned short* Cre; const unsigned short* Cim;
  const unsigned short* logdt; const unsigned short* Dsk; const unsigned short* w1; const unsigned short* w2;
  unsigned short* out; char* ws;
};

__device__ __forceinline__ void lds_wait(LAS unsigned* cnt, unsigned target) {
  while (__builtin_amdgcn_readfirstlane(__hip_atomic_load(cnt, __ATOMIC_RELAXED, __HIP_MEMORY_SCOPE_WORKGROUP)) < target)
    __builtin_amdgcn_s_sleep(1);
  asm volatile("" ::: "memory");
}
__device__ __forceinline__ void lds_wait2(LAS unsigned* c0, unsigned t0, LAS unsigned* c1, unsigned t1) {
  while (true) {
    const unsigned a = __hip_atomic_load(c0, __ATOMIC_RELAXED, __HIP_MEMORY_SCOPE_WORKGROUP);
    const unsigned b = __hip_atomic_load(c1, __ATOMIC_RELAXED, __HIP_MEMORY_SCOPE_WORKGROUP);
    if (__builtin_amdgcn_readfirstlane(a) >= t0 && __builtin_amdgcn_readfirstlane(b) >= t1) break;
    __builtin_amdgcn_s_sleep(1);
  }
  asm volatile("" ::: "memory");
}
__device__ __forceinline__ void lds_signal(LAS unsigned* cnt, int lane) {
  WAITL();
  if (lane == 0) __hip_atomic_fetch_add(cnt, 1u, __ATOMIC_RELAXED, __HIP_MEMORY_SCOPE_WORKGROUP);
}

template <int MODE> __device__ __forceinline__ void st16(void* ptr, u32x4 v) {
  if (MODE == 0) *(u32x4*)ptr = v;
  else if (MODE == 1) __builtin_nontemporal_store(v, (u32x4*)ptr);
  else if (MODE == 2) asm volatile("global_store_dwordx4 %0, %1, off sc1\n\ts_nop 1" :: "v"(ptr), "v"(v) : "memory");
  else asm volatile("global_store_dwordx4 %0, %1, off sc0 sc1\n\ts_nop 1" :: "v"(ptr), "v"(v) : "memory");
}
typedef __attribute__((ext_vector_type(2))) short s16x2;
template <int V> struct IC { static constexpr int v = V; };
typedef __attribute__((ext_vector_type(3))) unsigned u32x3;
typedef u32x3 __attribute__((aligned(4))) u32x3_a4;
constexpr int PB = 3;
constexpr int KT_P = 528;
constexpr int KT_CH = 54 * KT_P;
constexpr int L1_A = 0;
constexpr int L1_R = L1_A + 65536;
constexpr int L1_ST = L1_R + 2 * KT_CH;
constexpr int L1_CS = L1_ST + 1024;
constexpr int L1_C = L1_CS + 1536;
constexpr int L1_TH = L1_C + 64;
constexpr int L1_SIZE = L1_TH + 2688 + 16;

template <int CTRL> __device__ __forceinline__ float dpp_max(float v) {
  const int t = __builtin_amdgcn_update_dpp(0, (int)f2u(v), CTRL, 0xf, 0xf, true);
  return __builtin_fmaxf(v, u2f((unsigned)t));
}
__device__ __forceinline__ float wave_max_nonneg(float v) {
  v = dpp_max<0x140>(dpp_max<0x141>(dpp_max<0x4E>(dpp_max<0xB1>(v))));
  const float a = u2f((unsigned)__builtin_amdgcn_readlane((int)f2u(v), 0));
  const float b = u2f((unsigned)__builtin_amdgcn_readlane((int)f2u(v), 16));
  const float c = u2f((unsigned)__builtin_amdgcn_readlane((int)f2u(v), 32));
  const float d = u2f((unsigned)__builtin_amdgcn_readlane((int)f2u(v), 48));
  return __builtin_fmaxf(__builtin_fmaxf(a, b), __builtin_fmaxf(c, d));
}

__global__ void __launch_bounds__(512, 2) k_phase1(Params p) {
  __shared__ __attribute__((aligned(16))) char lds[L1_SIZE];
  const int tid = TIDX, lane = tid & 63;
  const int w = __builtin_amdgcn_readfirstlane(tid >> 6);
  const int b = BIDX;
  LAS unsigned* cnt = (LAS unsigned*)(LAS char*)(lds + L1_C);
  if (tid < 16) cnt[tid] = 0u;
  wg_sync();
  const int xcd = b & 7, idx = b >> 3;
  const int rt = (xcd >> 1) * 8 + (idx >> 2), cg = (xcd & 1) * 4 + (idx & 3);
  const int row0 = rt * 64;
  const int c = lane & 15, g = lane >> 4;
  const unsigned gl16 = (unsigned)((g ^ c) * 16);
  const char* arow = lds + L1_A + c * 1024;
  if (w >= 4) {
    const int gi = w - 4, ch = gi & 1, kh = gi >> 1;
    const int col0 = (cg == 7) ? 1280 : cg * 192 + 96 * ch;
    const bool gw = (cg < 7) || (ch == 0);
    const int rot = rt & 7;
    if (gw) {
      const char* bsrc = (const char*)p.w1 + (size_t)(256 * kh) * (DFF * 2) + col0 * 2;
      const unsigned bvo = (unsigned)(8 * g * (DFF * 2) + 12 * c);
      u32x3 bq[PB][8];
      auto issueB = [&](int s) {
        const char* q = bsrc + (size_t)((s + rot) & 7) * (32 * DFF * 2);
#pragma unroll
        for (int j = 0; j < 8; ++j) bq[s % PB][j] = *(const u32x3_a4*)(q + j * (DFF * 2) + bvo);
      };
      lds_wait(cnt + 3, 4);
#pragma unroll
      for (int s = 0; s < PB; ++s) issueB(s);
      SB();
      f32x4 acc[4][6];
      f32x4 acs[6];
      const s16x8 onesf = {0x3f80, 0x3f80, 0x3f80, 0x3f80, 0x3f80, 0x3f80, 0x3f80, 0x3f80};
#pragma unroll
      for (int t = 0; t < 6; ++t) acs[t] = (f32x4){0.f, 0.f, 0.f, 0.f};
#pragma unroll
      for (int i = 0; i < 4; ++i)
#pragma unroll
        for (int t = 0; t < 6; ++t) acc[i][t] = (f32x4){0.f, 0.f, 0.f, 0.f};
      lds_wait(cnt + 0, 4);
      auto mkF = [&](int s, int t) -> s16x8 {
        u32x4 F;
#pragma unroll
        for (int q = 0; q < 4; ++q)
          F[q] = __builtin_amdgcn_perm(bq[s % PB][2 * q + 1][t >> 1], bq[s % PB][2 * q][t >> 1], (t & 1) ? 0x07060302u : 0x05040100u);
        return __builtin_bit_cast(s16x8, F);
      };
      s16x8 Fc = mkF(0, 0);
#pragma unroll
      for (int s = 0; s < 8; ++s) {
        s16x8 af[4];
        {
          const char* ap = arow + (gl16 ^ (unsigned)((8 * kh + ((s + rot) & 7)) * 64));
#pragma unroll
          for (int mt = 0; mt < 4; ++mt) af[mt] = *(const s16x8*)(ap + mt * 16384);
        }
        const char* q = bsrc + (size_t)((s + PB - 1 + rot) & 7) * (32 * DFF * 2);
#pragma unroll
        for (int t = 0; t < 6; ++t) {
          s16x8 Fn = Fc;
          if (t + 1 < 6) Fn = mkF(s, t + 1);
          else if (s + 1 < 8) Fn = mkF(s + 1, 0);
          if (s >= 1 && s + PB - 1 < 8 && t < 4) {
            bq[(s + PB - 1) % PB][2 * t] = *(const u32x3_a4*)(q + (2 * t) * (DFF * 2) + bvo);
            bq[(s + PB - 1) % PB][2 * t + 1] = *(const u32x3_a4*)(q + (2 * t + 1) * (DFF * 2) + bvo);
          }
          SB();
#pragma unroll
          for (int mt = 0; mt < 4; ++mt)
            acc[mt][t] = __builtin_amdgcn_mfma_f32_16x16x32_bf16(Fc, af[mt], acc[mt][t], 0, 0, 0);
          acs[t] = __builtin_amdgcn_mfma_f32_16x16x32_bf16(Fc, onesf, acs[t], 0, 0, 0);
          SB();
          Fc = Fn;
        }
      }
      int tz = TIDX;
      asm volatile("" : "+v"(tz));
      {
      const int lane = tz & 63, c = tz & 15, g = (tz >> 4) & 3;
      lds_wait2(cnt + 8, 2, cnt + 9, 2);
      f32x4* rx = (f32x4*)(lds + L1_R + gi * 12288);
      const f32x4* ro = (const f32x4*)(lds + L1_R + (gi ^ 2) * 12288);
      f32x4* csl = (f32x4*)(lds + L1_CS);
      auto fin = [&](auto KH) {
        constexpr int kk = decltype(KH)::v;
#pragma unroll
        for (int m = 0; m < 2; ++m)
#pragma unroll
          for (int t = 0; t < 6; ++t) rx[(m * 6 + t) * 64 + lane] = acc[2 * (1 - kk) + m][t];
        if (c == 0) {
#pragma unroll
          for (int t = 0; t < 6; ++t) csl[(gi * 6 + t) * 4 + g] = acs[t];
        }
        lds_signal(cnt + 5 + ch, lane);
        lds_wait2(cnt + 5 + ch, 2, cnt + 4, 4);
        f32x4 cso[6], oo[2][6], stv[2];
#pragma unroll
        for (int t = 0; t < 6; ++t) cso[t] = csl[((gi ^ 2) * 6 + t) * 4 + g];
#pragma unroll
        for (int m = 0; m < 2; ++m) {
          stv[m] = *(const f32x4*)(lds + L1_ST + (16 * (2 * kk + m) + c) * 16);
#pragma unroll
          for (int t = 0; t < 6; ++t) oo[m][t] = ro[(m * 6 + t) * 64 + lane];
        }
        SB();
#pragma unroll
        for (int t = 0; t < 6; ++t) acs[t] += cso[t];
#pragma unroll
        for (int m = 0; m < 2; ++m) {
          const int mt = 2 * kk + m;
          const f32x4 st = stv[m];
          float hv[24];
#pragma unroll
          for (int t = 0; t < 6; ++t) {
#pragma unroll
            for (int r = 0; r < 4; ++r)
              hv[6 * r + t] = __builtin_fmaf(-st[0], acs[t][r], acc[2 * kk + m][t][r] + oo[m][t][r]) * st[2];
          }
#pragma unroll
          for (int ck = 0; ck < 3; ++ck) {
            const int F0 = col0 + 24 * g + 8 * ck;
            u32x4 o;
#pragma unroll
            for (int e = 0; e < 4; ++e) {
              const s16x2 pv = __builtin_bit_cast(s16x2, pkbf(hv[8 * ck + 2 * e], hv[8 * ck + 2 * e + 1]));
              o[e] = __builtin_bit_cast(unsigned, __builtin_elementwise_max(pv, (s16x2){0, 0}));
            }
            st16<0>(p.ws + WS_H + ((size_t)(rt * 4 + mt) * 43 + (F0 >> 5)) * 1024 + (c + 16 * ((F0 >> 3) & 3)) * 16, o);
          }
        }
      };
      if (kh == 0) fin(IC<0>{}); else fin(IC<1>{});
      }
    }
  } else {
    const int kw = w, chl = kw >> 1, nh = kw & 1;
    const int d = 2 * b + chl;
    char* tch = lds + L1_R + chl * KT_CH;
    unsigned short* th = (unsigned short*)(lds + L1_TH + chl * 1344);
    float a2, fr, wsc_inv;
    f32x2 W;
    bool grow;
    u32x4 xq[16];
    u32x2 w2r[8];
    const int blk = 4 * b + kw;
    const bool dow2 = blk < 344;
    {
      {
        const char* xr = (const char*)p.x + (size_t)(row0 + 16 * kw) * 1024 + lane * 16;
#pragma unroll
        for (int i = 0; i < 16; ++i) xq[i] = *(const u32x4*)(xr + i * 1024);
      }
      SB();
      const size_t o = (size_t)d * NS + 64 * nh + lane, o2 = o ^ 64;
      const float lg = bf2f(p.logdt[d]);
      const float are = bf2f(p.Are[o]), aim = bf2f(p.Aim[o]);
      const float bre = bf2f(p.Bre[o]), bim = bf2f(p.Bim[o]);
      const float cr = bf2f(p.Cre[o]), ci = bf2f(p.Cim[o]);
      const float are2 = bf2f(p.Are[o2]);
      const float bre2 = bf2f(p.Bre[o2]), bim2 = bf2f(p.Bim[o2]);
      const float cr2 = bf2f(p.Cre[o2]), ci2 = bf2f(p.Cim[o2]);
      if (dow2) {
        const int ct = blk / 43, ks = blk % 43;
        const unsigned short* wsrc = p.w2 + (size_t)(32 * ks + 8 * g) * DM + 64 * ct + 4 * c;
#pragma unroll
        for (int jj = 0; jj < 8; ++jj) w2r[jj] = *(const u32x2*)(wsrc + (size_t)jj * DM);
      }
      SB();
      if (lane == 0) __hip_atomic_fetch_add(cnt + 3, 1u, __ATOMIC_RELAXED, __HIP_MEMORY_SCOPE_WORKGROUP);
      SB();
      const float dt = rbf(exp_acc(lg));
      const float br = dt * bre, bi = dt * bim;
      a2 = (dt * are) * 1.4426950408889634f;
      fr = (dt * aim) * 0.15915494309189535f;
      W = (f32x2){cr * br - ci * bi, -(cr * bi + ci * br)};
      const float m1 = (__builtin_fabsf(cr) + __builtin_fabsf(ci)) * (__builtin_fabsf(bre) + __builtin_fabsf(bim));
      const float m2 = (__builtin_fabsf(cr2) + __builtin_fabsf(ci2)) * (__builtin_fabsf(bre2) + __builtin_fabsf(bim2));
      const float mw = wave_max_nonneg(__builtin_fmaxf(m1, m2)) * dt;
      const int eb = (int)(f2u(mw) >> 23);
      int ex = 136 - eb;
      ex = ex > 120 ? 120 : (ex < -120 ? -120 : ex);
      const float wsc = u2f((unsigned)(127 + ex) << 23);
      wsc_inv = u2f((unsigned)(127 - ex) << 23);
      W = (f32x2){W[0] * wsc, W[1] * wsc};
      const float gmax = __builtin_fmaxf(dt * are, dt * are2);
      grow = __builtin_amdgcn_ballot_w64(!(gmax * 2048.f < 2.f) || eb >= 250) != 0ull;
    }
    auto cexpq = [&](float q) -> f32x2 {
      const float e = __builtin_amdgcn_exp2f(a2 * q);
      const float ph = __builtin_amdgcn_fractf(fr * q);
      return (f32x2){__builtin_amdgcn_cosf(ph) * e, __builtin_amdgcn_sinf(ph) * e};
    };
    auto cmul = [](f32x2 a, f32x2 bb) -> f32x2 {
      return (f32x2){__builtin_fmaf(a[0], bb[0], -(a[1] * bb[1])), __builtin_fmaf(a[0], bb[1], a[1] * bb[0])};
    };
    char* tw = tch + (64 * nh + lane) * 4;
    auto put = [&](int row, f32x2 v) {
      float v0 = v[0], v1 = v[1];
      asm("" : "+v"(v0), "+v"(v1));
      *(unsigned*)(tw + row * KT_P) = pkh(v0, v1);
    };
    auto xstore = [&]() {
#pragma unroll
      for (int i = 0; i < 16; ++i) *(u32x4*)(lds + L1_A + (16 * kw + i) * 1024 + ((lane ^ i) * 16)) = xq[i];
      lds_signal(cnt + 0, lane);
      if (dow2) {
        u32x4 f4[4];
#pragma unroll
        for (int q2 = 0; q2 < 4; ++q2) {
          f4[0][q2] = __builtin_amdgcn_perm(w2r[2 * q2 + 1][0], w2r[2 * q2][0], 0x05040100u);
          f4[1][q2] = __builtin_amdgcn_perm(w2r[2 * q2 + 1][0], w2r[2 * q2][0], 0x07060302u);
          f4[2][q2] = __builtin_amdgcn_perm(w2r[2 * q2 + 1][1], w2r[2 * q2][1], 0x05040100u);
          f4[3][q2] = __builtin_amdgcn_perm(w2r[2 * q2 + 1][1], w2r[2 * q2][1], 0x07060302u);
        }
        char* dst2 = p.ws + WS_W2F + (size_t)blk * 4096 + lane * 16;
#pragma unroll
        for (int t = 0; t < 4; ++t) st16<2>(dst2 + t * 1024, f4[t]);
      }
    };
    xstore();
    SB();
    if (!grow) {
      const f32x2 E1 = cexpq(1.f), E256 = cexpq(256.f), E8 = cexpq(8.f);
      f32x2 y0 = (f32x2){1.f, 0.f}, y1 = cexpq(64.f), y2 = cexpq(128.f), y3 = cexpq(192.f);
      const f32x2 C1 = {E1[0], -E1[1]}, CL = {E256[0], -E256[1]};
      const f32x2 C2 = cmul(C1, C1), C4 = cmul(C2, C2);
      f32x2 xs[21];
      xs[0] = W;
      xs[8] = cmul(W, CL);
      xs[1] = cmul(xs[0], C1);  xs[12] = cmul(xs[8], CL);  xs[9] = cmul(xs[8], C2);
      xs[2] = cmul(xs[1], C1);  xs[14] = cmul(xs[12], CL); xs[10] = cmul(xs[9], C2); xs[13] = cmul(xs[12], C4);
      xs[3] = cmul(xs[2], C1);  xs[16] = cmul(xs[14], CL); xs[11] = cmul(xs[10], C2); xs[15] = cmul(xs[14], C4);
      xs[4] = cmul(xs[0], C4);  xs[17] = cmul(xs[16], CL);
      xs[5] = cmul(xs[1], C4);  xs[18] = cmul(xs[17], CL);
      xs[6] = cmul(xs[2], C4);  xs[19] = cmul(xs[18], CL);
      xs[7] = cmul(xs[3], C4);  xs[20] = cmul(xs[19], CL);
#pragma unroll
      for (int i = 0; i < 21; ++i) put(i, xs[i]);
      put(21, y0); put(29, y1); put(37, y2); put(45, y3);
#pragma unroll
      for (int i = 1; i < 8; ++i) {
        y0 = cmul(y0, E8); y1 = cmul(y1, E8); y2 = cmul(y2, E8); y3 = cmul(y3, E8);
        put(21 + i, y0); put(29 + i, y1); put(37 + i, y2); put(45 + i, y3);
      }
    } else {
      *(f32x4*)(tch + (64 * nh + lane) * 16) = (f32x4){a2, fr, W[0], -W[1]};
    }
    lds_signal(cnt + 1 + chl, lane);
    {
      lds_wait(cnt + 0, 4);
      const s16x8 onesf = {0x3f80, 0x3f80, 0x3f80, 0x3f80, 0x3f80, 0x3f80, 0x3f80, 0x3f80};
      f32x4 as0 = {0.f, 0.f, 0.f, 0.f}, aq0 = as0, as1 = as0, aq1 = as0;
      const char* ab2 = arow + kw * 16384;
      s16x8 xf[16];
#pragma unroll
      for (int s = 0; s < 16; ++s) xf[s] = *(const s16x8*)(ab2 + (gl16 ^ (unsigned)(s * 64)));
      SB();
#pragma unroll
      for (int s = 0; s < 16; s += 2) {
        as0 = __builtin_amdgcn_mfma_f32_16x16x32_bf16(onesf, xf[s], as0, 0, 0, 0);
        aq0 = __builtin_amdgcn_mfma_f32_16x16x32_bf16(xf[s], xf[s], aq0, 0, 0, 0);
        as1 = __builtin_amdgcn_mfma_f32_16x16x32_bf16(onesf, xf[s + 1], as1, 0, 0, 0);
        aq1 = __builtin_amdgcn_mfma_f32_16x16x32_bf16(xf[s + 1], xf[s + 1], aq1, 0, 0, 0);
      }
      const int r3 = c & 3;
      const f32x4 aq = aq0 + aq1;
      const float q = (r3 == 0) ? aq[0] : (r3 == 1) ? aq[1] : (r3 == 2) ? aq[2] : aq[3];
      const float mean = (as0[0] + as1[0]) * (1.f / DM);
      const float var = __builtin_fmaxf(q * (1.f / DM) - mean * mean, 0.f);
      const float mb = rbf(mean);
      const float dn = rbf(__builtin_sqrtf(rbf(rbf(var) + 1e-5f)));
      if (g == (c >> 2)) {
        const int r = 16 * kw + c;
        *(f32x4*)(lds + L1_ST + r * 16) = (f32x4){mb, dn, 1.f / dn, 0.f};
        if (cg == 0) *(f32x2*)(p.ws + WS_ST + (size_t)(row0 + r) * 8) = (f32x2){mb, dn};
      }
      lds_signal(cnt + 4, lane);
    }
    lds_wait(cnt + 1 + chl, 2);
    f32x4 acc[2];
    acc[0] = (f32x4){0.f, 0.f, 0.f, 0.f}; acc[1] = acc[0];
    const int j = 16 * nh + c;
    if (!grow) {
      const char* fa = tch + c * KT_P + 16 * g;
      const char* fb = tch + (21 + j) * KT_P + 16 * g;
      f16x8 fA0[8], fA1[8], fB[8];
#pragma unroll
      for (int s = 0; s < 8; ++s) {
        fB[s] = *(const f16x8*)(fb + 64 * s);
        fA0[s] = *(const f16x8*)(fa + 64 * s);
        fA1[s] = *(const f16x8*)(fa + 16 * KT_P + 64 * s);
      }
      SB();
#pragma unroll
      for (int s = 0; s < 8; ++s) {
        acc[0] = __builtin_amdgcn_mfma_f32_16x16x32_f16(fA0[s], fB[s], acc[0], 0, 0, 0);
        acc[1] = __builtin_amdgcn_mfma_f32_16x16x32_f16(fA1[s], fB[s], acc[1], 0, 0, 0);
      }
    } else {
      for (int e = 0; e < 8; ++e) {
        const int row = 16 * (e >> 2) + 4 * g + (e & 3);
        const int qx = row < 8 ? row : row < 12 ? 256 + 2 * (row - 8) : row < 16 ? 512 + 256 * ((row - 12) >> 1) + 4 * (row & 1)
                                                                   : row < 21 ? 1024 + 256 * (row - 16) : 0;
        const float q = (float)(qx + 8 * j);
        float sacc = 0.f;
        for (int n = 0; n < 128; ++n) {
          const f32x4 pr = *(const f32x4*)(tch + n * 16);
          const float ph = __builtin_amdgcn_fractf(pr[1] * q);
          sacc += __builtin_amdgcn_exp2f(pr[0] * q) * (pr[2] * __builtin_amdgcn_cosf(ph) - pr[3] * __builtin_amdgcn_sinf(ph));
        }
        if (e < 4) acc[0][e & 3] = sacc; else acc[1][e & 3] = sacc;
      }
    }
    {
      const bool v1 = (g == 0), v2 = (g == 1 && j == 0);
      float mx = __builtin_fmaxf(__builtin_fmaxf(__builtin_fabsf(acc[0][0]), __builtin_fabsf(acc[0][1])),
                                 __builtin_fmaxf(__builtin_fabsf(acc[0][2]), __builtin_fabsf(acc[0][3])));
      const float m1 = __builtin_fmaxf(__builtin_fmaxf(__builtin_fabsf(acc[1][1]), __builtin_fabsf(acc[1][2])), __builtin_fabsf(acc[1][3]));
      mx = __builtin_fmaxf(mx, (v1 || v2) ? __builtin_fabsf(acc[1][0]) : 0.f);
      mx = __builtin_fmaxf(mx, v1 ? m1 : 0.f);
      mx = wave_max_nonneg(mx);
      float* mxl = (float*)(lds + L1_TH + 2688) + kw;
      if (lane == 0) *mxl = mx;
      lds_signal(cnt + 8 + chl, lane);
    {
      lds_wait(cnt + 4, 4);
      const int q4 = c >> 2, pp = c & 3;
      f32x4 st[16];
#pragma unroll
      for (int r = 0; r < 16; ++r) st[r] = *(const f32x4*)(lds + L1_ST + (16 * g + r) * 16);
      const int ck = 8 * cg + 2 * kw + (pp >> 1);
      unsigned ov[8];
      u32x2 vv[4];
#pragma unroll
      for (int k = 0; k < 4; ++k) {
        const int r16 = 4 * k + q4;
        const char* a0 = lds + L1_A + (16 * g + r16) * 1024 + ((ck ^ r16) * 16) + 8 * (pp & 1);
        vv[k] = __builtin_bit_cast(u32x2, __builtin_amdgcn_ds_read_tr16_b64_v4i16((LAS s16x4*)a0));
      }
      SB();
#pragma unroll
      for (int k = 0; k < 4; ++k) {
        const u32x2 v = vv[k];
#pragma unroll
        for (int e = 0; e < 2; ++e) {
          const unsigned ve = v[e];
          const f32x4 s0 = st[4 * k + 2 * e], s1 = st[4 * k + 2 * e + 1];
          const float t0 = rbf(bflo(ve) - s0[0]), t1 = rbf(bfhi(ve) - s1[0]);
          float q0 = t0 * s0[2], q1 = t1 * s1[2];
          q0 = __builtin_fmaf(__builtin_fmaf(-q0, s0[1], t0), s0[2], q0);
          q1 = __builtin_fmaf(__builtin_fmaf(-q1, s1[1], t1), s1[2], q1);
          const unsigned xb = pkbf(q0, q1);
          ov[2 * k + e] = pkh(bflo(xb), bfhi(xb));
        }
      }
      unsigned short* dst = (unsigned short*)(p.ws + WS_XNT) + (size_t)(64 * cg + 16 * kw + c) * SL + row0 + 16 * g;
      st16<2>(dst, (u32x4){ov[0], ov[1], ov[2], ov[3]});
      st16<2>(dst + 8, (u32x4){ov[4], ov[5], ov[6], ov[7]});
    }
    {
      char* ydst = p.ws + WS_YT + (size_t)b * 8192 + kw * 2048 + lane * 16;
      const u32x4 sent = {0xffffffffu, 0xffffffffu, 0xffffffffu, 0xffffffffu};
      st16<2>(ydst, sent);
      st16<2>(ydst + 1024, sent);
    }
      lds_wait(cnt + 8 + chl, 2);
      mx = __builtin_fmaxf(mx, ((const float*)(lds + L1_TH + 2688))[kw ^ 1]);
      const int eb = (int)(f2u(mx) >> 23);
      int ex = 140 - eb;
      ex = (eb == 0 || eb == 255) ? 0 : (ex > 120 ? 120 : ex);
      const float sc = u2f((unsigned)(127 + ex) << 23);
      if (lane == 0 && nh == 0) ((float*)(p.ws + WS_KS))[d] = u2f((unsigned)(127 - ex) << 23) * wsc_inv;
      const int b0 = (g < 2 ? 4 * g + 8 * j : g == 2 ? 256 + 4 * j : 384 + 2 * j);
      *(unsigned*)(th + b0) = pkh(acc[0][0] * sc, acc[0][1] * sc);
      *(unsigned*)(th + b0 + (g == 3 ? 64 : 2)) = pkh(acc[0][2] * sc, acc[0][3] * sc);
      const unsigned p01 = pkh(acc[1][0] * sc, acc[1][1] * sc), p23 = pkh(acc[1][2] * sc, acc[1][3] * sc);
      if (v1) {
        th[512 + j] = (unsigned short)p01; th[544 + j] = (unsigned short)(p01 >> 16);
        th[576 + j] = (unsigned short)p23; th[608 + j] = (unsigned short)(p23 >> 16);
      }
      if (v2) th[640] = (unsigned short)p01;
      lds_signal(cnt + 10 + chl, lane);
    }
    {
      lds_wait(cnt + 10 + chl, 2);
      const unsigned short* tu = th;
      unsigned short* kr = (unsigned short*)(p.ws + WS_KR) + (size_t)d * KRL;
      if (nh == 0) {
#pragma unroll
        for (int k = 0; k < 2; ++k) {
          const int ck = lane + 64 * k, M = 256 - ck;
          const unsigned a = tu[384 + M], bb = tu[383 + M];
          const unsigned aa = a | (a << 16), b2 = bb | (bb << 16);
          const unsigned mid = ((M & 1) ? bb : a) | (bb << 16);
          st16<2>(kr + 8 * ck, (u32x4){aa, aa, mid, b2});
        }
        if (lane < 4) st16<2>(kr + 8 * (256 + lane), (u32x4){lane == 0 ? (unsigned)tu[0] : 0u, 0u, 0u, 0u});
      } else {
        {
          const int ck = 128 + lane, M = 256 - ck;
          const unsigned a = tu[256 + 2 * M], bb = tu[255 + 2 * M], cc = tu[254 + 2 * M];
          st16<2>(kr + 8 * ck, (u32x4){a | (a << 16), a | (bb << 16), bb | (bb << 16), cc | (cc << 16)});
        }
        {
          const int ck = 192 + lane, M = 256 - ck;
          const bool s1 = lane < 32;
          const int base = s1 ? 128 + 4 * M : 8 * M;
          const int o1[8] = {0, 0, 1, 2, 2, 2, 3, 4};
          unsigned hv[8];
#pragma unroll
          for (int e = 0; e < 8; ++e) hv[e] = tu[base - (s1 ? o1[e] : e)];
          st16<2>(kr + 8 * ck, (u32x4){hv[0] | (hv[1] << 16), hv[2] | (hv[3] << 16), hv[4] | (hv[5] << 16), hv[6] | (hv[7] << 16)});
        }
      }
    }
  }
}

#define GAS __attribute__((address_space(1)))
constexpr int KCS = KRL * 2;
constexpr int CV_CH = 8 * KCS + 4608;
constexpr int L2_CR = 2 * CV_CH;
constexpr int L2_GR = L2_CR + 16384;
constexpr int L2_Y = L2_GR + 49152;
constexpr int L2_T1 = L2_Y + 64 * 136;
constexpr int L2_C = L2_T1 + 8192;
constexpr int L2_SIZE = L2_C + 64;

__global__ void __launch_bounds__(512, 2) k_phase2(Params p) {
  __shared__ __attribute__((aligned(16))) char lds[L2_SIZE];
  const int tid = TIDX, lane = tid & 63;
  const int w = __builtin_amdgcn_readfirstlane(tid >> 6);
  const int b = BIDX;
  LAS unsigned* cnt = (LAS unsigned*)(LAS char*)(lds + L2_C);
  if (tid < 16) cnt[tid] = 0u;
  wg_sync();
  const int xcd = b & 7, idx = b >> 3;
  const int rt = xcd * 4 + (idx >> 3), ct = idx & 7;
  const int row0 = rt * 64, col0 = ct * 64;
  const int c = lane & 15, g = lane >> 4;
  if (w < 4) {
    const int chl = w >> 1, hf = w & 1;
    const int d = 2 * b + chl;
    char* kl = lds + chl * CV_CH;
    char* ul = kl + 8 * KCS;
    const int erow = row0 + 16 * w + 4 * g;
    u32x2 xv[4];
    f32x4 st01, st23;
    u32x2 dk;
    float isc;
    {
      const int t2 = tid & 127;
      const u32x4* ksrc = (const u32x4*)(p.ws + WS_KR + (size_t)d * KCS);
      const u32x4* usrc = (const u32x4*)(p.ws + WS_XNT + (size_t)d * SL * 2);
      u32x4 kc[3], kp[3];
#pragma unroll
      for (int k = 0; k < 3; ++k) {
        int ci = t2 + 128 * k;
        ci = ci < 260 ? ci : 259;
        kc[k] = ksrc[ci];
        kp[k] = ksrc[ci > 0 ? ci - 1 : 0];
      }
      u32x4 uv0 = usrc[t2], uv1 = usrc[t2 + 128];
      SB();
#pragma unroll
      for (int r = 0; r < 4; ++r) xv[r] = *(const u32x2*)(p.x + (size_t)(erow + r) * DM + col0 + 4 * c);
      st01 = *(const f32x4*)(p.ws + WS_ST + (size_t)erow * 8);
      st23 = *(const f32x4*)(p.ws + WS_ST + (size_t)erow * 8 + 16);
      dk = *(const u32x2*)(p.Dsk + col0 + 4 * c);
      isc = ((const float*)(p.ws + WS_KS))[d];
      SB();
      const u32x4 z = {0u, 0u, 0u, 0u};
      if (t2 < 32) *(u32x4*)(ul + t2 * 16) = z;
#pragma unroll
      for (int k = 0; k < 3; ++k) {
        const int ci = t2 + 128 * k;
        if (ci < 260) {
          unsigned wd[8] = {kp[k][0], kp[k][1], kp[k][2], kp[k][3], kc[k][0], kc[k][1], kc[k][2], kc[k][3]};
          unsigned od[7];
#pragma unroll
          for (int j = 0; j < 7; ++j) od[j] = __builtin_amdgcn_alignbit(wd[j + 1], wd[j], 16);
          char* dst = kl + ci * 16;
#pragma unroll
          for (int sg = 0; sg < 8; ++sg) {
            u32x4 o;
            if ((sg & 1) == 0) { const int s0 = (8 - sg) >> 1; o = (u32x4){wd[s0], wd[s0 + 1], wd[s0 + 2], wd[s0 + 3]}; }
            else { const int s0 = (7 - sg) >> 1; o = (u32x4){od[s0], od[s0 + 1], od[s0 + 2], od[s0 + 3]}; }
            *(u32x4*)(dst + sg * KCS) = o;
          }
        }
      }
      *(u32x4*)(ul + 512 + t2 * 16) = uv0;
      *(u32x4*)(ul + 512 + (t2 + 128) * 16) = uv1;
    }
    lds_signal(cnt + chl, lane);
    lds_wait(cnt + chl, 2);
    const int a = c;
    f32x4 acc[8];
#pragma unroll
    for (int r = 0; r < 8; ++r) acc[r] = (f32x4){0.f, 0.f, 0.f, 0.f};
    const char* ab = kl + (a & 7) * KCS + 2 * (2032 + 8 * g - 8 * (a >> 3)) - 64 * 4 * hf;
    const char* bb = ul + 2 * (240 + 16 * a + 8 * g) - 64 * 4 * hf;
    f16x8 bf[2][8], af[2][8];
#pragma unroll
    for (int t = 0; t < 8; ++t) { bf[0][t] = *(const f16x8*)(bb + 512 * t); af[0][t] = *(const f16x8*)(ab - 512 * t); }
#pragma unroll
    for (int i = 0; i < 4; ++i) {
      if (i + 1 < 4) {
#pragma unroll
        for (int t = 0; t < 8; ++t) {
          bf[(i + 1) & 1][t] = *(const f16x8*)(bb - 64 * (i + 1) + 512 * t);
          af[(i + 1) & 1][t] = *(const f16x8*)(ab - 64 * (i + 1) - 512 * t);
        }
      }
      SB();
#pragma unroll
      for (int v = 0; v < 8; ++v)
#pragma unroll
        for (int t = v; t < 8; ++t)
          acc[t] = __builtin_amdgcn_mfma_f32_16x16x32_f16(af[i & 1][v], bf[i & 1][t - v], acc[t], 0, 0, 0);
      SB();
    }
    f32x4* red = (f32x4*)(lds + L2_CR + chl * 8192);
    f32x4 mine[4];
    if (hf) {
#pragma unroll
      for (int r = 0; r < 4; ++r) { red[(4 + r) * 64 + lane] = acc[r]; mine[r] = acc[4 + r]; }
    } else {
#pragma unroll
      for (int r = 0; r < 4; ++r) { red[r * 64 + lane] = acc[4 + r]; mine[r] = acc[r]; }
    }
    lds_signal(cnt + 2 + chl, lane);
    lds_wait(cnt + 2 + chl, 2);
    {
      unsigned pk[4][2];
#pragma unroll
      for (int r = 0; r < 4; ++r) {
        const f32x4 v = (mine[r] + red[(4 * (1 - hf) + r) * 64 + lane]) * isc;
        pk[r][0] = pkbf(v[0], v[1]);
        pk[r][1] = pkbf(v[2], v[3]);
      }
      const auto rsrc = __builtin_amdgcn_make_buffer_rsrc((void*)(p.ws + WS_YT), 0, DM * SL * 2, 0x00020000);
#pragma unroll
      for (int r = 0; r < 2; ++r) {
        auto s0 = __builtin_amdgcn_permlane16_swap(pk[r][0], pk[r + 2][0], false, false);
        auto s1 = __builtin_amdgcn_permlane16_swap(pk[r][1], pk[r + 2][1], false, false);
        const unsigned a0 = s0[0], b0 = s0[1], a1 = s1[0], b1 = s1[1];
        const u32x4 o = {a0, a1, b0, b1};
        const int tile = r + 2 * (g & 1);
        const int off = (d * SL + 1024 * hf + 256 * tile + 16 * a + 8 * (g >> 1)) * 2;
        __builtin_amdgcn_raw_buffer_store_b128(o, rsrc, off, 0, 16);
      }
    }
    {
      const int ch = 16 * w + (lane >> 2), k0 = (lane & 3) * 2;
      const auto rsrc = __builtin_amdgcn_make_buffer_rsrc((void*)(p.ws + WS_YT), 0, DM * SL * 2, 0x00020000);
      const int off = ((col0 + ch) * SL + row0) * 2 + k0 * 16;
      float xf[4][4], xd[4][4];
      {
        const float dkf[4] = {bflo(dk[0]), bfhi(dk[0]), bflo(dk[1]), bfhi(dk[1])};
#pragma unroll
        for (int r = 0; r < 4; ++r) {
          xf[r][0] = bflo(xv[r][0]); xf[r][1] = bfhi(xv[r][0]); xf[r][2] = bflo(xv[r][1]); xf[r][3] = bfhi(xv[r][1]);
          const float mb = (r == 0) ? st01[0] : (r == 1) ? st01[2] : (r == 2) ? st23[0] : st23[2];
          const float den = (r == 0) ? st01[1] : (r == 1) ? st01[3] : (r == 2) ? st23[1] : st23[3];
          const float rc = 1.f / den;
#pragma unroll
          for (int t = 0; t < 4; ++t) {
            const float a0 = rbf(xf[r][t] - mb);
            float q = a0 * rc;
            q = __builtin_fmaf(__builtin_fmaf(-q, den, a0), rc, q);
            xd[r][t] = rbf(rbf(q) * dkf[t]);
            asm volatile("" : "+v"(xd[r][t]), "+v"(xf[r][t]));
          }
        }
      }
      u32x4 v0, v1;
      unsigned spins = 0;
      lds_wait(cnt + 7, 4);
      while (true) {
        v0 = __builtin_amdgcn_raw_buffer_load_b128(rsrc, off, 0, 16);
        v1 = __builtin_amdgcn_raw_buffer_load_b128(rsrc, off + 16, 0, 16);
        bool bad = false;
#pragma unroll
        for (int e = 0; e < 4; ++e) {
          const unsigned a0 = v0[e], a1 = v1[e];
          bad |= ((a0 & 0xffffu) == 0xffffu) | ((a0 >> 16) == 0xffffu) | ((a1 & 0xffffu) == 0xffffu) | ((a1 >> 16) == 0xffffu);
        }
        if (__builtin_amdgcn_ballot_w64(bad) == 0ull) break;
        if (++spins > (1u << 18)) break;
        __builtin_amdgcn_s_sleep(4);
      }
      char* yd = lds + L2_Y + ch * 136 + k0 * 16;
      *(u32x2*)yd = (u32x2){v0[0], v0[1]}; *(u32x2*)(yd + 8) = (u32x2){v0[2], v0[3]};
      *(u32x2*)(yd + 16) = (u32x2){v1[0], v1[1]}; *(u32x2*)(yd + 24) = (u32x2){v1[2], v1[3]};
      lds_signal(cnt + 4, lane);
      lds_wait(cnt + 4, 4);
      u32x2 yv[4];
#pragma unroll
      for (int t = 0; t < 4; ++t) yv[t] = *(const u32x2*)(lds + L2_Y + (4 * c + t) * 136 + (16 * w + 4 * g) * 2);
#pragma unroll
      for (int r = 0; r < 4; ++r) {
        float o[4];
#pragma unroll
        for (int t = 0; t < 4; ++t) {
          const unsigned yw = yv[t][r >> 1];
          const float y = (r & 1) ? bfhi(yw) : bflo(yw);
          o[t] = xf[r][t] + rbf(y + xd[r][t]);
        }
        *(u32x2*)(lds + L2_T1 + ((w * 4 + r) * 64 + lane) * 8) = (u32x2){pkbf(o[0], o[1]), pkbf(o[2], o[3])};
      }
    }
    lds_signal(cnt + 8 + w, lane);
  } else {
    const int gw = w - 4;
    const int s0 = 11 * gw;
    const int ns = (gw == 3) ? 10 : 11;
    const char* hb = p.ws + WS_H + ((size_t)(rt * 4) * 43 + s0) * 1024 + lane * 16;
    const char* wb = p.ws + WS_W2F + ((size_t)ct * 43 + s0) * 4096 + lane * 16;
    constexpr int P = 4;
    u32x4 ar[P][4];
    u32x4 br[P][4];
    const int rot = 0;

    auto issue = [&](int s) {
      int ks = s + rot;
      ks = ks >= ns ? ks - ns : ks;
#pragma unroll
      for (int mt = 0; mt < 4; ++mt) ar[s % P][mt] = *(const u32x4*)(hb + (size_t)mt * 43 * 1024 + ks * 1024);
#pragma unroll
      for (int j = 0; j < 4; ++j) br[s % P][j] = *(const u32x4*)(wb + ks * 4096 + j * 1024);
    };
#pragma unroll
    for (int s = 0; s < P; ++s) issue(s);
    SB();
    f32x4 acc[4][4];
#pragma unroll
    for (int i = 0; i < 4; ++i)
#pragma unroll
      for (int t = 0; t < 4; ++t) acc[i][t] = (f32x4){0.f, 0.f, 0.f, 0.f};
#pragma unroll
    for (int s = 0; s < 11; ++s) {
      if (s < ns) {
#pragma unroll
        for (int mt = 0; mt < 4; ++mt)
#pragma unroll
          for (int t = 0; t < 4; ++t)
            acc[mt][t] = __builtin_amdgcn_mfma_f32_16x16x32_bf16(__builtin_bit_cast(s16x8, ar[s % P][mt]),
                                                                 __builtin_bit_cast(s16x8, br[s % P][t]), acc[mt][t], 0, 0, 0);
        SB();
        if (s + P < 11 && s + P < ns) issue(s + P);
        if (s == 6 && lane == 0) __hip_atomic_fetch_add(cnt + 7, 1u, __ATOMIC_RELAXED, __HIP_MEMORY_SCOPE_WORKGROUP);
        SB();
      }
    }

    const int erow = row0 + 16 * gw + 4 * g;
    f32x4* red = (f32x4*)(lds + L2_GR);
    f32x4 f[4];
#pragma unroll
    for (int mt = 0; mt < 4; ++mt) {
      if (mt != gw) {
        const int slot = (gw > mt) ? (gw - 1) : gw;
#pragma unroll
        for (int t = 0; t < 4; ++t) red[((mt * 3 + slot) * 4 + t) * 64 + lane] = acc[mt][t];
      } else {
#pragma unroll
        for (int t = 0; t < 4; ++t) f[t] = acc[mt][t];
      }
    }
    lds_signal(cnt + 6, lane);
    lds_wait(cnt + 6, 4);
    f32x4 pr[4][3];
#pragma unroll
    for (int t = 0; t < 4; ++t)
#pragma unroll
      for (int sl = 0; sl < 3; ++sl) pr[t][sl] = red[((gw * 3 + sl) * 4 + t) * 64 + lane];
    SB();
#pragma unroll
    for (int t = 0; t < 4; ++t) {
      f[t] += (pr[t][0] + pr[t][1]) + pr[t][2];
#pragma unroll
      for (int r = 0; r < 4; ++r) f[t][r] = rbf(f[t][r]);
      asm volatile("" : "+v"(f[t]));
    }
    lds_wait(cnt + 8 + gw, 1);
#pragma unroll
    for (int r = 0; r < 4; ++r) {
      const u32x2 tv = *(const u32x2*)(lds + L2_T1 + ((gw * 4 + r) * 64 + lane) * 8);
      const unsigned t0 = tv[0], t1 = tv[1];
      u32x2 ov;
      ov[0] = pkbf(bflo(t0) + f[0][r], bfhi(t0) + f[1][r]);
      ov[1] = pkbf(bflo(t1) + f[2][r], bfhi(t1) + f[3][r]);
      asm volatile("global_store_dwordx2 %0, %1, off sc1" :: "v"(p.out + (size_t)(erow + r) * DM + col0 + 4 * c), "v"(ov) : "memory");
    }
  }
}

#if defined(__HIP_DEVICE_COMPILE__)
#pragma clang attribute pop
#endif

extern "C" void kernel_launch(void* const* d_in, const int* in_sizes, int n_in,
                              void* d_out, int out_size, void* d_ws, size_t ws_size,
                              hipStream_t stream) {
  Params p;
  p.x = (const unsigned short*)d_in[0];
  p.Are = (const unsigned short*)d_in[1];
  p.Aim = (const unsigned short*)d_in[2];
  p.Bre = (const unsigned short*)d_in[3];
  p.Bim = (const unsigned short*)d_in[4];
  p.Cre = (const unsigned short*)d_in[5];
  p.Cim = (const unsigned short*)d_in[6];
  p.logdt = (const unsigned short*)d_in[7];
  p.Dsk = (const unsigned short*)d_in[8];
  p.w1 = (const unsigned short*)d_in[10];
  p.w2 = (const unsigned short*)d_in[11];
  p.out = (unsigned short*)d_out;
  p.ws = (char*)d_ws;
  k_phase1<<<256, 512, 0, stream>>>(p);
  k_phase2<<<256, 512, 0, stream>>>(p);
}
```
